```python
import torch

def _softmax(x, axis=-1):
    return torch.softmax(x, dim=axis)
def _gelu(x):
    return torch.nn.functional.gelu(x, approximate="tanh")
def _silu(x):
    return torch.nn.functional.silu(x)
def _sigmoid(x):
    return torch.sigmoid(x)
def _layernorm(x, axis=-1, eps=1e-5):
    m = x.mean(dim=axis, keepdim=True)
    v = x.var(dim=axis, keepdim=True, unbiased=False)
    return (x - m) / torch.sqrt(v + eps)
def _scatter_add(tgt, idx, src, axis=0):
    return tgt.clone().index_add_(axis, idx, src)
def _segment_sum(src, seg, n):
    out = torch.zeros((n,) + src.shape[1:], dtype=src.dtype)
    return out.index_add_(0, seg, src)
def _pad(x, pw):
    flat = tuple(v for pair in reversed(pw) for v in pair)
    return torch.nn.functional.pad(x, flat)

kernel_name = 'standalone__mha_block_cached__near_ridge'

def setup_inputs():

    _w = (lambda a: (lambda t: t.to(torch.bfloat16) if t.is_floating_point() else t)(torch.as_tensor(a)))
    B, Sq, D = 8, 192, 640
    H, Sk, Dh = 32, 512, 128
    return {
        "x": _w((lambda *s: torch.randn(*s, dtype=torch.bfloat16) * 0.8)(B, Sq, D)),
        "k_cache": _w((lambda *s: torch.randn(*s, dtype=torch.bfloat16) * 0.8)(B, H, Sk, Dh)),
        "v_cache": _w((lambda *s: torch.randn(*s, dtype=torch.bfloat16) * 0.05)(B, H, Sk, Dh)),
        "wq": _w((lambda *s: torch.randn(*s, dtype=torch.bfloat16) * 0.8)(D, H * Dh) * (D ** -0.5)),
        "wo": _w((lambda *s: torch.randn(*s, dtype=torch.bfloat16) * 0.05)(H * Dh, D) * ((H * Dh) ** -0.5)),
    }

def reference(x, k_cache, v_cache, wq, wo):
    B, Sq, D = x.shape
    H, Dh = 32, 128
    q = (lambda x, *d: x.permute(*d))((x @ wq).reshape(B, Sq, H, Dh), 0, 2, 1, 3)
    scores = torch.einsum("bhqd,bhkd->bhqk", q, k_cache) * (Dh ** -0.5)
    p = _softmax(scores, axis=-1)
    o = torch.einsum("bhqk,bhkd->bhqd", p, v_cache)
    return (lambda x, *d: x.permute(*d))(o, 0, 2, 1, 3).reshape(B, Sq, H * Dh) @ wo
```

```cpp
#include <hip/hip_runtime.h>
#include <hip/hip_bf16.h>
typedef unsigned short u16;
__device__ __forceinline__ float bf2f(u16 v){ return __uint_as_float(((unsigned)v)<<16); }
__device__ __forceinline__ u16 f2bf(float f){ unsigned u=__float_as_uint(f); u += 0x7FFF + ((u>>16)&1); return (u16)(u>>16); }
constexpr int B=8,SQ=192,D=640,H=32,SK=512,DH=128;
__global__ void qproj(const u16* x,const u16* wq,float* q){
  int n = blockIdx.x*256+threadIdx.x;
  int m = blockIdx.y;
  float acc=0;
  for(int k=0;k<D;k++) acc += bf2f(x[m*D+k])*bf2f(wq[k*4096+n]);
  q[(size_t)m*4096+n]=acc;
}
__global__ void attn(const float* q,const u16* kc,const u16* vc,float* o){
  __shared__ float qs[128]; __shared__ float p[512]; __shared__ float red[512];
  int qi=blockIdx.x, h=blockIdx.y, b=blockIdx.z; int t=threadIdx.x;
  if(t<128) qs[t]=q[((size_t)(b*SQ+qi))*4096+h*128+t];
  __syncthreads();
  const u16* kr = kc + (((size_t)(b*H+h))*SK+t)*DH;
  float s=0; for(int d=0;d<128;d++) s+=qs[d]*bf2f(kr[d]);
  s*=0.08838834764831845f;
  red[t]=s; __syncthreads();
  for(int off=256;off>0;off>>=1){ if(t<off) red[t]=fmaxf(red[t],red[t+off]); __syncthreads(); }
  float m=red[0]; __syncthreads();
  float e=__expf(s-m); p[t]=e; red[t]=e; __syncthreads();
  for(int off=256;off>0;off>>=1){ if(t<off) red[t]+=red[t+off]; __syncthreads(); }
  float l=red[0];
  if(t<128){ float acc=0; const u16* vb=vc+(((size_t)(b*H+h))*SK)*DH+t;
    for(int k=0;k<SK;k++) acc+=p[k]*bf2f(vb[(size_t)k*DH]);
    o[((size_t)(b*SQ+qi))*4096+h*128+t]=acc/l; }
}
__global__ void oproj(const float* o,const u16* wo,u16* out){
  int n = blockIdx.x*128+threadIdx.x;
  int m = blockIdx.y;
  float acc=0;
  for(int k=0;k<4096;k++) acc += o[(size_t)m*4096+k]*bf2f(wo[k*640+n]);
  out[m*640+n]=f2bf(acc);
}
extern "C" void kernel_launch(void* const* d_in, const int* in_sizes, int n_in, void* d_out, int out_size, void* d_ws, size_t ws_size, hipStream_t stream){
  const u16* x=(const u16*)d_in[0]; const u16* kc=(const u16*)d_in[1]; const u16* vc=(const u16*)d_in[2]; const u16* wq=(const u16*)d_in[3]; const u16* wo=(const u16*)d_in[4];
  float* q=(float*)d_ws; float* o=q+(size_t)1536*4096;
  qproj<<<dim3(16,1536),256,0,stream>>>(x,wq,q);
  attn<<<dim3(192,32,8),512,0,stream>>>(q,kc,vc,o);
  oproj<<<dim3(5,1536),128,0,stream>>>(o,wo,(u16*)d_out);
}
```

```cpp
#include <hip/hip_runtime.h>
#include <hip/hip_bf16.h>
#include <stdint.h>

__global__ __launch_bounds__(256, 1) void mha_attn_kernel(const void* xp, const void* kp, const void* vp, const void* wp, const void* wo, void* op, void* out) {
  __shared__ __attribute__((aligned(16))) char lds[163840];
  const unsigned ldsb = (unsigned)(uintptr_t)lds;
  asm volatile(
    "v_and_b32 v4, 63, %[tid]\n"
    "v_lshrrev_b32 v27, 6, %[tid]\n"
    "s_and_b32 s47, %[bid], 7\n"
    "s_lshr_b32 s52, %[bid], 3\n"
    "v_readfirstlane_b32 s36, v27\n"
    "s_and_b32 s39, s47, 1\n"
    "s_lshl_b32 s39, s39, 2\n"
    "s_and_b32 s53, s52, 3\n"
    "s_add_u32 s39, s39, s53\n"
    "s_mov_b32 s68, s53\n"
    "s_lshr_b32 s40, s47, 1\n"
    "s_lshl_b32 s40, s40, 3\n"
    "s_lshr_b32 s52, s52, 2\n"
    "s_add_u32 s40, s40, s52\n"
    "s_mul_i32 s59, s53, 3\n"
    "s_add_u32 s59, s59, s52\n"
    "s_sub_u32 s47, s59, 10\n"
    "s_cmp_ge_u32 s59, 10\n"
    "s_cselect_b32 s59, s47, s59\n"
    "s_and_b32 s37, s36, 1\n"
    "s_lshr_b32 s38, s36, 1\n"
    "s_lshl_b32 s48, s36, 12\n"
    "v_bfe_u32 v28, v4, 2, 2\n"
    "v_and_b32 v29, 3, v4\n"
    "v_lshrrev_b32 v30, 4, v4\n"
    "s_mul_i32 s47, s39, 245760\n"
    "s_add_u32 s28, %[xp_lo], s47\n"
    "s_addc_u32 s29, %[xp_hi], 0\n"
    "s_and_b32 s29, s29, 0xffff\n"
    "s_mov_b32 s30, 245760\n"
    "s_mov_b32 s31, 0x20000\n"
    "s_mul_i32 s47, s37, 122880\n"
    "s_mul_i32 s52, s38, 20480\n"
    "s_add_u32 s47, s47, s52\n"
    "s_add_u32 s46, s47, 1280\n"
    "s_lshl_b32 s42, s59, 7\n"
    "s_add_u32 s42, s42, s47\n"
    "s_lshl_b32 s45, s37, 12\n"
    "s_lshl_b32 s52, s38, 11\n"
    "s_add_u32 s45, s45, s52\n"
    "s_add_u32 s45, s45, 114688\n"
    "v_lshrrev_b32 v252, 5, v4\n"
    "v_lshl_add_u32 v253, v252, 2, v28\n"
    "v_mul_u32_u24 v253, 1280, v253\n"
    "v_bfe_u32 v254, v4, 4, 1\n"
    "v_lshl_add_u32 v253, v254, 6, v253\n"
    "v_xor_b32 v254, v29, v252\n"
    "v_lshl_add_u32 v24, v254, 4, v253\n"
    "v_xor_b32 v255, 32, v24\n"
    "v_add_u32 v25, 9216, v255\n"
    "s_add_u32 m0, s45, 0\n"
    "s_add_u32 s43, s42, 0\n"
    "buffer_load_dwordx4 v24, s[28:31], s43 offen lds\n"
    "buffer_load_dwordx4 v25, s[28:31], s43 offen offset:1024 lds\n"
    "s_mov_b32 s64, %[wp_lo]\n"
    "s_and_b32 s65, %[wp_hi], 0xffff\n"
    "s_mov_b32 s66, 5242880\n"
    "s_mov_b32 s67, 0x20000\n"
    "s_lshl_b32 s47, s40, 8\n"
    "s_lshl_b32 s52, s36, 17\n"
    "s_add_u32 s47, s47, s52\n"
    "s_add_u32 s58, s47, 5242880\n"
    "s_lshl_b32 s44, s59, 19\n"
    "s_add_u32 s44, s44, s47\n"
    "v_lshlrev_b32 v13, 13, v28\n"
    "v_lshl_add_u32 v13, v30, 6, v13\n"
    "v_lshl_add_u32 v13, v29, 4, v13\n"
    "v_add_u32 v14, 31744, v13\n"
    "v_add_u32 v15, 63488, v13\n"
    "v_add_u32 v16, 95232, v13\n"
    "s_add_u32 m0, s48, 81920\n"
    "s_nop 0\n"
    "buffer_load_dwordx4 v13, s[64:67], s44 offen lds\n"
    "buffer_load_dwordx4 v14, s[64:67], s44 offen offset:1024 lds\n"
    "buffer_load_dwordx4 v15, s[64:67], s44 offen offset:2048 lds\n"
    "buffer_load_dwordx4 v16, s[64:67], s44 offen offset:3072 lds\n"
    "s_add_u32 s44, s44, 524288\n"
    "s_sub_u32 s47, s44, 5242880\n"
    "s_cmp_ge_u32 s44, s58\n"
    "s_cselect_b32 s44, s47, s44\n"
    "s_getpc_b64 s[54:55]\n"
    ".L_pcbase:\n"
    "s_lshr_b32 s47, %[bid], 3\n"
    "s_lshl_b32 s47, s47, 10\n"
    "v_and_b32 v252, 7, %[tid]\n"
    "v_lshlrev_b32 v252, 7, v252\n"
    "v_add_u32 v252, s47, v252\n"
    "v_min_u32 v253, .L_codeend-.L_pcbase-4, v252\n"
    "global_load_dword v208, v253, s[54:55]\n"
    "s_add_u32 m0, s45, 8192\n"
    "s_add_u32 s43, s42, 40960\n"
    "buffer_load_dwordx4 v24, s[28:31], s43 offen lds\n"
    "buffer_load_dwordx4 v25, s[28:31], s43 offen offset:1024 lds\n"
    "s_lshl_b32 s47, s39, 5\n"
    "s_add_u32 s47, s47, s40\n"
    "s_lshl_b32 s47, s47, 17\n"
    "s_add_u32 s20, %[kp_lo], s47\n"
    "s_addc_u32 s21, %[kp_hi], 0\n"
    "s_and_b32 s21, s21, 0xffff\n"
    "s_mov_b32 s22, 131072\n"
    "s_mov_b32 s23, 0x20000\n"
    "s_add_u32 s24, %[vp_lo], s47\n"
    "s_addc_u32 s25, %[vp_hi], 0\n"
    "s_and_b32 s25, s25, 0xffff\n"
    "s_mov_b32 s26, 131072\n"
    "s_mov_b32 s27, 0x20000\n"
    "v_lshlrev_b32 v9, 8, v28\n"
    "v_lshl_add_u32 v9, v30, 6, v9\n"
    "v_lshl_add_u32 v9, v29, 4, v9\n"
    "v_xor_b32 v10, 16, v9\n"
    "v_xor_b32 v11, 32, v9\n"
    "v_xor_b32 v12, 48, v9\n"
    "v_and_b32 v27, 31, v4\n"
    "v_lshrrev_b32 v28, 5, v4\n"
    "v_bfe_u32 v29, v4, 2, 2\n"
    "v_xor_b32 v30, v28, v29\n"
    "v_lshlrev_b32 v30, 4, v30\n"
    "v_and_b32 v252, 3, v4\n"
    "v_lshl_add_u32 v30, v252, 6, v30\n"
    "v_lshrrev_b32 v253, 2, v27\n"
    "s_lshl_b32 s47, s37, 12\n"
    "s_add_u32 s47, s47, 114688\n"
    "v_lshl_add_u32 v17, v253, 9, v30\n"
    "v_add_u32 v17, s47, v17\n"
    "v_xor_b32 v18, 32, v17\n"
    "s_lshl_b32 s47, s38, 13\n"
    "s_add_u32 s47, s47, 0\n"
    "v_lshl_add_u32 v5, v253, 10, v30\n"
    "v_add_u32 v5, s47, v5\n"
    "v_xor_b32 v6, 32, v5\n"
    "v_and_b32 v254, 1, v252\n"
    "v_lshrrev_b32 v255, 1, v252\n"
    "v_lshl_or_b32 v254, v254, 1, v255\n"
    "v_lshlrev_b32 v254, 3, v254\n"
    "v_lshl_add_u32 v254, v29, 6, v254\n"
    "v_bfe_u32 v255, v4, 4, 1\n"
    "v_lshl_add_u32 v254, v255, 5, v254\n"
    "s_lshl_b32 s47, s38, 9\n"
    "s_add_u32 s52, s47, 81920\n"
    "v_lshl_add_u32 v19, v28, 11, v254\n"
    "v_add_u32 v19, s52, v19\n"
    "s_lshl_b32 s52, s38, 13\n"
    "s_add_u32 s52, s52, 16384\n"
    "s_add_u32 s53, s52, s47\n"
    "v_lshl_add_u32 v7, v28, 10, v254\n"
    "v_add_u32 v8, s52, v7\n"
    "v_add_u32 v7, s53, v7\n"
    "s_sub_u32 s47, 512, s47\n"
    "v_add_u32 v8, s47, v8\n"
    "s_lshl_b32 s47, s37, 13\n"
    "s_add_u32 s47, s47, 114688\n"
    "v_lshlrev_b32 v20, 4, v4\n"
    "v_add_u32 v20, s47, v20\n"
    "s_lshl_b32 s47, s38, 12\n"
    "v_add_u32 v31, s47, v20\n"
    "s_mov_b32 s41, 0x3e0293ee\n"
    "s_mov_b32 s49, 0x35800000\n"
    "v_mov_b32 v26, 0x44800000\n"
    "s_mul_i32 s47, s39, 1572864\n"
    "s_lshl_b32 s52, s40, 13\n"
    "s_add_u32 s47, s47, s52\n"
    "s_add_u32 s60, %[op_lo], s47\n"
    "s_addc_u32 s61, %[op_hi], 0\n"
    "s_and_b32 s61, s61, 0xffff\n"
    "s_sub_u32 s62, 1572864, s52\n"
    "s_mov_b32 s63, 0x20000\n"
    "v_mov_b32 v128, 0\n"
    "v_mov_b32 v129, 0\n"
    "v_mov_b32 v130, 0\n"
    "v_mov_b32 v131, 0\n"
    "v_mov_b32 v132, 0\n"
    "v_mov_b32 v133, 0\n"
    "v_mov_b32 v134, 0\n"
    "v_mov_b32 v135, 0\n"
    "v_mov_b32 v136, 0\n"
    "v_mov_b32 v137, 0\n"
    "v_mov_b32 v138, 0\n"
    "v_mov_b32 v139, 0\n"
    "v_mov_b32 v140, 0\n"
    "v_mov_b32 v141, 0\n"
    "v_mov_b32 v142, 0\n"
    "v_mov_b32 v143, 0\n"
    "v_mov_b32 v144, 0\n"
    "v_mov_b32 v145, 0\n"
    "v_mov_b32 v146, 0\n"
    "v_mov_b32 v147, 0\n"
    "v_mov_b32 v148, 0\n"
    "v_mov_b32 v149, 0\n"
    "v_mov_b32 v150, 0\n"
    "v_mov_b32 v151, 0\n"
    "v_mov_b32 v152, 0\n"
    "v_mov_b32 v153, 0\n"
    "v_mov_b32 v154, 0\n"
    "v_mov_b32 v155, 0\n"
    "v_mov_b32 v156, 0\n"
    "v_mov_b32 v157, 0\n"
    "v_mov_b32 v158, 0\n"
    "v_mov_b32 v159, 0\n"
    "v_mov_b32 v160, 0\n"
    "v_mov_b32 v161, 0\n"
    "v_mov_b32 v162, 0\n"
    "v_mov_b32 v163, 0\n"
    "v_mov_b32 v164, 0\n"
    "v_mov_b32 v165, 0\n"
    "v_mov_b32 v166, 0\n"
    "v_mov_b32 v167, 0\n"
    "v_mov_b32 v168, 0\n"
    "v_mov_b32 v169, 0\n"
    "v_mov_b32 v170, 0\n"
    "v_mov_b32 v171, 0\n"
    "v_mov_b32 v172, 0\n"
    "v_mov_b32 v173, 0\n"
    "v_mov_b32 v174, 0\n"
    "v_mov_b32 v175, 0\n"
    "v_mov_b32 v21, 0\n"
    "v_mov_b32 v22, 0\n"
    "v_mov_b32 v23, 0\n"
    "s_add_u32 m0, s48, 98304\n"
    "s_nop 0\n"
    "buffer_load_dwordx4 v13, s[64:67], s44 offen lds\n"
    "buffer_load_dwordx4 v14, s[64:67], s44 offen offset:1024 lds\n"
    "buffer_load_dwordx4 v15, s[64:67], s44 offen offset:2048 lds\n"
    "buffer_load_dwordx4 v16, s[64:67], s44 offen offset:3072 lds\n"
    "s_add_u32 s44, s44, 524288\n"
    "s_sub_u32 s47, s44, 5242880\n"
    "s_cmp_ge_u32 s44, s58\n"
    "s_cselect_b32 s44, s47, s44\n"
    "s_add_u32 m0, s45, 16384\n"
    "s_add_u32 s43, s42, 81920\n"
    "buffer_load_dwordx4 v24, s[28:31], s43 offen lds\n"
    "buffer_load_dwordx4 v25, s[28:31], s43 offen offset:1024 lds\n"
    "s_add_u32 s42, s42, 128\n"
    "s_sub_u32 s47, s42, 1280\n"
    "s_cmp_ge_u32 s42, s46\n"
    "s_cselect_b32 s42, s47, s42\n"
    "s_waitcnt vmcnt(9)\n"
    "s_barrier\n"
    "ds_read_b64_tr_b16 a[192:193], v19 offset:0\n"
    "ds_read_b64_tr_b16 a[194:195], v19 offset:1024\n"
    "ds_read_b64_tr_b16 a[196:197], v19 offset:256\n"
    "ds_read_b64_tr_b16 a[198:199], v19 offset:1280\n"
    "ds_read_b64_tr_b16 a[200:201], v19 offset:4096\n"
    "ds_read_b64_tr_b16 a[202:203], v19 offset:5120\n"
    "ds_read_b64_tr_b16 a[204:205], v19 offset:4352\n"
    "ds_read_b64_tr_b16 a[206:207], v19 offset:5376\n"
    "ds_read_b64_tr_b16 a[208:209], v19 offset:8192\n"
    "ds_read_b64_tr_b16 a[210:211], v19 offset:9216\n"
    "ds_read_b64_tr_b16 a[212:213], v19 offset:8448\n"
    "ds_read_b64_tr_b16 a[214:215], v19 offset:9472\n"
    "ds_read_b64_tr_b16 a[216:217], v19 offset:12288\n"
    "ds_read_b64_tr_b16 a[218:219], v19 offset:13312\n"
    "ds_read_b64_tr_b16 a[220:221], v19 offset:12544\n"
    "ds_read_b64_tr_b16 a[222:223], v19 offset:13568\n"
    "ds_read_b128 a[224:227], v17 offset:0\n"
    "ds_read_b128 a[228:231], v18 offset:0\n"
    "ds_read_b128 a[232:235], v17 offset:256\n"
    "ds_read_b128 a[236:239], v18 offset:256\n"
    "s_waitcnt lgkmcnt(0)\n"
    "v_mfma_f32_32x32x16_bf16 v[32:47], a[192:195], a[224:227], 0\n"
    "s_waitcnt vmcnt(6)\n"
    "s_barrier\n"
    "ds_read_b128 a[240:243], v17 offset:8192\n"
    "v_mfma_f32_32x32x16_bf16 v[80:95], a[196:199], a[224:227], 0\n"
    "ds_read_b128 a[244:247], v18 offset:8192\n"
    "s_add_u32 m0, s45, 24576\n"
    "s_add_u32 s43, s42, 0\n"
    "buffer_load_dwordx4 v24, s[28:31], s43 offen lds\n"
    "v_mfma_f32_32x32x16_bf16 v[32:47], a[200:203], a[228:231], v[32:47]\n"
    "ds_read_b128 a[248:251], v17 offset:8448\n"
    "buffer_load_dwordx4 v25, s[28:31], s43 offen offset:1024 lds\n"
    "s_add_u32 m0, s45, 32768\n"
    "s_add_u32 s43, s42, 40960\n"
    "v_mfma_f32_32x32x16_bf16 v[80:95], a[204:207], a[228:231], v[80:95]\n"
    "ds_read_b128 a[252:255], v18 offset:8448\n"
    "buffer_load_dwordx4 v24, s[28:31], s43 offen lds\n"
    "buffer_load_dwordx4 v25, s[28:31], s43 offen offset:1024 lds\n"
    "s_add_u32 m0, s48, 81920\n"
    "v_mfma_f32_32x32x16_bf16 v[32:47], a[208:211], a[232:235], v[32:47]\n"
    "s_nop 0\n"
    "buffer_load_dwordx4 v13, s[64:67], s44 offen lds\n"
    "buffer_load_dwordx4 v14, s[64:67], s44 offen offset:1024 lds\n"
    "v_mfma_f32_32x32x16_bf16 v[80:95], a[212:215], a[232:235], v[80:95]\n"
    "buffer_load_dwordx4 v15, s[64:67], s44 offen offset:2048 lds\n"
    "buffer_load_dwordx4 v16, s[64:67], s44 offen offset:3072 lds\n"
    "s_add_u32 s44, s44, 524288\n"
    "v_mfma_f32_32x32x16_bf16 v[32:47], a[216:219], a[236:239], v[32:47]\n"
    "s_sub_u32 s47, s44, 5242880\n"
    "s_cmp_ge_u32 s44, s58\n"
    "s_cselect_b32 s44, s47, s44\n"
    "v_mfma_f32_32x32x16_bf16 v[80:95], a[220:223], a[236:239], v[80:95]\n"
    "s_waitcnt lgkmcnt(0)\n"
    "v_mfma_f32_32x32x16_bf16 v[48:63], a[192:195], a[240:243], 0\n"
    "s_waitcnt vmcnt(8)\n"
    "s_barrier\n"
    "ds_read_b128 a[224:227], v17 offset:16384\n"
    "v_mfma_f32_32x32x16_bf16 v[96:111], a[196:199], a[240:243], 0\n"
    "ds_read_b128 a[228:231], v18 offset:16384\n"
    "s_add_u32 m0, s45, 40960\n"
    "s_add_u32 s43, s42, 81920\n"
    "v_mfma_f32_32x32x16_bf16 v[48:63], a[200:203], a[244:247], v[48:63]\n"
    "ds_read_b128 a[232:235], v17 offset:16640\n"
    "buffer_load_dwordx4 v24, s[28:31], s43 offen lds\n"
    "buffer_load_dwordx4 v25, s[28:31], s43 offen offset:1024 lds\n"
    "v_mfma_f32_32x32x16_bf16 v[96:111], a[204:207], a[244:247], v[96:111]\n"
    "ds_read_b128 a[236:239], v18 offset:16640\n"
    "s_add_u32 s42, s42, 128\n"
    "s_sub_u32 s47, s42, 1280\n"
    "v_mfma_f32_32x32x16_bf16 v[48:63], a[208:211], a[248:251], v[48:63]\n"
    "ds_read_b64_tr_b16 a[0:1], v19 offset:16384\n"
    "ds_read_b64_tr_b16 a[2:3], v19 offset:17408\n"
    "s_cmp_ge_u32 s42, s46\n"
    "s_cselect_b32 s42, s47, s42\n"
    "s_add_u32 m0, s45, 0\n"
    "v_mfma_f32_32x32x16_bf16 v[96:111], a[212:215], a[248:251], v[96:111]\n"
    "ds_read_b64_tr_b16 a[4:5], v19 offset:16640\n"
    "ds_read_b64_tr_b16 a[6:7], v19 offset:17664\n"
    "s_add_u32 s43, s42, 0\n"
    "buffer_load_dwordx4 v24, s[28:31], s43 offen lds\n"
    "v_mfma_f32_32x32x16_bf16 v[48:63], a[216:219], a[252:255], v[48:63]\n"
    "ds_read_b64_tr_b16 a[8:9], v19 offset:20480\n"
    "ds_read_b64_tr_b16 a[10:11], v19 offset:21504\n"
    "buffer_load_dwordx4 v25, s[28:31], s43 offen offset:1024 lds\n"
    "v_mfma_f32_32x32x16_bf16 v[96:111], a[220:223], a[252:255], v[96:111]\n"
    "ds_read_b64_tr_b16 a[12:13], v19 offset:20736\n"
    "ds_read_b64_tr_b16 a[14:15], v19 offset:21760\n"
    "s_waitcnt lgkmcnt(8)\n"
    "v_mfma_f32_32x32x16_bf16 v[64:79], a[192:195], a[224:227], 0\n"
    "s_waitcnt vmcnt(10)\n"
    "s_barrier\n"
    "ds_read_b64_tr_b16 a[16:17], v19 offset:24576\n"
    "ds_read_b64_tr_b16 a[18:19], v19 offset:25600\n"
    "v_mfma_f32_32x32x16_bf16 v[112:127], a[196:199], a[224:227], 0\n"
    "ds_read_b64_tr_b16 a[20:21], v19 offset:24832\n"
    "ds_read_b64_tr_b16 a[22:23], v19 offset:25856\n"
    "ds_read_b128 a[240:243], v17 offset:24576\n"
    "s_add_u32 m0, s45, 8192\n"
    "v_mfma_f32_32x32x16_bf16 v[64:79], a[200:203], a[228:231], v[64:79]\n"
    "ds_read_b64_tr_b16 a[24:25], v19 offset:28672\n"
    "ds_read_b64_tr_b16 a[26:27], v19 offset:29696\n"
    "ds_read_b128 a[244:247], v18 offset:24576\n"
    "s_add_u32 s43, s42, 40960\n"
    "v_mfma_f32_32x32x16_bf16 v[112:127], a[204:207], a[228:231], v[112:127]\n"
    "ds_read_b64_tr_b16 a[28:29], v19 offset:28928\n"
    "ds_read_b64_tr_b16 a[30:31], v19 offset:29952\n"
    "ds_read_b128 a[248:251], v17 offset:24832\n"
    "v_mfma_f32_32x32x16_bf16 v[64:79], a[208:211], a[232:235], v[64:79]\n"
    "ds_read_b128 a[252:255], v18 offset:24832\n"
    "buffer_load_dwordx4 v24, s[28:31], s43 offen lds\n"
    "v_mfma_f32_32x32x16_bf16 v[112:127], a[212:215], a[232:235], v[112:127]\n"
    "buffer_load_dwordx4 v25, s[28:31], s43 offen offset:1024 lds\n"
    "v_mfma_f32_32x32x16_bf16 v[64:79], a[216:219], a[236:239], v[64:79]\n"
    "v_mfma_f32_32x32x16_bf16 v[112:127], a[220:223], a[236:239], v[112:127]\n"
    "s_waitcnt lgkmcnt(0)\n"
    "v_mfma_f32_32x32x16_bf16 v[32:47], a[0:3], a[240:243], v[32:47]\n"
    "s_waitcnt vmcnt(10)\n"
    "s_barrier\n"
    "ds_read_b128 a[224:227], v17 offset:32768\n"
    "v_mfma_f32_32x32x16_bf16 v[80:95], a[4:7], a[240:243], v[80:95]\n"
    "ds_read_b128 a[228:231], v18 offset:32768\n"
    "s_add_u32 m0, s45, 16384\n"
    "s_add_u32 s43, s42, 81920\n"
    "buffer_load_dwordx4 v24, s[28:31], s43 offen lds\n"
    "v_mfma_f32_32x32x16_bf16 v[32:47], a[8:11], a[244:247], v[32:47]\n"
    "ds_read_b128 a[232:235], v17 offset:33024\n"
    "buffer_load_dwordx4 v25, s[28:31], s43 offen offset:1024 lds\n"
    "s_add_u32 s42, s42, 128\n"
    "s_sub_u32 s47, s42, 1280\n"
    "v_mfma_f32_32x32x16_bf16 v[80:95], a[12:15], a[244:247], v[80:95]\n"
    "ds_read_b128 a[236:239], v18 offset:33024\n"
    "s_cmp_ge_u32 s42, s46\n"
    "s_cselect_b32 s42, s47, s42\n"
    "s_add_u32 m0, s48, 98304\n"
    "v_mfma_f32_32x32x16_bf16 v[32:47], a[16:19], a[248:251], v[32:47]\n"
    "s_nop 0\n"
    "buffer_load_dwordx4 v13, s[64:67], s44 offen lds\n"
    "buffer_load_dwordx4 v14, s[64:67], s44 offen offset:1024 lds\n"
    "v_mfma_f32_32x32x16_bf16 v[80:95], a[20:23], a[248:251], v[80:95]\n"
    "buffer_load_dwordx4 v15, s[64:67], s44 offen offset:2048 lds\n"
    "buffer_load_dwordx4 v16, s[64:67], s44 offen offset:3072 lds\n"
    "s_add_u32 s44, s44, 524288\n"
    "v_mfma_f32_32x32x16_bf16 v[32:47], a[24:27], a[252:255], v[32:47]\n"
    "s_sub_u32 s47, s44, 5242880\n"
    "s_cmp_ge_u32 s44, s58\n"
    "s_cselect_b32 s44, s47, s44\n"
    "v_mfma_f32_32x32x16_bf16 v[80:95], a[28:31], a[252:255], v[80:95]\n"
    "s_waitcnt lgkmcnt(0)\n"
    "v_mfma_f32_32x32x16_bf16 v[48:63], a[0:3], a[224:227], v[48:63]\n"
    "s_waitcnt vmcnt(10)\n"
    "s_barrier\n"
    "ds_read_b128 a[240:243], v17 offset:40960\n"
    "v_mfma_f32_32x32x16_bf16 v[96:111], a[4:7], a[224:227], v[96:111]\n"
    "ds_read_b128 a[244:247], v18 offset:40960\n"
    "s_add_u32 m0, s45, 24576\n"
    "v_mfma_f32_32x32x16_bf16 v[48:63], a[8:11], a[228:231], v[48:63]\n"
    "ds_read_b128 a[248:251], v17 offset:41216\n"
    "s_add_u32 s43, s42, 0\n"
    "v_mfma_f32_32x32x16_bf16 v[96:111], a[12:15], a[228:231], v[96:111]\n"
    "ds_read_b128 a[252:255], v18 offset:41216\n"
    "v_mfma_f32_32x32x16_bf16 v[48:63], a[16:19], a[232:235], v[48:63]\n"
    "ds_read_b64_tr_b16 a[192:193], v19 offset:0\n"
    "ds_read_b64_tr_b16 a[194:195], v19 offset:1024\n"
    "buffer_load_dwordx4 v24, s[28:31], s43 offen lds\n"
    "v_mfma_f32_32x32x16_bf16 v[96:111], a[20:23], a[232:235], v[96:111]\n"
    "ds_read_b64_tr_b16 a[196:197], v19 offset:256\n"
    "ds_read_b64_tr_b16 a[198:199], v19 offset:1280\n"
    "buffer_load_dwordx4 v25, s[28:31], s43 offen offset:1024 lds\n"
    "v_mfma_f32_32x32x16_bf16 v[48:63], a[24:27], a[236:239], v[48:63]\n"
    "ds_read_b64_tr_b16 a[200:201], v19 offset:4096\n"
    "ds_read_b64_tr_b16 a[202:203], v19 offset:5120\n"
    "v_mfma_f32_32x32x16_bf16 v[96:111], a[28:31], a[236:239], v[96:111]\n"
    "ds_read_b64_tr_b16 a[204:205], v19 offset:4352\n"
    "ds_read_b64_tr_b16 a[206:207], v19 offset:5376\n"
    "s_waitcnt lgkmcnt(8)\n"
    "v_mfma_f32_32x32x16_bf16 v[64:79], a[0:3], a[240:243], v[64:79]\n"
    "s_waitcnt vmcnt(10)\n"
    "s_barrier\n"
    "ds_read_b64_tr_b16 a[208:209], v19 offset:8192\n"
    "ds_read_b64_tr_b16 a[210:211], v19 offset:9216\n"
    "v_mfma_f32_32x32x16_bf16 v[112:127], a[4:7], a[240:243], v[112:127]\n"
    "ds_read_b64_tr_b16 a[212:213], v19 offset:8448\n"
    "ds_read_b64_tr_b16 a[214:215], v19 offset:9472\n"
    "ds_read_b128 a[224:227], v17 offset:0\n"
    "s_add_u32 m0, s45, 32768\n"
    "v_mfma_f32_32x32x16_bf16 v[64:79], a[8:11], a[244:247], v[64:79]\n"
    "ds_read_b64_tr_b16 a[216:217], v19 offset:12288\n"
    "ds_read_b64_tr_b16 a[218:219], v19 offset:13312\n"
    "ds_read_b128 a[228:231], v18 offset:0\n"
    "s_add_u32 s43, s42, 40960\n"
    "v_mfma_f32_32x32x16_bf16 v[112:127], a[12:15], a[244:247], v[112:127]\n"
    "ds_read_b64_tr_b16 a[220:221], v19 offset:12544\n"
    "ds_read_b64_tr_b16 a[222:223], v19 offset:13568\n"
    "ds_read_b128 a[232:235], v17 offset:256\n"
    "v_mfma_f32_32x32x16_bf16 v[64:79], a[16:19], a[248:251], v[64:79]\n"
    "ds_read_b128 a[236:239], v18 offset:256\n"
    "buffer_load_dwordx4 v24, s[28:31], s43 offen lds\n"
    "v_mfma_f32_32x32x16_bf16 v[112:127], a[20:23], a[248:251], v[112:127]\n"
    "buffer_load_dwordx4 v25, s[28:31], s43 offen offset:1024 lds\n"
    "v_mfma_f32_32x32x16_bf16 v[64:79], a[24:27], a[252:255], v[64:79]\n"
    "v_mfma_f32_32x32x16_bf16 v[112:127], a[28:31], a[252:255], v[112:127]\n"
    "s_waitcnt lgkmcnt(0)\n"
    "v_mfma_f32_32x32x16_bf16 v[32:47], a[192:195], a[224:227], v[32:47]\n"
    "s_waitcnt vmcnt(10)\n"
    "s_barrier\n"
    "ds_read_b128 a[240:243], v17 offset:8192\n"
    "v_mfma_f32_32x32x16_bf16 v[80:95], a[196:199], a[224:227], v[80:95]\n"
    "ds_read_b128 a[244:247], v18 offset:8192\n"
    "s_add_u32 m0, s45, 40960\n"
    "s_add_u32 s43, s42, 81920\n"
    "buffer_load_dwordx4 v24, s[28:31], s43 offen lds\n"
    "v_mfma_f32_32x32x16_bf16 v[32:47], a[200:203], a[228:231], v[32:47]\n"
    "ds_read_b128 a[248:251], v17 offset:8448\n"
    "buffer_load_dwordx4 v25, s[28:31], s43 offen offset:1024 lds\n"
    "s_add_u32 s42, s42, 128\n"
    "s_sub_u32 s47, s42, 1280\n"
    "v_mfma_f32_32x32x16_bf16 v[80:95], a[204:207], a[228:231], v[80:95]\n"
    "ds_read_b128 a[252:255], v18 offset:8448\n"
    "s_cmp_ge_u32 s42, s46\n"
    "s_cselect_b32 s42, s47, s42\n"
    "s_add_u32 m0, s48, 81920\n"
    "v_mfma_f32_32x32x16_bf16 v[32:47], a[208:211], a[232:235], v[32:47]\n"
    "s_nop 0\n"
    "buffer_load_dwordx4 v13, s[64:67], s44 offen lds\n"
    "buffer_load_dwordx4 v14, s[64:67], s44 offen offset:1024 lds\n"
    "v_mfma_f32_32x32x16_bf16 v[80:95], a[212:215], a[232:235], v[80:95]\n"
    "buffer_load_dwordx4 v15, s[64:67], s44 offen offset:2048 lds\n"
    "buffer_load_dwordx4 v16, s[64:67], s44 offen offset:3072 lds\n"
    "s_add_u32 s44, s44, 524288\n"
    "v_mfma_f32_32x32x16_bf16 v[32:47], a[216:219], a[236:239], v[32:47]\n"
    "s_sub_u32 s47, s44, 5242880\n"
    "s_cmp_ge_u32 s44, s58\n"
    "s_cselect_b32 s44, s47, s44\n"
    "v_mfma_f32_32x32x16_bf16 v[80:95], a[220:223], a[236:239], v[80:95]\n"
    "s_waitcnt lgkmcnt(0)\n"
    "v_mfma_f32_32x32x16_bf16 v[48:63], a[192:195], a[240:243], v[48:63]\n"
    "s_waitcnt vmcnt(10)\n"
    "s_barrier\n"
    "ds_read_b128 a[224:227], v17 offset:16384\n"
    "v_mfma_f32_32x32x16_bf16 v[96:111], a[196:199], a[240:243], v[96:111]\n"
    "ds_read_b128 a[228:231], v18 offset:16384\n"
    "s_add_u32 m0, s45, 0\n"
    "v_mfma_f32_32x32x16_bf16 v[48:63], a[200:203], a[244:247], v[48:63]\n"
    "ds_read_b128 a[232:235], v17 offset:16640\n"
    "s_add_u32 s43, s42, 0\n"
    "v_mfma_f32_32x32x16_bf16 v[96:111], a[204:207], a[244:247], v[96:111]\n"
    "ds_read_b128 a[236:239], v18 offset:16640\n"
    "v_mfma_f32_32x32x16_bf16 v[48:63], a[208:211], a[248:251], v[48:63]\n"
    "ds_read_b64_tr_b16 a[0:1], v19 offset:16384\n"
    "ds_read_b64_tr_b16 a[2:3], v19 offset:17408\n"
    "buffer_load_dwordx4 v24, s[28:31], s43 offen lds\n"
    "v_mfma_f32_32x32x16_bf16 v[96:111], a[212:215], a[248:251], v[96:111]\n"
    "ds_read_b64_tr_b16 a[4:5], v19 offset:16640\n"
    "ds_read_b64_tr_b16 a[6:7], v19 offset:17664\n"
    "buffer_load_dwordx4 v25, s[28:31], s43 offen offset:1024 lds\n"
    "v_mfma_f32_32x32x16_bf16 v[48:63], a[216:219], a[252:255], v[48:63]\n"
    "ds_read_b64_tr_b16 a[8:9], v19 offset:20480\n"
    "ds_read_b64_tr_b16 a[10:11], v19 offset:21504\n"
    "v_mfma_f32_32x32x16_bf16 v[96:111], a[220:223], a[252:255], v[96:111]\n"
    "ds_read_b64_tr_b16 a[12:13], v19 offset:20736\n"
    "ds_read_b64_tr_b16 a[14:15], v19 offset:21760\n"
    "s_waitcnt lgkmcnt(8)\n"
    "v_mfma_f32_32x32x16_bf16 v[64:79], a[192:195], a[224:227], v[64:79]\n"
    "s_waitcnt vmcnt(10)\n"
    "s_barrier\n"
    "ds_read_b64_tr_b16 a[16:17], v19 offset:24576\n"
    "ds_read_b64_tr_b16 a[18:19], v19 offset:25600\n"
    "v_mfma_f32_32x32x16_bf16 v[112:127], a[196:199], a[224:227], v[112:127]\n"
    "ds_read_b64_tr_b16 a[20:21], v19 offset:24832\n"
    "ds_read_b64_tr_b16 a[22:23], v19 offset:25856\n"
    "ds_read_b128 a[240:243], v17 offset:24576\n"
    "s_add_u32 m0, s45, 8192\n"
    "v_mfma_f32_32x32x16_bf16 v[64:79], a[200:203], a[228:231], v[64:79]\n"
    "ds_read_b64_tr_b16 a[24:25], v19 offset:28672\n"
    "ds_read_b64_tr_b16 a[26:27], v19 offset:29696\n"
    "ds_read_b128 a[244:247], v18 offset:24576\n"
    "s_add_u32 s43, s42, 40960\n"
    "v_mfma_f32_32x32x16_bf16 v[112:127], a[204:207], a[228:231], v[112:127]\n"
    "ds_read_b64_tr_b16 a[28:29], v19 offset:28928\n"
    "ds_read_b64_tr_b16 a[30:31], v19 offset:29952\n"
    "ds_read_b128 a[248:251], v17 offset:24832\n"
    "v_mfma_f32_32x32x16_bf16 v[64:79], a[208:211], a[232:235], v[64:79]\n"
    "ds_read_b128 a[252:255], v18 offset:24832\n"
    "buffer_load_dwordx4 v24, s[28:31], s43 offen lds\n"
    "v_mfma_f32_32x32x16_bf16 v[112:127], a[212:215], a[232:235], v[112:127]\n"
    "buffer_load_dwordx4 v25, s[28:31], s43 offen offset:1024 lds\n"
    "v_mfma_f32_32x32x16_bf16 v[64:79], a[216:219], a[236:239], v[64:79]\n"
    "v_mfma_f32_32x32x16_bf16 v[112:127], a[220:223], a[236:239], v[112:127]\n"
    "s_waitcnt lgkmcnt(0)\n"
    "v_mfma_f32_32x32x16_bf16 v[32:47], a[0:3], a[240:243], v[32:47]\n"
    "s_waitcnt vmcnt(10)\n"
    "s_barrier\n"
    "ds_read_b128 a[224:227], v17 offset:32768\n"
    "v_mfma_f32_32x32x16_bf16 v[80:95], a[4:7], a[240:243], v[80:95]\n"
    "ds_read_b128 a[228:231], v18 offset:32768\n"
    "s_add_u32 m0, s45, 16384\n"
    "s_add_u32 s43, s42, 81920\n"
    "buffer_load_dwordx4 v24, s[28:31], s43 offen lds\n"
    "v_mfma_f32_32x32x16_bf16 v[32:47], a[8:11], a[244:247], v[32:47]\n"
    "ds_read_b128 a[232:235], v17 offset:33024\n"
    "buffer_load_dwordx4 v25, s[28:31], s43 offen offset:1024 lds\n"
    "s_add_u32 s42, s42, 128\n"
    "s_sub_u32 s47, s42, 1280\n"
    "v_mfma_f32_32x32x16_bf16 v[80:95], a[12:15], a[244:247], v[80:95]\n"
    "ds_read_b128 a[236:239], v18 offset:33024\n"
    "s_cmp_ge_u32 s42, s46\n"
    "s_cselect_b32 s42, s47, s42\n"
    "s_add_u32 m0, s48, 98304\n"
    "v_mfma_f32_32x32x16_bf16 v[32:47], a[16:19], a[248:251], v[32:47]\n"
    "s_nop 0\n"
    "buffer_load_dwordx4 v13, s[64:67], s44 offen lds\n"
    "buffer_load_dwordx4 v14, s[64:67], s44 offen offset:1024 lds\n"
    "v_mfma_f32_32x32x16_bf16 v[80:95], a[20:23], a[248:251], v[80:95]\n"
    "buffer_load_dwordx4 v15, s[64:67], s44 offen offset:2048 lds\n"
    "buffer_load_dwordx4 v16, s[64:67], s44 offen offset:3072 lds\n"
    "s_add_u32 s44, s44, 524288\n"
    "v_mfma_f32_32x32x16_bf16 v[32:47], a[24:27], a[252:255], v[32:47]\n"
    "s_sub_u32 s47, s44, 5242880\n"
    "s_cmp_ge_u32 s44, s58\n"
    "s_cselect_b32 s44, s47, s44\n"
    "v_mfma_f32_32x32x16_bf16 v[80:95], a[28:31], a[252:255], v[80:95]\n"
    "s_waitcnt lgkmcnt(0)\n"
    "v_mfma_f32_32x32x16_bf16 v[48:63], a[0:3], a[224:227], v[48:63]\n"
    "s_waitcnt vmcnt(10)\n"
    "s_barrier\n"
    "ds_read_b128 a[240:243], v17 offset:40960\n"
    "v_mfma_f32_32x32x16_bf16 v[96:111], a[4:7], a[224:227], v[96:111]\n"
    "ds_read_b128 a[244:247], v18 offset:40960\n"
    "s_add_u32 m0, s45, 24576\n"
    "v_mfma_f32_32x32x16_bf16 v[48:63], a[8:11], a[228:231], v[48:63]\n"
    "ds_read_b128 a[248:251], v17 offset:41216\n"
    "s_add_u32 s43, s42, 0\n"
    "v_mfma_f32_32x32x16_bf16 v[96:111], a[12:15], a[228:231], v[96:111]\n"
    "ds_read_b128 a[252:255], v18 offset:41216\n"
    "v_mfma_f32_32x32x16_bf16 v[48:63], a[16:19], a[232:235], v[48:63]\n"
    "ds_read_b64_tr_b16 a[192:193], v19 offset:0\n"
    "ds_read_b64_tr_b16 a[194:195], v19 offset:1024\n"
    "buffer_load_dwordx4 v24, s[28:31], s43 offen lds\n"
    "v_mfma_f32_32x32x16_bf16 v[96:111], a[20:23], a[232:235], v[96:111]\n"
    "ds_read_b64_tr_b16 a[196:197], v19 offset:256\n"
    "ds_read_b64_tr_b16 a[198:199], v19 offset:1280\n"
    "buffer_load_dwordx4 v25, s[28:31], s43 offen offset:1024 lds\n"
    "v_mfma_f32_32x32x16_bf16 v[48:63], a[24:27], a[236:239], v[48:63]\n"
    "ds_read_b64_tr_b16 a[200:201], v19 offset:4096\n"
    "ds_read_b64_tr_b16 a[202:203], v19 offset:5120\n"
    "v_mfma_f32_32x32x16_bf16 v[96:111], a[28:31], a[236:239], v[96:111]\n"
    "ds_read_b64_tr_b16 a[204:205], v19 offset:4352\n"
    "ds_read_b64_tr_b16 a[206:207], v19 offset:5376\n"
    "s_cmp_eq_u32 s68, 0\n"
    "s_cbranch_scc0 .L_nb10_0\n"
    "s_add_u32 m0, s48, 0\n"
    "s_add_u32 s43, s48, 0\n"
    "buffer_load_dwordx4 v9, s[20:23], s43 offen nt lds\n"
    "buffer_load_dwordx4 v10, s[20:23], s43 offen offset:1024 nt lds\n"
    "buffer_load_dwordx4 v11, s[20:23], s43 offen offset:2048 nt lds\n"
    "buffer_load_dwordx4 v12, s[20:23], s43 offen offset:3072 nt lds\n"
    "s_add_u32 m0, s48, 16384\n"
    "s_add_u32 s43, s48, 0\n"
    "buffer_load_dwordx4 v9, s[24:27], s43 offen nt lds\n"
    "buffer_load_dwordx4 v9, s[24:27], s43 offen offset:1024 nt lds\n"
    "buffer_load_dwordx4 v9, s[24:27], s43 offen offset:2048 nt lds\n"
    "buffer_load_dwordx4 v9, s[24:27], s43 offen offset:3072 nt lds\n"
    ".L_nb10_0:\n"
    "s_waitcnt lgkmcnt(8)\n"
    "v_mfma_f32_32x32x16_bf16 v[64:79], a[0:3], a[240:243], v[64:79]\n"
    "s_waitcnt vmcnt(10)\n"
    "s_barrier\n"
    "ds_read_b64_tr_b16 a[208:209], v19 offset:8192\n"
    "ds_read_b64_tr_b16 a[210:211], v19 offset:9216\n"
    "v_mfma_f32_32x32x16_bf16 v[112:127], a[4:7], a[240:243], v[112:127]\n"
    "ds_read_b64_tr_b16 a[212:213], v19 offset:8448\n"
    "ds_read_b64_tr_b16 a[214:215], v19 offset:9472\n"
    "ds_read_b128 a[224:227], v17 offset:0\n"
    "s_add_u32 m0, s45, 32768\n"
    "v_mfma_f32_32x32x16_bf16 v[64:79], a[8:11], a[244:247], v[64:79]\n"
    "ds_read_b64_tr_b16 a[216:217], v19 offset:12288\n"
    "ds_read_b64_tr_b16 a[218:219], v19 offset:13312\n"
    "ds_read_b128 a[228:231], v18 offset:0\n"
    "s_add_u32 s43, s42, 40960\n"
    "v_mfma_f32_32x32x16_bf16 v[112:127], a[12:15], a[244:247], v[112:127]\n"
    "ds_read_b64_tr_b16 a[220:221], v19 offset:12544\n"
    "ds_read_b64_tr_b16 a[222:223], v19 offset:13568\n"
    "ds_read_b128 a[232:235], v17 offset:256\n"
    "v_mfma_f32_32x32x16_bf16 v[64:79], a[16:19], a[248:251], v[64:79]\n"
    "ds_read_b128 a[236:239], v18 offset:256\n"
    "buffer_load_dwordx4 v24, s[28:31], s43 offen lds\n"
    "v_mfma_f32_32x32x16_bf16 v[112:127], a[20:23], a[248:251], v[112:127]\n"
    "buffer_load_dwordx4 v25, s[28:31], s43 offen offset:1024 lds\n"
    "v_mfma_f32_32x32x16_bf16 v[64:79], a[24:27], a[252:255], v[64:79]\n"
    "v_mfma_f32_32x32x16_bf16 v[112:127], a[28:31], a[252:255], v[112:127]\n"
    "s_cmp_eq_u32 s68, 2\n"
    "s_cbranch_scc0 .L_nb11_2\n"
    "s_add_u32 m0, s48, 0\n"
    "s_add_u32 s43, s48, 0\n"
    "buffer_load_dwordx4 v9, s[20:23], s43 offen nt lds\n"
    "buffer_load_dwordx4 v10, s[20:23], s43 offen offset:1024 nt lds\n"
    "buffer_load_dwordx4 v11, s[20:23], s43 offen offset:2048 nt lds\n"
    "buffer_load_dwordx4 v12, s[20:23], s43 offen offset:3072 nt lds\n"
    "s_add_u32 m0, s48, 16384\n"
    "s_add_u32 s43, s48, 0\n"
    "buffer_load_dwordx4 v9, s[24:27], s43 offen nt lds\n"
    "buffer_load_dwordx4 v9, s[24:27], s43 offen offset:1024 nt lds\n"
    "buffer_load_dwordx4 v9, s[24:27], s43 offen offset:2048 nt lds\n"
    "buffer_load_dwordx4 v9, s[24:27], s43 offen offset:3072 nt lds\n"
    ".L_nb11_2:\n"
    "s_waitcnt lgkmcnt(0)\n"
    "v_mfma_f32_32x32x16_bf16 v[32:47], a[192:195], a[224:227], v[32:47]\n"
    "s_waitcnt vmcnt(10)\n"
    "s_barrier\n"
    "ds_read_b128 a[240:243], v17 offset:8192\n"
    "v_mfma_f32_32x32x16_bf16 v[80:95], a[196:199], a[224:227], v[80:95]\n"
    "ds_read_b128 a[244:247], v18 offset:8192\n"
    "s_add_u32 m0, s45, 40960\n"
    "s_add_u32 s43, s42, 81920\n"
    "buffer_load_dwordx4 v24, s[28:31], s43 offen lds\n"
    "v_mfma_f32_32x32x16_bf16 v[32:47], a[200:203], a[228:231], v[32:47]\n"
    "ds_read_b128 a[248:251], v17 offset:8448\n"
    "buffer_load_dwordx4 v25, s[28:31], s43 offen offset:1024 lds\n"
    "s_add_u32 s42, s42, 128\n"
    "s_sub_u32 s47, s42, 1280\n"
    "v_mfma_f32_32x32x16_bf16 v[80:95], a[204:207], a[228:231], v[80:95]\n"
    "ds_read_b128 a[252:255], v18 offset:8448\n"
    "s_cmp_ge_u32 s42, s46\n"
    "s_cselect_b32 s42, s47, s42\n"
    "s_add_u32 m0, s48, 81920\n"
    "v_mfma_f32_32x32x16_bf16 v[32:47], a[208:211], a[232:235], v[32:47]\n"
    "s_nop 0\n"
    "buffer_load_dwordx4 v13, s[64:67], s44 offen lds\n"
    "buffer_load_dwordx4 v14, s[64:67], s44 offen offset:1024 lds\n"
    "v_mfma_f32_32x32x16_bf16 v[80:95], a[212:215], a[232:235], v[80:95]\n"
    "buffer_load_dwordx4 v15, s[64:67], s44 offen offset:2048 lds\n"
    "buffer_load_dwordx4 v16, s[64:67], s44 offen offset:3072 lds\n"
    "s_add_u32 s44, s44, 524288\n"
    "v_mfma_f32_32x32x16_bf16 v[32:47], a[216:219], a[236:239], v[32:47]\n"
    "s_sub_u32 s47, s44, 5242880\n"
    "s_cmp_ge_u32 s44, s58\n"
    "s_cselect_b32 s44, s47, s44\n"
    "v_mfma_f32_32x32x16_bf16 v[80:95], a[220:223], a[236:239], v[80:95]\n"
    "s_waitcnt lgkmcnt(0)\n"
    "v_mfma_f32_32x32x16_bf16 v[48:63], a[192:195], a[240:243], v[48:63]\n"
    "s_waitcnt vmcnt(10)\n"
    "s_barrier\n"
    "ds_read_b128 a[224:227], v17 offset:16384\n"
    "v_mfma_f32_32x32x16_bf16 v[96:111], a[196:199], a[240:243], v[96:111]\n"
    "ds_read_b128 a[228:231], v18 offset:16384\n"
    "s_add_u32 m0, s45, 0\n"
    "v_mfma_f32_32x32x16_bf16 v[48:63], a[200:203], a[244:247], v[48:63]\n"
    "ds_read_b128 a[232:235], v17 offset:16640\n"
    "s_add_u32 s43, s42, 0\n"
    "v_mfma_f32_32x32x16_bf16 v[96:111], a[204:207], a[244:247], v[96:111]\n"
    "ds_read_b128 a[236:239], v18 offset:16640\n"
    "v_mfma_f32_32x32x16_bf16 v[48:63], a[208:211], a[248:251], v[48:63]\n"
    "ds_read_b64_tr_b16 a[0:1], v19 offset:16384\n"
    "ds_read_b64_tr_b16 a[2:3], v19 offset:17408\n"
    "buffer_load_dwordx4 v24, s[28:31], s43 offen lds\n"
    "v_mfma_f32_32x32x16_bf16 v[96:111], a[212:215], a[248:251], v[96:111]\n"
    "ds_read_b64_tr_b16 a[4:5], v19 offset:16640\n"
    "ds_read_b64_tr_b16 a[6:7], v19 offset:17664\n"
    "buffer_load_dwordx4 v25, s[28:31], s43 offen offset:1024 lds\n"
    "v_mfma_f32_32x32x16_bf16 v[48:63], a[216:219], a[252:255], v[48:63]\n"
    "ds_read_b64_tr_b16 a[8:9], v19 offset:20480\n"
    "ds_read_b64_tr_b16 a[10:11], v19 offset:21504\n"
    "v_mfma_f32_32x32x16_bf16 v[96:111], a[220:223], a[252:255], v[96:111]\n"
    "ds_read_b64_tr_b16 a[12:13], v19 offset:20736\n"
    "ds_read_b64_tr_b16 a[14:15], v19 offset:21760\n"
    "s_cmp_eq_u32 s68, 1\n"
    "s_cbranch_scc0 .L_nb13_1\n"
    "s_add_u32 m0, s48, 0\n"
    "s_add_u32 s43, s48, 0\n"
    "buffer_load_dwordx4 v9, s[20:23], s43 offen nt lds\n"
    "buffer_load_dwordx4 v10, s[20:23], s43 offen offset:1024 nt lds\n"
    "buffer_load_dwordx4 v11, s[20:23], s43 offen offset:2048 nt lds\n"
    "buffer_load_dwordx4 v12, s[20:23], s43 offen offset:3072 nt lds\n"
    "s_add_u32 m0, s48, 16384\n"
    "s_add_u32 s43, s48, 0\n"
    "buffer_load_dwordx4 v9, s[24:27], s43 offen nt lds\n"
    "buffer_load_dwordx4 v9, s[24:27], s43 offen offset:1024 nt lds\n"
    "buffer_load_dwordx4 v9, s[24:27], s43 offen offset:2048 nt lds\n"
    "buffer_load_dwordx4 v9, s[24:27], s43 offen offset:3072 nt lds\n"
    ".L_nb13_1:\n"
    "s_waitcnt lgkmcnt(8)\n"
    "v_mfma_f32_32x32x16_bf16 v[64:79], a[192:195], a[224:227], v[64:79]\n"
    "s_waitcnt vmcnt(10)\n"
    "s_barrier\n"
    "ds_read_b64_tr_b16 a[16:17], v19 offset:24576\n"
    "ds_read_b64_tr_b16 a[18:19], v19 offset:25600\n"
    "v_mfma_f32_32x32x16_bf16 v[112:127], a[196:199], a[224:227], v[112:127]\n"
    "ds_read_b64_tr_b16 a[20:21], v19 offset:24832\n"
    "ds_read_b64_tr_b16 a[22:23], v19 offset:25856\n"
    "ds_read_b128 a[240:243], v17 offset:24576\n"
    "s_add_u32 m0, s45, 8192\n"
    "v_mfma_f32_32x32x16_bf16 v[64:79], a[200:203], a[228:231], v[64:79]\n"
    "ds_read_b64_tr_b16 a[24:25], v19 offset:28672\n"
    "ds_read_b64_tr_b16 a[26:27], v19 offset:29696\n"
    "ds_read_b128 a[244:247], v18 offset:24576\n"
    "s_add_u32 s43, s42, 40960\n"
    "v_mfma_f32_32x32x16_bf16 v[112:127], a[204:207], a[228:231], v[112:127]\n"
    "ds_read_b64_tr_b16 a[28:29], v19 offset:28928\n"
    "ds_read_b64_tr_b16 a[30:31], v19 offset:29952\n"
    "ds_read_b128 a[248:251], v17 offset:24832\n"
    "v_mfma_f32_32x32x16_bf16 v[64:79], a[208:211], a[232:235], v[64:79]\n"
    "ds_read_b128 a[252:255], v18 offset:24832\n"
    "buffer_load_dwordx4 v24, s[28:31], s43 offen lds\n"
    "v_mfma_f32_32x32x16_bf16 v[112:127], a[212:215], a[232:235], v[112:127]\n"
    "buffer_load_dwordx4 v25, s[28:31], s43 offen offset:1024 lds\n"
    "v_mfma_f32_32x32x16_bf16 v[64:79], a[216:219], a[236:239], v[64:79]\n"
    "v_mfma_f32_32x32x16_bf16 v[112:127], a[220:223], a[236:239], v[112:127]\n"
    "s_cmp_eq_u32 s68, 3\n"
    "s_cbranch_scc0 .L_nb14_3\n"
    "s_add_u32 m0, s48, 0\n"
    "s_add_u32 s43, s48, 0\n"
    "buffer_load_dwordx4 v9, s[20:23], s43 offen nt lds\n"
    "buffer_load_dwordx4 v10, s[20:23], s43 offen offset:1024 nt lds\n"
    "buffer_load_dwordx4 v11, s[20:23], s43 offen offset:2048 nt lds\n"
    "buffer_load_dwordx4 v12, s[20:23], s43 offen offset:3072 nt lds\n"
    "s_add_u32 m0, s48, 16384\n"
    "s_add_u32 s43, s48, 0\n"
    "buffer_load_dwordx4 v9, s[24:27], s43 offen nt lds\n"
    "buffer_load_dwordx4 v9, s[24:27], s43 offen offset:1024 nt lds\n"
    "buffer_load_dwordx4 v9, s[24:27], s43 offen offset:2048 nt lds\n"
    "buffer_load_dwordx4 v9, s[24:27], s43 offen offset:3072 nt lds\n"
    ".L_nb14_3:\n"
    "s_waitcnt lgkmcnt(0)\n"
    "v_mfma_f32_32x32x16_bf16 v[32:47], a[0:3], a[240:243], v[32:47]\n"
    "s_waitcnt vmcnt(10)\n"
    "s_barrier\n"
    "ds_read_b128 a[224:227], v17 offset:32768\n"
    "v_mfma_f32_32x32x16_bf16 v[80:95], a[4:7], a[240:243], v[80:95]\n"
    "ds_read_b128 a[228:231], v18 offset:32768\n"
    "s_add_u32 m0, s45, 16384\n"
    "s_add_u32 s43, s42, 81920\n"
    "buffer_load_dwordx4 v24, s[28:31], s43 offen lds\n"
    "v_mfma_f32_32x32x16_bf16 v[32:47], a[8:11], a[244:247], v[32:47]\n"
    "ds_read_b128 a[232:235], v17 offset:33024\n"
    "buffer_load_dwordx4 v25, s[28:31], s43 offen offset:1024 lds\n"
    "s_add_u32 s42, s42, 128\n"
    "s_sub_u32 s47, s42, 1280\n"
    "v_mfma_f32_32x32x16_bf16 v[80:95], a[12:15], a[244:247], v[80:95]\n"
    "ds_read_b128 a[236:239], v18 offset:33024\n"
    "s_cmp_ge_u32 s42, s46\n"
    "s_cselect_b32 s42, s47, s42\n"
    "s_add_u32 m0, s48, 98304\n"
    "v_mfma_f32_32x32x16_bf16 v[32:47], a[16:19], a[248:251], v[32:47]\n"
    "s_nop 0\n"
    "buffer_load_dwordx4 v13, s[64:67], s44 offen lds\n"
    "buffer_load_dwordx4 v14, s[64:67], s44 offen offset:1024 lds\n"
    "v_mfma_f32_32x32x16_bf16 v[80:95], a[20:23], a[248:251], v[80:95]\n"
    "buffer_load_dwordx4 v15, s[64:67], s44 offen offset:2048 lds\n"
    "buffer_load_dwordx4 v16, s[64:67], s44 offen offset:3072 lds\n"
    "s_add_u32 s44, s44, 524288\n"
    "v_mfma_f32_32x32x16_bf16 v[32:47], a[24:27], a[252:255], v[32:47]\n"
    "s_sub_u32 s47, s44, 5242880\n"
    "s_cmp_ge_u32 s44, s58\n"
    "s_cselect_b32 s44, s47, s44\n"
    "v_mfma_f32_32x32x16_bf16 v[80:95], a[28:31], a[252:255], v[80:95]\n"
    "s_waitcnt lgkmcnt(0)\n"
    "v_mfma_f32_32x32x16_bf16 v[48:63], a[0:3], a[224:227], v[48:63]\n"
    "s_waitcnt vmcnt(10)\n"
    "s_barrier\n"
    "ds_read_b128 a[240:243], v17 offset:40960\n"
    "v_mfma_f32_32x32x16_bf16 v[96:111], a[4:7], a[224:227], v[96:111]\n"
    "ds_read_b128 a[244:247], v18 offset:40960\n"
    "s_add_u32 m0, s45, 24576\n"
    "v_mfma_f32_32x32x16_bf16 v[48:63], a[8:11], a[228:231], v[48:63]\n"
    "ds_read_b128 a[248:251], v17 offset:41216\n"
    "s_add_u32 s43, s42, 0\n"
    "v_mfma_f32_32x32x16_bf16 v[96:111], a[12:15], a[228:231], v[96:111]\n"
    "ds_read_b128 a[252:255], v18 offset:41216\n"
    "v_mfma_f32_32x32x16_bf16 v[48:63], a[16:19], a[232:235], v[48:63]\n"
    "ds_read_b64_tr_b16 a[192:193], v19 offset:0\n"
    "ds_read_b64_tr_b16 a[194:195], v19 offset:1024\n"
    "buffer_load_dwordx4 v24, s[28:31], s43 offen lds\n"
    "v_mfma_f32_32x32x16_bf16 v[96:111], a[20:23], a[232:235], v[96:111]\n"
    "ds_read_b64_tr_b16 a[196:197], v19 offset:256\n"
    "ds_read_b64_tr_b16 a[198:199], v19 offset:1280\n"
    "buffer_load_dwordx4 v25, s[28:31], s43 offen offset:1024 lds\n"
    "v_mfma_f32_32x32x16_bf16 v[48:63], a[24:27], a[236:239], v[48:63]\n"
    "ds_read_b64_tr_b16 a[200:201], v19 offset:4096\n"
    "ds_read_b64_tr_b16 a[202:203], v19 offset:5120\n"
    "v_mfma_f32_32x32x16_bf16 v[96:111], a[28:31], a[236:239], v[96:111]\n"
    "ds_read_b64_tr_b16 a[204:205], v19 offset:4352\n"
    "ds_read_b64_tr_b16 a[206:207], v19 offset:5376\n"
    "s_waitcnt lgkmcnt(8)\n"
    "v_mfma_f32_32x32x16_bf16 v[64:79], a[0:3], a[240:243], v[64:79]\n"
    "s_waitcnt vmcnt(10)\n"
    "s_barrier\n"
    "ds_read_b64_tr_b16 a[208:209], v19 offset:8192\n"
    "ds_read_b64_tr_b16 a[210:211], v19 offset:9216\n"
    "v_mfma_f32_32x32x16_bf16 v[112:127], a[4:7], a[240:243], v[112:127]\n"
    "ds_read_b64_tr_b16 a[212:213], v19 offset:8448\n"
    "ds_read_b64_tr_b16 a[214:215], v19 offset:9472\n"
    "ds_read_b128 a[224:227], v17 offset:0\n"
    "s_add_u32 m0, s45, 32768\n"
    "v_mfma_f32_32x32x16_bf16 v[64:79], a[8:11], a[244:247], v[64:79]\n"
    "ds_read_b64_tr_b16 a[216:217], v19 offset:12288\n"
    "ds_read_b64_tr_b16 a[218:219], v19 offset:13312\n"
    "ds_read_b128 a[228:231], v18 offset:0\n"
    "s_add_u32 s43, s42, 40960\n"
    "v_mfma_f32_32x32x16_bf16 v[112:127], a[12:15], a[244:247], v[112:127]\n"
    "ds_read_b64_tr_b16 a[220:221], v19 offset:12544\n"
    "ds_read_b64_tr_b16 a[222:223], v19 offset:13568\n"
    "ds_read_b128 a[232:235], v17 offset:256\n"
    "v_mfma_f32_32x32x16_bf16 v[64:79], a[16:19], a[248:251], v[64:79]\n"
    "ds_read_b128 a[236:239], v18 offset:256\n"
    "buffer_load_dwordx4 v24, s[28:31], s43 offen lds\n"
    "v_mfma_f32_32x32x16_bf16 v[112:127], a[20:23], a[248:251], v[112:127]\n"
    "buffer_load_dwordx4 v25, s[28:31], s43 offen offset:1024 lds\n"
    "v_mfma_f32_32x32x16_bf16 v[64:79], a[24:27], a[252:255], v[64:79]\n"
    "v_mfma_f32_32x32x16_bf16 v[112:127], a[28:31], a[252:255], v[112:127]\n"
    "s_cmp_eq_u32 s68, 0\n"
    "s_cbranch_scc0 .L_nb17_0\n"
    "s_add_u32 m0, s48, 32768\n"
    "s_add_u32 s43, s48, 16384\n"
    "buffer_load_dwordx4 v9, s[20:23], s43 offen nt lds\n"
    "buffer_load_dwordx4 v10, s[20:23], s43 offen offset:1024 nt lds\n"
    "buffer_load_dwordx4 v11, s[20:23], s43 offen offset:2048 nt lds\n"
    "buffer_load_dwordx4 v12, s[20:23], s43 offen offset:3072 nt lds\n"
    ".L_nb17_0:\n"
    "s_waitcnt lgkmcnt(0)\n"
    "v_mfma_f32_32x32x16_bf16 v[32:47], a[192:195], a[224:227], v[32:47]\n"
    "s_waitcnt vmcnt(10)\n"
    "s_barrier\n"
    "ds_read_b128 a[240:243], v17 offset:8192\n"
    "v_mfma_f32_32x32x16_bf16 v[80:95], a[196:199], a[224:227], v[80:95]\n"
    "ds_read_b128 a[244:247], v18 offset:8192\n"
    "s_add_u32 m0, s45, 40960\n"
    "s_add_u32 s43, s42, 81920\n"
    "buffer_load_dwordx4 v24, s[28:31], s43 offen lds\n"
    "v_mfma_f32_32x32x16_bf16 v[32:47], a[200:203], a[228:231], v[32:47]\n"
    "ds_read_b128 a[248:251], v17 offset:8448\n"
    "buffer_load_dwordx4 v25, s[28:31], s43 offen offset:1024 lds\n"
    "s_add_u32 s42, s42, 128\n"
    "s_sub_u32 s47, s42, 1280\n"
    "v_mfma_f32_32x32x16_bf16 v[80:95], a[204:207], a[228:231], v[80:95]\n"
    "ds_read_b128 a[252:255], v18 offset:8448\n"
    "s_cmp_ge_u32 s42, s46\n"
    "s_cselect_b32 s42, s47, s42\n"
    "s_add_u32 m0, s48, 81920\n"
    "v_mfma_f32_32x32x16_bf16 v[32:47], a[208:211], a[232:235], v[32:47]\n"
    "s_nop 0\n"
    "buffer_load_dwordx4 v13, s[64:67], s44 offen lds\n"
    "buffer_load_dwordx4 v14, s[64:67], s44 offen offset:1024 lds\n"
    "v_mfma_f32_32x32x16_bf16 v[80:95], a[212:215], a[232:235], v[80:95]\n"
    "buffer_load_dwordx4 v15, s[64:67], s44 offen offset:2048 lds\n"
    "buffer_load_dwordx4 v16, s[64:67], s44 offen offset:3072 lds\n"
    "s_add_u32 s44, s44, 524288\n"
    "v_mfma_f32_32x32x16_bf16 v[32:47], a[216:219], a[236:239], v[32:47]\n"
    "s_sub_u32 s47, s44, 5242880\n"
    "s_cmp_ge_u32 s44, s58\n"
    "s_cselect_b32 s44, s47, s44\n"
    "v_mfma_f32_32x32x16_bf16 v[80:95], a[220:223], a[236:239], v[80:95]\n"
    "s_cmp_eq_u32 s68, 2\n"
    "s_cbranch_scc0 .L_nb18_2\n"
    "s_add_u32 m0, s48, 32768\n"
    "s_add_u32 s43, s48, 16384\n"
    "buffer_load_dwordx4 v9, s[20:23], s43 offen nt lds\n"
    "buffer_load_dwordx4 v10, s[20:23], s43 offen offset:1024 nt lds\n"
    "buffer_load_dwordx4 v11, s[20:23], s43 offen offset:2048 nt lds\n"
    "buffer_load_dwordx4 v12, s[20:23], s43 offen offset:3072 nt lds\n"
    ".L_nb18_2:\n"
    "s_waitcnt lgkmcnt(0)\n"
    "v_mfma_f32_32x32x16_bf16 v[48:63], a[192:195], a[240:243], v[48:63]\n"
    "s_waitcnt vmcnt(10)\n"
    "s_barrier\n"
    "ds_read_b128 a[224:227], v17 offset:16384\n"
    "v_mfma_f32_32x32x16_bf16 v[96:111], a[196:199], a[240:243], v[96:111]\n"
    "ds_read_b128 a[228:231], v18 offset:16384\n"
    "s_add_u32 m0, s45, 0\n"
    "v_mfma_f32_32x32x16_bf16 v[48:63], a[200:203], a[244:247], v[48:63]\n"
    "ds_read_b128 a[232:235], v17 offset:16640\n"
    "s_add_u32 s43, s42, 0\n"
    "v_mfma_f32_32x32x16_bf16 v[96:111], a[204:207], a[244:247], v[96:111]\n"
    "ds_read_b128 a[236:239], v18 offset:16640\n"
    "v_mfma_f32_32x32x16_bf16 v[48:63], a[208:211], a[248:251], v[48:63]\n"
    "ds_read_b64_tr_b16 a[0:1], v19 offset:16384\n"
    "ds_read_b64_tr_b16 a[2:3], v19 offset:17408\n"
    "buffer_load_dwordx4 v24, s[28:31], s43 offen lds\n"
    "v_mfma_f32_32x32x16_bf16 v[96:111], a[212:215], a[248:251], v[96:111]\n"
    "ds_read_b64_tr_b16 a[4:5], v19 offset:16640\n"
    "ds_read_b64_tr_b16 a[6:7], v19 offset:17664\n"
    "buffer_load_dwordx4 v25, s[28:31], s43 offen offset:1024 lds\n"
    "v_mfma_f32_32x32x16_bf16 v[48:63], a[216:219], a[252:255], v[48:63]\n"
    "ds_read_b64_tr_b16 a[8:9], v19 offset:20480\n"
    "ds_read_b64_tr_b16 a[10:11], v19 offset:21504\n"
    "v_mfma_f32_32x32x16_bf16 v[96:111], a[220:223], a[252:255], v[96:111]\n"
    "ds_read_b64_tr_b16 a[12:13], v19 offset:20736\n"
    "ds_read_b64_tr_b16 a[14:15], v19 offset:21760\n"
    "s_waitcnt lgkmcnt(8)\n"
    "v_mfma_f32_32x32x16_bf16 v[64:79], a[192:195], a[224:227], v[64:79]\n"
    "s_waitcnt vmcnt(10)\n"
    "s_barrier\n"
    "ds_read_b64_tr_b16 a[16:17], v19 offset:24576\n"
    "ds_read_b64_tr_b16 a[18:19], v19 offset:25600\n"
    "v_mfma_f32_32x32x16_bf16 v[112:127], a[196:199], a[224:227], v[112:127]\n"
    "ds_read_b64_tr_b16 a[20:21], v19 offset:24832\n"
    "ds_read_b64_tr_b16 a[22:23], v19 offset:25856\n"
    "ds_read_b128 a[240:243], v17 offset:24576\n"
    "s_add_u32 m0, s45, 8192\n"
    "v_mfma_f32_32x32x16_bf16 v[64:79], a[200:203], a[228:231], v[64:79]\n"
    "ds_read_b64_tr_b16 a[24:25], v19 offset:28672\n"
    "ds_read_b64_tr_b16 a[26:27], v19 offset:29696\n"
    "ds_read_b128 a[244:247], v18 offset:24576\n"
    "s_add_u32 s43, s42, 40960\n"
    "v_mfma_f32_32x32x16_bf16 v[112:127], a[204:207], a[228:231], v[112:127]\n"
    "ds_read_b64_tr_b16 a[28:29], v19 offset:28928\n"
    "ds_read_b64_tr_b16 a[30:31], v19 offset:29952\n"
    "ds_read_b128 a[248:251], v17 offset:24832\n"
    "v_mfma_f32_32x32x16_bf16 v[64:79], a[208:211], a[232:235], v[64:79]\n"
    "ds_read_b128 a[252:255], v18 offset:24832\n"
    "buffer_load_dwordx4 v24, s[28:31], s43 offen lds\n"
    "v_mfma_f32_32x32x16_bf16 v[112:127], a[212:215], a[232:235], v[112:127]\n"
    "buffer_load_dwordx4 v25, s[28:31], s43 offen offset:1024 lds\n"
    "v_mfma_f32_32x32x16_bf16 v[64:79], a[216:219], a[236:239], v[64:79]\n"
    "v_mfma_f32_32x32x16_bf16 v[112:127], a[220:223], a[236:239], v[112:127]\n"
    "s_cmp_eq_u32 s68, 1\n"
    "s_cbranch_scc0 .L_nb20_1\n"
    "s_add_u32 m0, s48, 32768\n"
    "s_add_u32 s43, s48, 16384\n"
    "buffer_load_dwordx4 v9, s[20:23], s43 offen nt lds\n"
    "buffer_load_dwordx4 v10, s[20:23], s43 offen offset:1024 nt lds\n"
    "buffer_load_dwordx4 v11, s[20:23], s43 offen offset:2048 nt lds\n"
    "buffer_load_dwordx4 v12, s[20:23], s43 offen offset:3072 nt lds\n"
    ".L_nb20_1:\n"
    "s_waitcnt lgkmcnt(0)\n"
    "v_mfma_f32_32x32x16_bf16 v[32:47], a[0:3], a[240:243], v[32:47]\n"
    "s_waitcnt vmcnt(10)\n"
    "s_barrier\n"
    "ds_read_b128 a[224:227], v17 offset:32768\n"
    "v_mfma_f32_32x32x16_bf16 v[80:95], a[4:7], a[240:243], v[80:95]\n"
    "ds_read_b128 a[228:231], v18 offset:32768\n"
    "s_add_u32 m0, s45, 16384\n"
    "s_add_u32 s43, s42, 81920\n"
    "buffer_load_dwordx4 v24, s[28:31], s43 offen lds\n"
    "v_mfma_f32_32x32x16_bf16 v[32:47], a[8:11], a[244:247], v[32:47]\n"
    "ds_read_b128 a[232:235], v17 offset:33024\n"
    "buffer_load_dwordx4 v25, s[28:31], s43 offen offset:1024 lds\n"
    "s_add_u32 s42, s42, 128\n"
    "s_sub_u32 s47, s42, 1280\n"
    "v_mfma_f32_32x32x16_bf16 v[80:95], a[12:15], a[244:247], v[80:95]\n"
    "ds_read_b128 a[236:239], v18 offset:33024\n"
    "s_cmp_ge_u32 s42, s46\n"
    "s_cselect_b32 s42, s47, s42\n"
    "s_add_u32 m0, s48, 98304\n"
    "v_mfma_f32_32x32x16_bf16 v[32:47], a[16:19], a[248:251], v[32:47]\n"
    "s_nop 0\n"
    "buffer_load_dwordx4 v13, s[64:67], s44 offen lds\n"
    "buffer_load_dwordx4 v14, s[64:67], s44 offen offset:1024 lds\n"
    "v_mfma_f32_32x32x16_bf16 v[80:95], a[20:23], a[248:251], v[80:95]\n"
    "buffer_load_dwordx4 v15, s[64:67], s44 offen offset:2048 lds\n"
    "buffer_load_dwordx4 v16, s[64:67], s44 offen offset:3072 lds\n"
    "s_add_u32 s44, s44, 524288\n"
    "v_mfma_f32_32x32x16_bf16 v[32:47], a[24:27], a[252:255], v[32:47]\n"
    "s_sub_u32 s47, s44, 5242880\n"
    "s_cmp_ge_u32 s44, s58\n"
    "s_cselect_b32 s44, s47, s44\n"
    "v_mfma_f32_32x32x16_bf16 v[80:95], a[28:31], a[252:255], v[80:95]\n"
    "s_cmp_eq_u32 s68, 3\n"
    "s_cbranch_scc0 .L_nb21_3\n"
    "s_add_u32 m0, s48, 32768\n"
    "s_add_u32 s43, s48, 16384\n"
    "buffer_load_dwordx4 v9, s[20:23], s43 offen nt lds\n"
    "buffer_load_dwordx4 v10, s[20:23], s43 offen offset:1024 nt lds\n"
    "buffer_load_dwordx4 v11, s[20:23], s43 offen offset:2048 nt lds\n"
    "buffer_load_dwordx4 v12, s[20:23], s43 offen offset:3072 nt lds\n"
    ".L_nb21_3:\n"
    "s_waitcnt lgkmcnt(0)\n"
    "v_mfma_f32_32x32x16_bf16 v[48:63], a[0:3], a[224:227], v[48:63]\n"
    "s_waitcnt vmcnt(10)\n"
    "s_barrier\n"
    "ds_read_b128 a[240:243], v17 offset:40960\n"
    "v_mfma_f32_32x32x16_bf16 v[96:111], a[4:7], a[224:227], v[96:111]\n"
    "ds_read_b128 a[244:247], v18 offset:40960\n"
    "s_add_u32 m0, s45, 24576\n"
    "v_mfma_f32_32x32x16_bf16 v[48:63], a[8:11], a[228:231], v[48:63]\n"
    "ds_read_b128 a[248:251], v17 offset:41216\n"
    "s_add_u32 s43, s42, 0\n"
    "v_mfma_f32_32x32x16_bf16 v[96:111], a[12:15], a[228:231], v[96:111]\n"
    "ds_read_b128 a[252:255], v18 offset:41216\n"
    "v_mfma_f32_32x32x16_bf16 v[48:63], a[16:19], a[232:235], v[48:63]\n"
    "ds_read_b64_tr_b16 a[192:193], v19 offset:0\n"
    "ds_read_b64_tr_b16 a[194:195], v19 offset:1024\n"
    "buffer_load_dwordx4 v24, s[28:31], s43 offen lds\n"
    "v_mfma_f32_32x32x16_bf16 v[96:111], a[20:23], a[232:235], v[96:111]\n"
    "ds_read_b64_tr_b16 a[196:197], v19 offset:256\n"
    "ds_read_b64_tr_b16 a[198:199], v19 offset:1280\n"
    "buffer_load_dwordx4 v25, s[28:31], s43 offen offset:1024 lds\n"
    "v_mfma_f32_32x32x16_bf16 v[48:63], a[24:27], a[236:239], v[48:63]\n"
    "ds_read_b64_tr_b16 a[200:201], v19 offset:4096\n"
    "ds_read_b64_tr_b16 a[202:203], v19 offset:5120\n"
    "v_mfma_f32_32x32x16_bf16 v[96:111], a[28:31], a[236:239], v[96:111]\n"
    "ds_read_b64_tr_b16 a[204:205], v19 offset:4352\n"
    "ds_read_b64_tr_b16 a[206:207], v19 offset:5376\n"
    "s_waitcnt lgkmcnt(8)\n"
    "v_mfma_f32_32x32x16_bf16 v[64:79], a[0:3], a[240:243], v[64:79]\n"
    "s_waitcnt vmcnt(10)\n"
    "s_barrier\n"
    "ds_read_b64_tr_b16 a[208:209], v19 offset:8192\n"
    "ds_read_b64_tr_b16 a[210:211], v19 offset:9216\n"
    "v_mfma_f32_32x32x16_bf16 v[112:127], a[4:7], a[240:243], v[112:127]\n"
    "ds_read_b64_tr_b16 a[212:213], v19 offset:8448\n"
    "ds_read_b64_tr_b16 a[214:215], v19 offset:9472\n"
    "ds_read_b128 a[224:227], v17 offset:0\n"
    "s_add_u32 m0, s45, 32768\n"
    "v_mfma_f32_32x32x16_bf16 v[64:79], a[8:11], a[244:247], v[64:79]\n"
    "ds_read_b64_tr_b16 a[216:217], v19 offset:12288\n"
    "ds_read_b64_tr_b16 a[218:219], v19 offset:13312\n"
    "ds_read_b128 a[228:231], v18 offset:0\n"
    "s_add_u32 s43, s42, 40960\n"
    "v_mfma_f32_32x32x16_bf16 v[112:127], a[12:15], a[244:247], v[112:127]\n"
    "ds_read_b64_tr_b16 a[220:221], v19 offset:12544\n"
    "ds_read_b64_tr_b16 a[222:223], v19 offset:13568\n"
    "ds_read_b128 a[232:235], v17 offset:256\n"
    "v_mfma_f32_32x32x16_bf16 v[64:79], a[16:19], a[248:251], v[64:79]\n"
    "ds_read_b128 a[236:239], v18 offset:256\n"
    "buffer_load_dwordx4 v24, s[28:31], s43 offen lds\n"
    "v_mfma_f32_32x32x16_bf16 v[112:127], a[20:23], a[248:251], v[112:127]\n"
    "buffer_load_dwordx4 v25, s[28:31], s43 offen offset:1024 lds\n"
    "v_mfma_f32_32x32x16_bf16 v[64:79], a[24:27], a[252:255], v[64:79]\n"
    "v_mfma_f32_32x32x16_bf16 v[112:127], a[28:31], a[252:255], v[112:127]\n"
    "s_waitcnt lgkmcnt(0)\n"
    "v_mfma_f32_32x32x16_bf16 v[32:47], a[192:195], a[224:227], v[32:47]\n"
    "s_waitcnt vmcnt(10)\n"
    "s_barrier\n"
    "ds_read_b128 a[240:243], v17 offset:8192\n"
    "v_mfma_f32_32x32x16_bf16 v[80:95], a[196:199], a[224:227], v[80:95]\n"
    "ds_read_b128 a[244:247], v18 offset:8192\n"
    "s_add_u32 m0, s45, 40960\n"
    "s_add_u32 s43, s42, 81920\n"
    "buffer_load_dwordx4 v24, s[28:31], s43 offen lds\n"
    "v_mfma_f32_32x32x16_bf16 v[32:47], a[200:203], a[228:231], v[32:47]\n"
    "ds_read_b128 a[248:251], v17 offset:8448\n"
    "buffer_load_dwordx4 v25, s[28:31], s43 offen offset:1024 lds\n"
    "s_add_u32 s42, s42, 128\n"
    "v_mfma_f32_32x32x16_bf16 v[80:95], a[204:207], a[228:231], v[80:95]\n"
    "ds_read_b128 a[252:255], v18 offset:8448\n"
    "s_sub_u32 s47, s42, 1280\n"
    "s_cmp_ge_u32 s42, s46\n"
    "s_cselect_b32 s42, s47, s42\n"
    "v_mfma_f32_32x32x16_bf16 v[32:47], a[208:211], a[232:235], v[32:47]\n"
    "s_add_u32 m0, s48, 49152\n"
    "s_add_u32 s43, s48, 16384\n"
    "v_mfma_f32_32x32x16_bf16 v[80:95], a[212:215], a[232:235], v[80:95]\n"
    "buffer_load_dwordx4 v9, s[24:27], s43 offen nt lds\n"
    "buffer_load_dwordx4 v9, s[24:27], s43 offen offset:1024 nt lds\n"
    "v_mfma_f32_32x32x16_bf16 v[32:47], a[216:219], a[236:239], v[32:47]\n"
    "buffer_load_dwordx4 v9, s[24:27], s43 offen offset:2048 nt lds\n"
    "buffer_load_dwordx4 v9, s[24:27], s43 offen offset:3072 nt lds\n"
    "v_mfma_f32_32x32x16_bf16 v[80:95], a[220:223], a[236:239], v[80:95]\n"
    "s_waitcnt lgkmcnt(0)\n"
    "v_mfma_f32_32x32x16_bf16 v[48:63], a[192:195], a[240:243], v[48:63]\n"
    "s_waitcnt vmcnt(10)\n"
    "s_barrier\n"
    "ds_read_b128 a[224:227], v17 offset:16384\n"
    "v_mfma_f32_32x32x16_bf16 v[96:111], a[196:199], a[240:243], v[96:111]\n"
    "ds_read_b128 a[228:231], v18 offset:16384\n"
    "s_add_u32 m0, s48, 65536\n"
    "v_mfma_f32_32x32x16_bf16 v[48:63], a[200:203], a[244:247], v[48:63]\n"
    "ds_read_b128 a[232:235], v17 offset:16640\n"
    "s_add_u32 s43, s48, 32768\n"
    "v_mfma_f32_32x32x16_bf16 v[96:111], a[204:207], a[244:247], v[96:111]\n"
    "ds_read_b128 a[236:239], v18 offset:16640\n"
    "buffer_load_dwordx4 v9, s[20:23], s43 offen nt lds\n"
    "v_mfma_f32_32x32x16_bf16 v[48:63], a[208:211], a[248:251], v[48:63]\n"
    "ds_read_b64_tr_b16 a[0:1], v19 offset:16384\n"
    "ds_read_b64_tr_b16 a[2:3], v19 offset:17408\n"
    "buffer_load_dwordx4 v10, s[20:23], s43 offen offset:1024 nt lds\n"
    "v_mfma_f32_32x32x16_bf16 v[96:111], a[212:215], a[248:251], v[96:111]\n"
    "ds_read_b64_tr_b16 a[4:5], v19 offset:16640\n"
    "ds_read_b64_tr_b16 a[6:7], v19 offset:17664\n"
    "buffer_load_dwordx4 v11, s[20:23], s43 offen offset:2048 nt lds\n"
    "v_mfma_f32_32x32x16_bf16 v[48:63], a[216:219], a[252:255], v[48:63]\n"
    "ds_read_b64_tr_b16 a[8:9], v19 offset:20480\n"
    "ds_read_b64_tr_b16 a[10:11], v19 offset:21504\n"
    "buffer_load_dwordx4 v12, s[20:23], s43 offen offset:3072 nt lds\n"
    "v_mfma_f32_32x32x16_bf16 v[96:111], a[220:223], a[252:255], v[96:111]\n"
    "ds_read_b64_tr_b16 a[12:13], v19 offset:20736\n"
    "ds_read_b64_tr_b16 a[14:15], v19 offset:21760\n"
    "s_waitcnt lgkmcnt(8)\n"
    "v_mfma_f32_32x32x16_bf16 v[64:79], a[192:195], a[224:227], v[64:79]\n"
    "s_waitcnt vmcnt(12)\n"
    "s_barrier\n"
    "ds_read_b64_tr_b16 a[16:17], v19 offset:24576\n"
    "ds_read_b64_tr_b16 a[18:19], v19 offset:25600\n"
    "v_mfma_f32_32x32x16_bf16 v[112:127], a[196:199], a[224:227], v[112:127]\n"
    "ds_read_b64_tr_b16 a[20:21], v19 offset:24832\n"
    "ds_read_b64_tr_b16 a[22:23], v19 offset:25856\n"
    "ds_read_b128 a[240:243], v17 offset:24576\n"
    "v_mfma_f32_32x32x16_bf16 v[64:79], a[200:203], a[228:231], v[64:79]\n"
    "ds_read_b64_tr_b16 a[24:25], v19 offset:28672\n"
    "ds_read_b64_tr_b16 a[26:27], v19 offset:29696\n"
    "ds_read_b128 a[244:247], v18 offset:24576\n"
    "v_mfma_f32_32x32x16_bf16 v[112:127], a[204:207], a[228:231], v[112:127]\n"
    "ds_read_b64_tr_b16 a[28:29], v19 offset:28928\n"
    "ds_read_b64_tr_b16 a[30:31], v19 offset:29952\n"
    "ds_read_b128 a[248:251], v17 offset:24832\n"
    "v_mfma_f32_32x32x16_bf16 v[64:79], a[208:211], a[232:235], v[64:79]\n"
    "ds_read_b128 a[252:255], v18 offset:24832\n"
    "v_mfma_f32_32x32x16_bf16 v[112:127], a[212:215], a[232:235], v[112:127]\n"
    "v_mfma_f32_32x32x16_bf16 v[64:79], a[216:219], a[236:239], v[64:79]\n"
    "v_mfma_f32_32x32x16_bf16 v[112:127], a[220:223], a[236:239], v[112:127]\n"
    "s_waitcnt lgkmcnt(0)\n"
    "v_mfma_f32_32x32x16_bf16 v[32:47], a[0:3], a[240:243], v[32:47]\n"
    "s_waitcnt vmcnt(10)\n"
    "s_barrier\n"
    "ds_read_b128 a[224:227], v17 offset:32768\n"
    "v_mfma_f32_32x32x16_bf16 v[80:95], a[4:7], a[240:243], v[80:95]\n"
    "ds_read_b128 a[228:231], v18 offset:32768\n"
    "v_mfma_f32_32x32x16_bf16 v[32:47], a[8:11], a[244:247], v[32:47]\n"
    "ds_read_b128 a[232:235], v17 offset:33024\n"
    "v_mfma_f32_32x32x16_bf16 v[80:95], a[12:15], a[244:247], v[80:95]\n"
    "ds_read_b128 a[236:239], v18 offset:33024\n"
    "v_mfma_f32_32x32x16_bf16 v[32:47], a[16:19], a[248:251], v[32:47]\n"
    "ds_read_b128 a[192:195], v5 offset:0\n"
    "ds_read_b128 a[196:199], v6 offset:0\n"
    "v_mfma_f32_32x32x16_bf16 v[80:95], a[20:23], a[248:251], v[80:95]\n"
    "ds_read_b128 a[200:203], v5 offset:256\n"
    "ds_read_b128 a[204:207], v6 offset:256\n"
    "v_mfma_f32_32x32x16_bf16 v[32:47], a[24:27], a[252:255], v[32:47]\n"
    "ds_read_b128 a[208:211], v5 offset:512\n"
    "ds_read_b128 a[212:215], v6 offset:512\n"
    "v_mfma_f32_32x32x16_bf16 v[80:95], a[28:31], a[252:255], v[80:95]\n"
    "ds_read_b128 a[216:219], v5 offset:768\n"
    "ds_read_b128 a[220:223], v6 offset:768\n"
    "s_waitcnt lgkmcnt(0)\n"
    "v_mfma_f32_32x32x16_bf16 v[48:63], a[0:3], a[224:227], v[48:63]\n"
    "s_waitcnt vmcnt(8)\n"
    "s_barrier\n"
    "ds_read_b128 a[240:243], v17 offset:40960\n"
    "v_mfma_f32_32x32x16_bf16 v[96:111], a[4:7], a[224:227], v[96:111]\n"
    "ds_read_b128 a[244:247], v18 offset:40960\n"
    "v_mul_f32 v32, s41, v32\n"
    "v_mul_f32 v33, s41, v33\n"
    "v_mul_f32 v34, s41, v34\n"
    "v_mul_f32 v35, s41, v35\n"
    "v_mul_f32 v36, s41, v36\n"
    "v_mul_f32 v37, s41, v37\n"
    "v_mul_f32 v38, s41, v38\n"
    "v_mul_f32 v39, s41, v39\n"
    "v_mfma_f32_32x32x16_bf16 v[48:63], a[8:11], a[228:231], v[48:63]\n"
    "ds_read_b128 a[248:251], v17 offset:41216\n"
    "v_cvt_pk_bf16_f32 v224, v32, v33\n"
    "v_cvt_pk_bf16_f32 v225, v34, v35\n"
    "v_cvt_pk_bf16_f32 v226, v36, v37\n"
    "v_cvt_pk_bf16_f32 v227, v38, v39\n"
    "ds_write_b128 v31, v[224:227] offset:0\n"
    "v_mul_f32 v40, s41, v40\n"
    "v_mul_f32 v41, s41, v41\n"
    "v_mfma_f32_32x32x16_bf16 v[96:111], a[12:15], a[228:231], v[96:111]\n"
    "ds_read_b128 a[252:255], v18 offset:41216\n"
    "v_mul_f32 v42, s41, v42\n"
    "v_mul_f32 v43, s41, v43\n"
    "v_mul_f32 v44, s41, v44\n"
    "v_mul_f32 v45, s41, v45\n"
    "v_mul_f32 v46, s41, v46\n"
    "v_mul_f32 v47, s41, v47\n"
    "v_cvt_pk_bf16_f32 v228, v40, v41\n"
    "v_cvt_pk_bf16_f32 v229, v42, v43\n"
    "v_mfma_f32_32x32x16_bf16 v[48:63], a[16:19], a[232:235], v[48:63]\n"
    "v_cvt_pk_bf16_f32 v230, v44, v45\n"
    "v_cvt_pk_bf16_f32 v231, v46, v47\n"
    "ds_write_b128 v31, v[228:231] offset:1024\n"
    "v_mul_f32 v80, s41, v80\n"
    "v_mul_f32 v81, s41, v81\n"
    "v_mul_f32 v82, s41, v82\n"
    "v_mul_f32 v83, s41, v83\n"
    "v_mfma_f32_32x32x16_bf16 v[96:111], a[20:23], a[232:235], v[96:111]\n"
    "v_mul_f32 v84, s41, v84\n"
    "v_mul_f32 v85, s41, v85\n"
    "v_mul_f32 v86, s41, v86\n"
    "v_mul_f32 v87, s41, v87\n"
    "v_cvt_pk_bf16_f32 v232, v80, v81\n"
    "v_cvt_pk_bf16_f32 v233, v82, v83\n"
    "v_cvt_pk_bf16_f32 v234, v84, v85\n"
    "v_cvt_pk_bf16_f32 v235, v86, v87\n"
    "v_mfma_f32_32x32x16_bf16 v[48:63], a[24:27], a[236:239], v[48:63]\n"
    "ds_write_b128 v31, v[232:235] offset:2048\n"
    "v_mul_f32 v88, s41, v88\n"
    "v_mul_f32 v89, s41, v89\n"
    "v_mul_f32 v90, s41, v90\n"
    "v_mul_f32 v91, s41, v91\n"
    "v_mul_f32 v92, s41, v92\n"
    "v_mul_f32 v93, s41, v93\n"
    "v_mfma_f32_32x32x16_bf16 v[96:111], a[28:31], a[236:239], v[96:111]\n"
    "v_mul_f32 v94, s41, v94\n"
    "v_mul_f32 v95, s41, v95\n"
    "v_cvt_pk_bf16_f32 v236, v88, v89\n"
    "v_cvt_pk_bf16_f32 v237, v90, v91\n"
    "v_cvt_pk_bf16_f32 v238, v92, v93\n"
    "v_cvt_pk_bf16_f32 v239, v94, v95\n"
    "ds_write_b128 v31, v[236:239] offset:3072\n"
    "s_waitcnt lgkmcnt(0)\n"
    "v_mfma_f32_32x32x16_bf16 v[64:79], a[0:3], a[240:243], v[64:79]\n"
    "s_barrier\n"
    "ds_read_b128 a[96:99], v20 offset:0\n"
    "ds_read_b128 a[100:103], v20 offset:1024\n"
    "v_mfma_f32_32x32x16_bf16 v[112:127], a[4:7], a[240:243], v[112:127]\n"
    "ds_read_b128 a[104:107], v20 offset:2048\n"
    "ds_read_b128 a[108:111], v20 offset:3072\n"
    "v_mul_f32 v48, s41, v48\n"
    "v_mul_f32 v49, s41, v49\n"
    "v_mul_f32 v50, s41, v50\n"
    "v_mul_f32 v51, s41, v51\n"
    "v_mul_f32 v52, s41, v52\n"
    "v_mul_f32 v53, s41, v53\n"
    "v_mul_f32 v54, s41, v54\n"
    "v_mul_f32 v55, s41, v55\n"
    "v_mfma_f32_32x32x16_bf16 v[64:79], a[8:11], a[244:247], v[64:79]\n"
    "ds_read_b128 a[112:115], v20 offset:4096\n"
    "ds_read_b128 a[116:119], v20 offset:5120\n"
    "v_cvt_pk_bf16_f32 v224, v48, v49\n"
    "v_cvt_pk_bf16_f32 v225, v50, v51\n"
    "v_cvt_pk_bf16_f32 v226, v52, v53\n"
    "v_cvt_pk_bf16_f32 v227, v54, v55\n"
    "ds_write_b128 v31, v[224:227] offset:16384\n"
    "v_mul_f32 v56, s41, v56\n"
    "v_mul_f32 v57, s41, v57\n"
    "v_mfma_f32_32x32x16_bf16 v[112:127], a[12:15], a[244:247], v[112:127]\n"
    "ds_read_b128 a[120:123], v20 offset:6144\n"
    "ds_read_b128 a[124:127], v20 offset:7168\n"
    "v_mul_f32 v58, s41, v58\n"
    "v_mul_f32 v59, s41, v59\n"
    "v_mul_f32 v60, s41, v60\n"
    "v_mul_f32 v61, s41, v61\n"
    "v_mul_f32 v62, s41, v62\n"
    "v_mul_f32 v63, s41, v63\n"
    "v_cvt_pk_bf16_f32 v228, v56, v57\n"
    "v_cvt_pk_bf16_f32 v229, v58, v59\n"
    "v_mfma_f32_32x32x16_bf16 v[64:79], a[16:19], a[248:251], v[64:79]\n"
    "v_cvt_pk_bf16_f32 v230, v60, v61\n"
    "v_cvt_pk_bf16_f32 v231, v62, v63\n"
    "ds_write_b128 v31, v[228:231] offset:17408\n"
    "v_mul_f32 v96, s41, v96\n"
    "v_mul_f32 v97, s41, v97\n"
    "v_mul_f32 v98, s41, v98\n"
    "v_mul_f32 v99, s41, v99\n"
    "v_mfma_f32_32x32x16_bf16 v[112:127], a[20:23], a[248:251], v[112:127]\n"
    "v_mul_f32 v100, s41, v100\n"
    "v_mul_f32 v101, s41, v101\n"
    "v_mul_f32 v102, s41, v102\n"
    "v_mul_f32 v103, s41, v103\n"
    "v_cvt_pk_bf16_f32 v232, v96, v97\n"
    "v_cvt_pk_bf16_f32 v233, v98, v99\n"
    "v_cvt_pk_bf16_f32 v234, v100, v101\n"
    "v_cvt_pk_bf16_f32 v235, v102, v103\n"
    "v_mfma_f32_32x32x16_bf16 v[64:79], a[24:27], a[252:255], v[64:79]\n"
    "ds_write_b128 v31, v[232:235] offset:18432\n"
    "v_mul_f32 v104, s41, v104\n"
    "v_mul_f32 v105, s41, v105\n"
    "v_mul_f32 v106, s41, v106\n"
    "v_mul_f32 v107, s41, v107\n"
    "v_mul_f32 v108, s41, v108\n"
    "v_mul_f32 v109, s41, v109\n"
    "v_mfma_f32_32x32x16_bf16 v[112:127], a[28:31], a[252:255], v[112:127]\n"
    "v_mul_f32 v110, s41, v110\n"
    "v_mul_f32 v111, s41, v111\n"
    "v_cvt_pk_bf16_f32 v236, v104, v105\n"
    "v_cvt_pk_bf16_f32 v237, v106, v107\n"
    "v_cvt_pk_bf16_f32 v238, v108, v109\n"
    "v_cvt_pk_bf16_f32 v239, v110, v111\n"
    "ds_write_b128 v31, v[236:239] offset:19456\n"
    "s_waitcnt lgkmcnt(0)\n"
    "v_mfma_f32_32x32x16_bf16 v[176:191], a[192:195], a[96:99], v[128:143]\n"
    "s_barrier\n"
    "ds_read_b128 a[128:131], v20 offset:16384\n"
    "ds_read_b128 a[132:135], v20 offset:17408\n"
    "v_mfma_f32_32x32x16_bf16 v[176:191], a[196:199], a[100:103], v[176:191]\n"
    "ds_read_b128 a[136:139], v20 offset:18432\n"
    "ds_read_b128 a[140:143], v20 offset:19456\n"
    "v_mul_f32 v64, s41, v64\n"
    "v_mul_f32 v65, s41, v65\n"
    "v_mul_f32 v66, s41, v66\n"
    "v_mul_f32 v67, s41, v67\n"
    "v_mul_f32 v68, s41, v68\n"
    "v_mul_f32 v69, s41, v69\n"
    "v_mul_f32 v70, s41, v70\n"
    "v_mul_f32 v71, s41, v71\n"
    "v_mfma_f32_32x32x16_bf16 v[176:191], a[200:203], a[104:107], v[176:191]\n"
    "ds_read_b128 a[144:147], v20 offset:20480\n"
    "ds_read_b128 a[148:151], v20 offset:21504\n"
    "v_cvt_pk_bf16_f32 v224, v64, v65\n"
    "v_cvt_pk_bf16_f32 v225, v66, v67\n"
    "v_cvt_pk_bf16_f32 v226, v68, v69\n"
    "v_cvt_pk_bf16_f32 v227, v70, v71\n"
    "ds_write_b128 v31, v[224:227] offset:32768\n"
    "v_mul_f32 v72, s41, v72\n"
    "v_mul_f32 v73, s41, v73\n"
    "v_mfma_f32_32x32x16_bf16 v[176:191], a[204:207], a[108:111], v[176:191]\n"
    "ds_read_b128 a[152:155], v20 offset:22528\n"
    "ds_read_b128 a[156:159], v20 offset:23552\n"
    "v_mul_f32 v74, s41, v74\n"
    "v_mul_f32 v75, s41, v75\n"
    "v_mul_f32 v76, s41, v76\n"
    "v_mul_f32 v77, s41, v77\n"
    "v_mul_f32 v78, s41, v78\n"
    "v_mul_f32 v79, s41, v79\n"
    "v_cvt_pk_bf16_f32 v228, v72, v73\n"
    "v_cvt_pk_bf16_f32 v229, v74, v75\n"
    "s_add_u32 m0, s48, 81920\n"
    "s_add_u32 s43, s48, 32768\n"
    "buffer_load_dwordx4 v9, s[24:27], s43 offen nt lds\n"
    "v_mfma_f32_32x32x16_bf16 v[176:191], a[208:211], a[112:115], v[176:191]\n"
    "v_cvt_pk_bf16_f32 v230, v76, v77\n"
    "v_cvt_pk_bf16_f32 v231, v78, v79\n"
    "ds_write_b128 v31, v[228:231] offset:33792\n"
    "v_mul_f32 v112, s41, v112\n"
    "v_mul_f32 v113, s41, v113\n"
    "v_mul_f32 v114, s41, v114\n"
    "v_mul_f32 v115, s41, v115\n"
    "v_mfma_f32_32x32x16_bf16 v[176:191], a[212:215], a[116:119], v[176:191]\n"
    "v_mul_f32 v116, s41, v116\n"
    "v_mul_f32 v117, s41, v117\n"
    "v_mul_f32 v118, s41, v118\n"
    "v_mul_f32 v119, s41, v119\n"
    "v_cvt_pk_bf16_f32 v232, v112, v113\n"
    "v_cvt_pk_bf16_f32 v233, v114, v115\n"
    "v_cvt_pk_bf16_f32 v234, v116, v117\n"
    "v_cvt_pk_bf16_f32 v235, v118, v119\n"
    "v_mfma_f32_32x32x16_bf16 v[176:191], a[216:219], a[120:123], v[176:191]\n"
    "ds_write_b128 v31, v[232:235] offset:34816\n"
    "v_mul_f32 v120, s41, v120\n"
    "v_mul_f32 v121, s41, v121\n"
    "v_mul_f32 v122, s41, v122\n"
    "v_mul_f32 v123, s41, v123\n"
    "v_mul_f32 v124, s41, v124\n"
    "v_mul_f32 v125, s41, v125\n"
    "v_mfma_f32_32x32x16_bf16 v[176:191], a[220:223], a[124:127], v[176:191]\n"
    "v_mul_f32 v126, s41, v126\n"
    "v_mul_f32 v127, s41, v127\n"
    "v_cvt_pk_bf16_f32 v236, v120, v121\n"
    "v_cvt_pk_bf16_f32 v237, v122, v123\n"
    "v_cvt_pk_bf16_f32 v238, v124, v125\n"
    "v_cvt_pk_bf16_f32 v239, v126, v127\n"
    "ds_write_b128 v31, v[236:239] offset:35840\n"
    "s_waitcnt lgkmcnt(0)\n"
    "v_mfma_f32_32x32x16_bf16 v[192:207], a[192:195], a[128:131], v[144:159]\n"
    "s_waitcnt vmcnt(15)\n"
    "s_barrier\n"
    "ds_read_b128 a[160:163], v20 offset:32768\n"
    "ds_read_b128 a[164:167], v20 offset:33792\n"
    "v_mfma_f32_32x32x16_bf16 v[192:207], a[196:199], a[132:135], v[192:207]\n"
    "v_exp_f32 v248, v176\n"
    "v_exp_f32 v249, v177\n"
    "v_exp_f32 v250, v178\n"
    "ds_read_b128 a[168:171], v20 offset:34816\n"
    "ds_read_b128 a[172:175], v20 offset:35840\n"
    "buffer_load_dwordx4 v9, s[24:27], s43 offen offset:1024 nt lds\n"
    "v_mfma_f32_32x32x16_bf16 v[192:207], a[200:203], a[136:139], v[192:207]\n"
    "v_add_f32 v24, v248, v249\n"
    "v_cvt_pk_bf16_f32 v224, v248, v249\n"
    "v_exp_f32 v251, v179\n"
    "ds_read_b128 a[176:179], v20 offset:36864\n"
    "ds_read_b128 a[180:183], v20 offset:37888\n"
    "v_mfma_f32_32x32x16_bf16 v[192:207], a[204:207], a[140:143], v[192:207]\n"
    "v_add_f32 v24, v24, v250\n"
    "v_exp_f32 v248, v180\n"
    "ds_read_b128 a[184:187], v20 offset:38912\n"
    "ds_read_b128 a[188:191], v20 offset:39936\n"
    "v_mfma_f32_32x32x16_bf16 v[192:207], a[208:211], a[144:147], v[192:207]\n"
    "v_add_f32 v24, v24, v251\n"
    "v_cvt_pk_bf16_f32 v225, v250, v251\n"
    "v_exp_f32 v249, v181\n"
    "ds_read_b64_tr_b16 a[224:225], v7 offset:0\n"
    "ds_read_b64_tr_b16 a[228:229], v7 offset:256\n"
    "v_mfma_f32_32x32x16_bf16 v[192:207], a[212:215], a[148:151], v[192:207]\n"
    "v_add_f32 v24, v24, v248\n"
    "v_exp_f32 v250, v182\n"
    "ds_read_b64_tr_b16 a[232:233], v8 offset:0\n"
    "ds_read_b64_tr_b16 a[236:237], v8 offset:256\n"
    "v_mfma_f32_32x32x16_bf16 v[192:207], a[216:219], a[152:155], v[192:207]\n"
    "v_add_f32 v24, v24, v249\n"
    "v_cvt_pk_bf16_f32 v226, v248, v249\n"
    "v_exp_f32 v251, v183\n"
    "ds_read_b64_tr_b16 a[240:241], v7 offset:4096\n"
    "ds_read_b64_tr_b16 a[244:245], v7 offset:4352\n"
    "v_mfma_f32_32x32x16_bf16 v[192:207], a[220:223], a[156:159], v[192:207]\n"
    "v_add_f32 v24, v24, v250\n"
    "v_exp_f32 v248, v184\n"
    "ds_read_b64_tr_b16 a[248:249], v8 offset:4096\n"
    "ds_read_b64_tr_b16 a[252:253], v8 offset:4352\n"
    "s_waitcnt lgkmcnt(8)\n"
    "v_mfma_f32_32x32x16_bf16 v[208:223], a[192:195], a[160:163], v[160:175]\n"
    "v_add_f32 v24, v24, v251\n"
    "v_cvt_pk_bf16_f32 v227, v250, v251\n"
    "ds_read_b64_tr_b16 a[226:227], v7 offset:2048\n"
    "buffer_load_dwordx4 v9, s[24:27], s43 offen offset:2048 nt lds\n"
    "v_mfma_f32_32x32x16_bf16 v[208:223], a[196:199], a[164:167], v[208:223]\n"
    "v_exp_f32 v249, v185\n"
    "v_add_f32 v24, v24, v248\n"
    "v_exp_f32 v250, v186\n"
    "v_add_f32 v24, v24, v249\n"
    "ds_read_b64_tr_b16 a[230:231], v7 offset:2304\n"
    "v_mfma_f32_32x32x16_bf16 v[208:223], a[200:203], a[168:171], v[208:223]\n"
    "v_cvt_pk_bf16_f32 v228, v248, v249\n"
    "v_exp_f32 v251, v187\n"
    "v_add_f32 v24, v24, v250\n"
    "v_exp_f32 v248, v188\n"
    "ds_read_b64_tr_b16 a[234:235], v8 offset:2048\n"
    "v_mfma_f32_32x32x16_bf16 v[208:223], a[204:207], a[172:175], v[208:223]\n"
    "v_add_f32 v24, v24, v251\n"
    "v_cvt_pk_bf16_f32 v229, v250, v251\n"
    "v_exp_f32 v249, v189\n"
    "v_add_f32 v24, v24, v248\n"
    "ds_read_b64_tr_b16 a[238:239], v8 offset:2304\n"
    "v_mfma_f32_32x32x16_bf16 v[208:223], a[208:211], a[176:179], v[208:223]\n"
    "v_exp_f32 v250, v190\n"
    "v_add_f32 v24, v24, v249\n"
    "v_cvt_pk_bf16_f32 v230, v248, v249\n"
    "v_exp_f32 v251, v191\n"
    "ds_read_b64_tr_b16 a[242:243], v7 offset:6144\n"
    "v_mfma_f32_32x32x16_bf16 v[208:223], a[212:215], a[180:183], v[208:223]\n"
    "v_add_f32 v24, v24, v250\n"
    "v_add_f32 v24, v24, v251\n"
    "v_cvt_pk_bf16_f32 v231, v250, v251\n"
    "v_exp_f32 v248, v192\n"
    "v_med3_f32 v252, v24, s49, v26\n"
    "ds_read_b64_tr_b16 a[246:247], v7 offset:6400\n"
    "v_mfma_f32_32x32x16_bf16 v[208:223], a[216:219], a[184:187], v[208:223]\n"
    "v_cmp_neq_f32 vcc, v252, v24\n"
    "v_exp_f32 v249, v193\n"
    "ds_read_b64_tr_b16 a[250:251], v8 offset:6144\n"
    "buffer_load_dwordx4 v9, s[24:27], s43 offen offset:3072 nt lds\n"
    "v_mfma_f32_32x32x16_bf16 v[208:223], a[220:223], a[188:191], v[208:223]\n"
    "s_cbranch_vccnz .L_stub0\n"
    ".L_ret0:\n"
    "v_add_f32 v21, v21, v24\n"
    "ds_read_b64_tr_b16 a[254:255], v8 offset:6400\n"
    "v_add_u32 v5, 32768, v5\n"
    "v_add_u32 v6, 32768, v6\n"
    "s_waitcnt lgkmcnt(4)\n"
    "v_mfma_f32_32x32x16_bf16 v[32:47], a[224:227], v[224:227], 0\n"
    "v_exp_f32 v250, v194\n"
    "v_add_f32 v25, v248, v249\n"
    "v_cvt_pk_bf16_f32 v232, v248, v249\n"
    "ds_read_b128 a[192:195], v5 offset:0\n"
    "v_mfma_f32_32x32x16_bf16 v[80:95], a[228:231], v[224:227], 0\n"
    "v_exp_f32 v251, v195\n"
    "v_add_f32 v25, v25, v250\n"
    "ds_read_b128 a[196:199], v6 offset:0\n"
    "v_mfma_f32_32x32x16_bf16 a[0:15], a[232:235], v[224:227], 0\n"
    "v_exp_f32 v248, v196\n"
    "v_add_f32 v25, v25, v251\n"
    "v_cvt_pk_bf16_f32 v233, v250, v251\n"
    "ds_read_b128 a[200:203], v5 offset:256\n"
    "v_mfma_f32_32x32x16_bf16 a[48:63], a[236:239], v[224:227], 0\n"
    "v_exp_f32 v249, v197\n"
    "v_add_f32 v25, v25, v248\n"
    "ds_read_b128 a[204:207], v6 offset:256\n"
    "s_waitcnt lgkmcnt(4)\n"
    "v_mfma_f32_32x32x16_bf16 v[32:47], a[240:243], v[228:231], v[32:47]\n"
    "v_exp_f32 v250, v198\n"
    "v_add_f32 v25, v25, v249\n"
    "v_cvt_pk_bf16_f32 v234, v248, v249\n"
    "ds_read_b128 a[208:211], v5 offset:512\n"
    "v_mfma_f32_32x32x16_bf16 v[80:95], a[244:247], v[228:231], v[80:95]\n"
    "ds_read_b128 a[212:215], v6 offset:512\n"
    "s_add_u32 m0, s48, 98304\n"
    "s_add_u32 s43, s48, 49152\n"
    "buffer_load_dwordx4 v9, s[20:23], s43 offen nt lds\n"
    "v_mfma_f32_32x32x16_bf16 a[0:15], a[248:251], v[228:231], a[0:15]\n"
    "v_exp_f32 v251, v199\n"
    "v_add_f32 v25, v25, v250\n"
    "ds_read_b128 a[216:219], v5 offset:768\n"
    "v_mfma_f32_32x32x16_bf16 a[48:63], a[252:255], v[228:231], a[48:63]\n"
    "v_exp_f32 v248, v200\n"
    "v_add_f32 v25, v25, v251\n"
    "ds_read_b128 a[220:223], v6 offset:768\n"
    "s_waitcnt lgkmcnt(4)\n"
    "v_mfma_f32_32x32x16_bf16 v[176:191], a[192:195], a[96:99], v[128:143]\n"
    "v_cvt_pk_bf16_f32 v235, v250, v251\n"
    "v_exp_f32 v249, v201\n"
    "v_add_f32 v25, v25, v248\n"
    "s_add_u32 s76, %[op_lo], 33554432\n"
    "s_addc_u32 s77, %[op_hi], 0\n"
    "v_mfma_f32_32x32x16_bf16 v[176:191], a[196:199], a[100:103], v[176:191]\n"
    "v_exp_f32 v250, v202\n"
    "v_add_f32 v25, v25, v249\n"
    "v_cvt_pk_bf16_f32 v236, v248, v249\n"
    "s_and_b32 s77, s77, 0xffff\n"
    "v_mfma_f32_32x32x16_bf16 v[176:191], a[200:203], a[104:107], v[176:191]\n"
    "v_exp_f32 v251, v203\n"
    "v_add_f32 v25, v25, v250\n"
    "v_exp_f32 v248, v204\n"
    "s_mov_b32 s78, 133120\n"
    "v_mfma_f32_32x32x16_bf16 v[176:191], a[204:207], a[108:111], v[176:191]\n"
    "v_add_f32 v25, v25, v251\n"
    "v_cvt_pk_bf16_f32 v237, v250, v251\n"
    "v_exp_f32 v249, v205\n"
    "s_mov_b32 s79, 0x20000\n"
    "s_waitcnt lgkmcnt(0)\n"
    "v_mfma_f32_32x32x16_bf16 v[176:191], a[208:211], a[112:115], v[176:191]\n"
    "v_add_f32 v25, v25, v248\n"
    "buffer_load_dwordx4 v10, s[20:23], s43 offen offset:1024 nt lds\n"
    "s_and_b32 s75, %[bid], 7\n"
    "v_mfma_f32_32x32x16_bf16 v[176:191], a[212:215], a[116:119], v[176:191]\n"
    "v_exp_f32 v250, v206\n"
    "v_add_f32 v25, v25, v249\n"
    "v_cvt_pk_bf16_f32 v238, v248, v249\n"
    "s_lshl_b32 s75, s75, 8\n"
    "v_mfma_f32_32x32x16_bf16 v[176:191], a[216:219], a[120:123], v[176:191]\n"
    "v_exp_f32 v251, v207\n"
    "v_add_f32 v25, v25, v250\n"
    "v_add_f32 v25, v25, v251\n"
    "v_cvt_pk_bf16_f32 v239, v250, v251\n"
    "v_exp_f32 v248, v208\n"
    "s_and_b32 s74, %[bid], 0xf8\n"
    "v_mfma_f32_32x32x16_bf16 v[176:191], a[220:223], a[124:127], v[176:191]\n"
    "v_med3_f32 v252, v25, s49, v26\n"
    "v_cmp_neq_f32 vcc, v252, v25\n"
    "v_exp_f32 v249, v209\n"
    "s_cbranch_vccnz .L_stub1\n"
    ".L_ret1:\n"
    "v_add_f32 v22, v22, v25\n"
    "s_add_u32 s75, s75, s74\n"
    "v_mfma_f32_32x32x16_bf16 v[48:63], a[224:227], v[232:235], 0\n"
    "v_exp_f32 v250, v210\n"
    "v_add_f32 v24, v248, v249\n"
    "v_cvt_pk_bf16_f32 v240, v248, v249\n"
    "s_add_u32 s75, s75, 131072\n"
    "v_mfma_f32_32x32x16_bf16 v[96:111], a[228:231], v[232:235], 0\n"
    "v_exp_f32 v251, v211\n"
    "v_add_f32 v24, v24, v250\n"
    "v_exp_f32 v248, v212\n"
    "v_mov_b32 v20, 0\n"
    "v_mfma_f32_32x32x16_bf16 a[16:31], a[232:235], v[232:235], 0\n"
    "v_add_f32 v24, v24, v251\n"
    "v_cvt_pk_bf16_f32 v241, v250, v251\n"
    "buffer_load_dwordx4 v11, s[20:23], s43 offen offset:2048 nt lds\n"
    "v_mfma_f32_32x32x16_bf16 a[64:79], a[236:239], v[232:235], 0\n"
    "v_exp_f32 v249, v213\n"
    "v_add_f32 v24, v24, v248\n"
    "v_exp_f32 v250, v214\n"
    "v_add_f32 v24, v24, v249\n"
    "v_mfma_f32_32x32x16_bf16 v[48:63], a[240:243], v[236:239], v[48:63]\n"
    "v_cvt_pk_bf16_f32 v242, v248, v249\n"
    "v_exp_f32 v251, v215\n"
    "v_add_f32 v24, v24, v250\n"
    "v_exp_f32 v248, v216\n"
    "v_mfma_f32_32x32x16_bf16 v[96:111], a[244:247], v[236:239], v[96:111]\n"
    "v_add_f32 v24, v24, v251\n"
    "v_cvt_pk_bf16_f32 v243, v250, v251\n"
    "v_exp_f32 v249, v217\n"
    "v_add_f32 v24, v24, v248\n"
    "v_mfma_f32_32x32x16_bf16 a[16:31], a[248:251], v[236:239], a[16:31]\n"
    "v_exp_f32 v250, v218\n"
    "v_add_f32 v24, v24, v249\n"
    "v_cvt_pk_bf16_f32 v244, v248, v249\n"
    "v_exp_f32 v251, v219\n"
    "v_mfma_f32_32x32x16_bf16 a[64:79], a[252:255], v[236:239], a[64:79]\n"
    "v_add_f32 v24, v24, v250\n"
    "v_exp_f32 v248, v220\n"
    "v_add_f32 v24, v24, v251\n"
    "v_cvt_pk_bf16_f32 v245, v250, v251\n"
    "v_mfma_f32_32x32x16_bf16 v[192:207], a[192:195], a[128:131], v[144:159]\n"
    "v_exp_f32 v249, v221\n"
    "v_add_f32 v24, v24, v248\n"
    "v_exp_f32 v250, v222\n"
    "v_add_f32 v24, v24, v249\n"
    "v_mfma_f32_32x32x16_bf16 v[192:207], a[196:199], a[132:135], v[192:207]\n"
    "buffer_load_dwordx4 v12, s[20:23], s43 offen offset:3072 nt lds\n"
    "s_cmp_lg_u32 s36, 0\n"
    "s_cbranch_scc1 .L3_e0\n"
    "buffer_load_dwordx2 v[18:19], v20, s[76:79], s75 offen sc1\n"
    "s_lshl_b32 s74, %[bid], 9\n"
    "v_and_b32 v31, 3, v4\n"
    "v_lshlrev_b32 v31, 7, v31\n"
    "buffer_load_dword v17, v31, s[76:79], s74 offen sc1\n"
    ".L3_e0:\n"
    "v_mfma_f32_32x32x16_bf16 v[192:207], a[200:203], a[136:139], v[192:207]\n"
    "v_cvt_pk_bf16_f32 v246, v248, v249\n"
    "v_exp_f32 v251, v223\n"
    "v_add_f32 v24, v24, v250\n"
    "v_add_f32 v24, v24, v251\n"
    "v_cvt_pk_bf16_f32 v247, v250, v251\n"
    "v_mfma_f32_32x32x16_bf16 v[192:207], a[204:207], a[140:143], v[192:207]\n"
    "v_exp_f32 v248, v176\n"
    "v_med3_f32 v252, v24, s49, v26\n"
    "v_cmp_neq_f32 vcc, v252, v24\n"
    "v_exp_f32 v249, v177\n"
    "v_mfma_f32_32x32x16_bf16 v[192:207], a[208:211], a[144:147], v[192:207]\n"
    "s_cbranch_vccnz .L_stub2\n"
    ".L_ret2:\n"
    "v_add_f32 v23, v23, v24\n"
    "v_exp_f32 v250, v178\n"
    "v_add_f32 v25, v248, v249\n"
    "v_cvt_pk_bf16_f32 v224, v248, v249\n"
    "v_mfma_f32_32x32x16_bf16 v[192:207], a[212:215], a[148:151], v[192:207]\n"
    "v_exp_f32 v251, v179\n"
    "v_add_f32 v25, v25, v250\n"
    "v_exp_f32 v248, v180\n"
    "v_add_f32 v25, v25, v251\n"
    "v_mfma_f32_32x32x16_bf16 v[192:207], a[216:219], a[152:155], v[192:207]\n"
    "v_cvt_pk_bf16_f32 v225, v250, v251\n"
    "v_exp_f32 v249, v181\n"
    "v_add_f32 v25, v25, v248\n"
    "v_exp_f32 v250, v182\n"
    "v_mfma_f32_32x32x16_bf16 v[192:207], a[220:223], a[156:159], v[192:207]\n"
    "v_add_u32 v7, 32768, v7\n"
    "v_add_u32 v8, 32768, v8\n"
    "s_add_u32 m0, s48, 114688\n"
    "s_add_u32 s43, s48, 49152\n"
    "buffer_load_dwordx4 v9, s[24:27], s43 offen nt lds\n"
    "v_mfma_f32_32x32x16_bf16 v[64:79], a[224:227], v[240:243], 0\n"
    "s_waitcnt vmcnt(9)\n"
    "s_barrier\n"
    "ds_read_b64_tr_b16 a[224:225], v7 offset:0\n"
    "v_mfma_f32_32x32x16_bf16 v[112:127], a[228:231], v[240:243], 0\n"
    "v_add_f32 v25, v25, v249\n"
    "v_cvt_pk_bf16_f32 v226, v248, v249\n"
    "v_exp_f32 v251, v183\n"
    "ds_read_b64_tr_b16 a[228:229], v7 offset:256\n"
    "v_mfma_f32_32x32x16_bf16 a[32:47], a[232:235], v[240:243], 0\n"
    "v_add_f32 v25, v25, v250\n"
    "v_exp_f32 v248, v184\n"
    "v_add_f32 v25, v25, v251\n"
    "ds_read_b64_tr_b16 a[232:233], v8 offset:0\n"
    "v_mfma_f32_32x32x16_bf16 a[80:95], a[236:239], v[240:243], 0\n"
    "v_cvt_pk_bf16_f32 v227, v250, v251\n"
    "v_exp_f32 v249, v185\n"
    "v_add_f32 v25, v25, v248\n"
    "ds_read_b64_tr_b16 a[236:237], v8 offset:256\n"
    "v_mfma_f32_32x32x16_bf16 v[64:79], a[240:243], v[244:247], v[64:79]\n"
    "v_exp_f32 v250, v186\n"
    "v_add_f32 v25, v25, v249\n"
    "v_cvt_pk_bf16_f32 v228, v248, v249\n"
    "ds_read_b64_tr_b16 a[240:241], v7 offset:4096\n"
    "v_mfma_f32_32x32x16_bf16 v[112:127], a[244:247], v[244:247], v[112:127]\n"
    "v_exp_f32 v251, v187\n"
    "v_add_f32 v25, v25, v250\n"
    "ds_read_b64_tr_b16 a[244:245], v7 offset:4352\n"
    "v_mfma_f32_32x32x16_bf16 a[32:47], a[248:251], v[244:247], a[32:47]\n"
    "ds_read_b64_tr_b16 a[248:249], v8 offset:4096\n"
    "buffer_load_dwordx4 v9, s[24:27], s43 offen offset:1024 nt lds\n"
    "v_mfma_f32_32x32x16_bf16 a[80:95], a[252:255], v[244:247], a[80:95]\n"
    "v_exp_f32 v248, v188\n"
    "v_add_f32 v25, v25, v251\n"
    "v_cvt_pk_bf16_f32 v229, v250, v251\n"
    "ds_read_b64_tr_b16 a[252:253], v8 offset:4352\n"
    "v_mfma_f32_32x32x16_bf16 v[208:223], a[192:195], a[160:163], v[160:175]\n"
    "v_exp_f32 v249, v189\n"
    "v_add_f32 v25, v25, v248\n"
    "ds_read_b64_tr_b16 a[226:227], v7 offset:2048\n"
    "v_mfma_f32_32x32x16_bf16 v[208:223], a[196:199], a[164:167], v[208:223]\n"
    "v_exp_f32 v250, v190\n"
    "v_add_f32 v25, v25, v249\n"
    "v_cvt_pk_bf16_f32 v230, v248, v249\n"
    "ds_read_b64_tr_b16 a[230:231], v7 offset:2304\n"
    "v_mfma_f32_32x32x16_bf16 v[208:223], a[200:203], a[168:171], v[208:223]\n"
    "v_exp_f32 v251, v191\n"
    "v_add_f32 v25, v25, v250\n"
    "v_add_f32 v25, v25, v251\n"
    "ds_read_b64_tr_b16 a[234:235], v8 offset:2048\n"
    "v_mfma_f32_32x32x16_bf16 v[208:223], a[204:207], a[172:175], v[208:223]\n"
    "v_cvt_pk_bf16_f32 v231, v250, v251\n"
    "v_exp_f32 v248, v192\n"
    "v_cmp_lt_f32 vcc, v26, v25\n"
    "ds_read_b64_tr_b16 a[238:239], v8 offset:2304\n"
    "v_mfma_f32_32x32x16_bf16 v[208:223], a[208:211], a[176:179], v[208:223]\n"
    "v_exp_f32 v249, v193\n"
    "s_cbranch_vccnz .L_stub3\n"
    ".L_ret3:\n"
    "v_add_f32 v21, v21, v25\n"
    "ds_read_b64_tr_b16 a[242:243], v7 offset:6144\n"
    "v_mfma_f32_32x32x16_bf16 v[208:223], a[212:215], a[180:183], v[208:223]\n"
    "ds_read_b64_tr_b16 a[246:247], v7 offset:6400\n"
    "buffer_load_dwordx4 v9, s[24:27], s43 offen offset:2048 nt lds\n"
    "v_mfma_f32_32x32x16_bf16 v[208:223], a[216:219], a[184:187], v[208:223]\n"
    "v_exp_f32 v250, v194\n"
    "v_add_f32 v24, v248, v249\n"
    "v_cvt_pk_bf16_f32 v232, v248, v249\n"
    "ds_read_b64_tr_b16 a[250:251], v8 offset:6144\n"
    "v_mfma_f32_32x32x16_bf16 v[208:223], a[220:223], a[188:191], v[208:223]\n"
    "ds_read_b64_tr_b16 a[254:255], v8 offset:6400\n"
    "v_add_u32 v5, 32768, v5\n"
    "v_add_u32 v6, 32768, v6\n"
    "s_waitcnt lgkmcnt(4)\n"
    "v_mfma_f32_32x32x16_bf16 v[32:47], a[224:227], v[224:227], v[32:47]\n"
    "v_exp_f32 v251, v195\n"
    "v_add_f32 v24, v24, v250\n"
    "ds_read_b128 a[192:195], v5 offset:0\n"
    "v_mfma_f32_32x32x16_bf16 v[80:95], a[228:231], v[224:227], v[80:95]\n"
    "v_exp_f32 v248, v196\n"
    "v_add_f32 v24, v24, v251\n"
    "v_cvt_pk_bf16_f32 v233, v250, v251\n"
    "ds_read_b128 a[196:199], v6 offset:0\n"
    "v_mfma_f32_32x32x16_bf16 a[0:15], a[232:235], v[224:227], a[0:15]\n"
    "v_exp_f32 v249, v197\n"
    "v_add_f32 v24, v24, v248\n"
    "ds_read_b128 a[200:203], v5 offset:256\n"
    "v_mfma_f32_32x32x16_bf16 a[48:63], a[236:239], v[224:227], a[48:63]\n"
    "ds_read_b128 a[204:207], v6 offset:256\n"
    "buffer_load_dwordx4 v9, s[24:27], s43 offen offset:3072 nt lds\n"
    "s_waitcnt lgkmcnt(4)\n"
    "v_mfma_f32_32x32x16_bf16 v[32:47], a[240:243], v[228:231], v[32:47]\n"
    "v_exp_f32 v250, v198\n"
    "v_add_f32 v24, v24, v249\n"
    "v_cvt_pk_bf16_f32 v234, v248, v249\n"
    "ds_read_b128 a[208:211], v5 offset:512\n"
    "v_mfma_f32_32x32x16_bf16 v[80:95], a[244:247], v[228:231], v[80:95]\n"
    "v_exp_f32 v251, v199\n"
    "v_add_f32 v24, v24, v250\n"
    "ds_read_b128 a[212:215], v6 offset:512\n"
    "v_mfma_f32_32x32x16_bf16 a[0:15], a[248:251], v[228:231], a[0:15]\n"
    "v_exp_f32 v248, v200\n"
    "v_add_f32 v24, v24, v251\n"
    "v_cvt_pk_bf16_f32 v235, v250, v251\n"
    "ds_read_b128 a[216:219], v5 offset:768\n"
    "v_mfma_f32_32x32x16_bf16 a[48:63], a[252:255], v[228:231], a[48:63]\n"
    "v_exp_f32 v249, v201\n"
    "v_add_f32 v24, v24, v248\n"
    "ds_read_b128 a[220:223], v6 offset:768\n"
    "s_waitcnt lgkmcnt(4)\n"
    "v_mfma_f32_32x32x16_bf16 v[176:191], a[192:195], a[96:99], v[128:143]\n"
    "v_exp_f32 v250, v202\n"
    "v_add_f32 v24, v24, v249\n"
    "v_cvt_pk_bf16_f32 v236, v248, v249\n"
    "v_exp_f32 v251, v203\n"
    "v_mfma_f32_32x32x16_bf16 v[176:191], a[196:199], a[100:103], v[176:191]\n"
    "v_add_f32 v24, v24, v250\n"
    "v_exp_f32 v248, v204\n"
    "v_add_f32 v24, v24, v251\n"
    "v_cvt_pk_bf16_f32 v237, v250, v251\n"
    "v_mfma_f32_32x32x16_bf16 v[176:191], a[200:203], a[104:107], v[176:191]\n"
    "s_add_u32 m0, s48, 131072\n"
    "s_add_u32 s43, s48, 65536\n"
    "buffer_load_dwordx4 v9, s[20:23], s43 offen nt lds\n"
    "v_mfma_f32_32x32x16_bf16 v[176:191], a[204:207], a[108:111], v[176:191]\n"
    "v_exp_f32 v249, v205\n"
    "v_add_f32 v24, v24, v248\n"
    "v_exp_f32 v250, v206\n"
    "s_waitcnt lgkmcnt(0)\n"
    "v_mfma_f32_32x32x16_bf16 v[176:191], a[208:211], a[112:115], v[176:191]\n"
    "v_add_f32 v24, v24, v249\n"
    "v_cvt_pk_bf16_f32 v238, v248, v249\n"
    "v_exp_f32 v251, v207\n"
    "v_add_f32 v24, v24, v250\n"
    "v_add_f32 v24, v24, v251\n"
    "v_mfma_f32_32x32x16_bf16 v[176:191], a[212:215], a[116:119], v[176:191]\n"
    "v_cvt_pk_bf16_f32 v239, v250, v251\n"
    "v_exp_f32 v248, v208\n"
    "v_cmp_lt_f32 vcc, v26, v24\n"
    "v_exp_f32 v249, v209\n"
    "v_mfma_f32_32x32x16_bf16 v[176:191], a[216:219], a[120:123], v[176:191]\n"
    "s_cbranch_vccnz .L_stub4\n"
    ".L_ret4:\n"
    "v_add_f32 v22, v22, v24\n"
    "v_exp_f32 v250, v210\n"
    "v_add_f32 v25, v248, v249\n"
    "v_cvt_pk_bf16_f32 v240, v248, v249\n"
    "v_mfma_f32_32x32x16_bf16 v[176:191], a[220:223], a[124:127], v[176:191]\n"
    "v_exp_f32 v251, v211\n"
    "v_add_f32 v25, v25, v250\n"
    "v_exp_f32 v248, v212\n"
    "v_add_f32 v25, v25, v251\n"
    "v_mfma_f32_32x32x16_bf16 v[48:63], a[224:227], v[232:235], v[48:63]\n"
    "v_cvt_pk_bf16_f32 v241, v250, v251\n"
    "v_exp_f32 v249, v213\n"
    "buffer_load_dwordx4 v10, s[20:23], s43 offen offset:1024 nt lds\n"
    "v_mfma_f32_32x32x16_bf16 v[96:111], a[228:231], v[232:235], v[96:111]\n"
    "v_add_f32 v25, v25, v248\n"
    "v_exp_f32 v250, v214\n"
    "v_add_f32 v25, v25, v249\n"
    "v_cvt_pk_bf16_f32 v242, v248, v249\n"
    "v_mfma_f32_32x32x16_bf16 a[16:31], a[232:235], v[232:235], a[16:31]\n"
    "v_exp_f32 v251, v215\n"
    "v_add_f32 v25, v25, v250\n"
    "v_exp_f32 v248, v216\n"
    "v_add_f32 v25, v25, v251\n"
    "v_mfma_f32_32x32x16_bf16 a[64:79], a[236:239], v[232:235], a[64:79]\n"
    "v_cvt_pk_bf16_f32 v243, v250, v251\n"
    "v_exp_f32 v249, v217\n"
    "v_add_f32 v25, v25, v248\n"
    "v_exp_f32 v250, v218\n"
    "v_mfma_f32_32x32x16_bf16 v[48:63], a[240:243], v[236:239], v[48:63]\n"
    "v_add_f32 v25, v25, v249\n"
    "v_cvt_pk_bf16_f32 v244, v248, v249\n"
    "v_exp_f32 v251, v219\n"
    "v_add_f32 v25, v25, v250\n"
    "v_mfma_f32_32x32x16_bf16 v[96:111], a[244:247], v[236:239], v[96:111]\n"
    "v_exp_f32 v248, v220\n"
    "v_add_f32 v25, v25, v251\n"
    "v_cvt_pk_bf16_f32 v245, v250, v251\n"
    "v_exp_f32 v249, v221\n"
    "v_mfma_f32_32x32x16_bf16 a[16:31], a[248:251], v[236:239], a[16:31]\n"
    "v_add_f32 v25, v25, v248\n"
    "v_exp_f32 v250, v222\n"
    "v_add_f32 v25, v25, v249\n"
    "v_cvt_pk_bf16_f32 v246, v248, v249\n"
    "v_mfma_f32_32x32x16_bf16 a[64:79], a[252:255], v[236:239], a[64:79]\n"
    "v_exp_f32 v251, v223\n"
    "v_add_f32 v25, v25, v250\n"
    "buffer_load_dwordx4 v11, s[20:23], s43 offen offset:2048 nt lds\n"
    "v_mfma_f32_32x32x16_bf16 v[192:207], a[192:195], a[128:131], v[144:159]\n"
    "v_add_f32 v25, v25, v251\n"
    "v_cvt_pk_bf16_f32 v247, v250, v251\n"
    "v_exp_f32 v248, v176\n"
    "v_cmp_lt_f32 vcc, v26, v25\n"
    "v_mfma_f32_32x32x16_bf16 v[192:207], a[196:199], a[132:135], v[192:207]\n"
    "v_exp_f32 v249, v177\n"
    "s_cbranch_vccnz .L_stub5\n"
    ".L_ret5:\n"
    "v_add_f32 v23, v23, v25\n"
    "v_exp_f32 v250, v178\n"
    "v_mfma_f32_32x32x16_bf16 v[192:207], a[200:203], a[136:139], v[192:207]\n"
    "v_add_f32 v24, v248, v249\n"
    "v_cvt_pk_bf16_f32 v224, v248, v249\n"
    "v_exp_f32 v251, v179\n"
    "v_add_f32 v24, v24, v250\n"
    "v_mfma_f32_32x32x16_bf16 v[192:207], a[204:207], a[140:143], v[192:207]\n"
    "v_exp_f32 v248, v180\n"
    "v_add_f32 v24, v24, v251\n"
    "v_cvt_pk_bf16_f32 v225, v250, v251\n"
    "v_exp_f32 v249, v181\n"
    "v_mfma_f32_32x32x16_bf16 v[192:207], a[208:211], a[144:147], v[192:207]\n"
    "v_add_f32 v24, v24, v248\n"
    "v_exp_f32 v250, v182\n"
    "v_add_f32 v24, v24, v249\n"
    "v_cvt_pk_bf16_f32 v226, v248, v249\n"
    "v_mfma_f32_32x32x16_bf16 v[192:207], a[212:215], a[148:151], v[192:207]\n"
    "v_exp_f32 v251, v183\n"
    "v_add_f32 v24, v24, v250\n"
    "v_exp_f32 v248, v184\n"
    "v_add_f32 v24, v24, v251\n"
    "v_mfma_f32_32x32x16_bf16 v[192:207], a[216:219], a[152:155], v[192:207]\n"
    "v_cvt_pk_bf16_f32 v227, v250, v251\n"
    "v_exp_f32 v249, v185\n"
    "buffer_load_dwordx4 v12, s[20:23], s43 offen offset:3072 nt lds\n"
    "v_mfma_f32_32x32x16_bf16 v[192:207], a[220:223], a[156:159], v[192:207]\n"
    "v_add_f32 v24, v24, v248\n"
    "v_exp_f32 v250, v186\n"
    "v_add_f32 v24, v24, v249\n"
    "v_add_u32 v7, 32768, v7\n"
    "v_add_u32 v8, 32768, v8\n"
    "v_mfma_f32_32x32x16_bf16 v[64:79], a[224:227], v[240:243], v[64:79]\n"
    "s_waitcnt vmcnt(8)\n"
    "s_barrier\n"
    "ds_read_b64_tr_b16 a[224:225], v7 offset:0\n"
    "v_mfma_f32_32x32x16_bf16 v[112:127], a[228:231], v[240:243], v[112:127]\n"
    "v_cvt_pk_bf16_f32 v228, v248, v249\n"
    "v_exp_f32 v251, v187\n"
    "v_add_f32 v24, v24, v250\n"
    "ds_read_b64_tr_b16 a[228:229], v7 offset:256\n"
    "v_mfma_f32_32x32x16_bf16 a[32:47], a[232:235], v[240:243], a[32:47]\n"
    "v_exp_f32 v248, v188\n"
    "v_add_f32 v24, v24, v251\n"
    "v_cvt_pk_bf16_f32 v229, v250, v251\n"
    "ds_read_b64_tr_b16 a[232:233], v8 offset:0\n"
    "v_mfma_f32_32x32x16_bf16 a[80:95], a[236:239], v[240:243], a[80:95]\n"
    "v_exp_f32 v249, v189\n"
    "v_add_f32 v24, v24, v248\n"
    "ds_read_b64_tr_b16 a[236:237], v8 offset:256\n"
    "v_mfma_f32_32x32x16_bf16 v[64:79], a[240:243], v[244:247], v[64:79]\n"
    "ds_read_b64_tr_b16 a[240:241], v7 offset:4096\n"
    "s_add_u32 m0, s48, 0\n"
    "s_add_u32 s43, s48, 65536\n"
    "buffer_load_dwordx4 v9, s[24:27], s43 offen nt lds\n"
    "v_mfma_f32_32x32x16_bf16 v[112:127], a[244:247], v[244:247], v[112:127]\n"
    "v_exp_f32 v250, v190\n"
    "v_add_f32 v24, v24, v249\n"
    "v_cvt_pk_bf16_f32 v230, v248, v249\n"
    "ds_read_b64_tr_b16 a[244:245], v7 offset:4352\n"
    "v_mfma_f32_32x32x16_bf16 a[32:47], a[248:251], v[244:247], a[32:47]\n"
    "v_exp_f32 v251, v191\n"
    "v_add_f32 v24, v24, v250\n"
    "v_add_f32 v24, v24, v251\n"
    "ds_read_b64_tr_b16 a[248:249], v8 offset:4096\n"
    "v_mfma_f32_32x32x16_bf16 a[80:95], a[252:255], v[244:247], a[80:95]\n"
    "v_cvt_pk_bf16_f32 v231, v250, v251\n"
    "v_exp_f32 v248, v192\n"
    "v_cmp_lt_f32 vcc, v26, v24\n"
    "ds_read_b64_tr_b16 a[252:253], v8 offset:4352\n"
    "v_mfma_f32_32x32x16_bf16 v[208:223], a[192:195], a[160:163], v[160:175]\n"
    "v_exp_f32 v249, v193\n"
    "s_cbranch_vccnz .L_stub6\n"
    ".L_ret6:\n"
    "v_add_f32 v21, v21, v24\n"
    "ds_read_b64_tr_b16 a[226:227], v7 offset:2048\n"
    "v_mfma_f32_32x32x16_bf16 v[208:223], a[196:199], a[164:167], v[208:223]\n"
    "v_exp_f32 v250, v194\n"
    "v_add_f32 v25, v248, v249\n"
    "v_cvt_pk_bf16_f32 v232, v248, v249\n"
    "ds_read_b64_tr_b16 a[230:231], v7 offset:2304\n"
    "v_mfma_f32_32x32x16_bf16 v[208:223], a[200:203], a[168:171], v[208:223]\n"
    "v_exp_f32 v251, v195\n"
    "v_add_f32 v25, v25, v250\n"
    "ds_read_b64_tr_b16 a[234:235], v8 offset:2048\n"
    "v_mfma_f32_32x32x16_bf16 v[208:223], a[204:207], a[172:175], v[208:223]\n"
    "ds_read_b64_tr_b16 a[238:239], v8 offset:2304\n"
    "buffer_load_dwordx4 v9, s[24:27], s43 offen offset:1024 nt lds\n"
    "v_mfma_f32_32x32x16_bf16 v[208:223], a[208:211], a[176:179], v[208:223]\n"
    "v_exp_f32 v248, v196\n"
    "v_add_f32 v25, v25, v251\n"
    "v_cvt_pk_bf16_f32 v233, v250, v251\n"
    "ds_read_b64_tr_b16 a[242:243], v7 offset:6144\n"
    "v_mfma_f32_32x32x16_bf16 v[208:223], a[212:215], a[180:183], v[208:223]\n"
    "v_exp_f32 v249, v197\n"
    "v_add_f32 v25, v25, v248\n"
    "ds_read_b64_tr_b16 a[246:247], v7 offset:6400\n"
    "v_mfma_f32_32x32x16_bf16 v[208:223], a[216:219], a[184:187], v[208:223]\n"
    "v_exp_f32 v250, v198\n"
    "v_add_f32 v25, v25, v249\n"
    "v_cvt_pk_bf16_f32 v234, v248, v249\n"
    "ds_read_b64_tr_b16 a[250:251], v8 offset:6144\n"
    "v_mfma_f32_32x32x16_bf16 v[208:223], a[220:223], a[188:191], v[208:223]\n"
    "ds_read_b64_tr_b16 a[254:255], v8 offset:6400\n"
    "v_add_u32 v5, 32768, v5\n"
    "v_add_u32 v6, 32768, v6\n"
    "s_waitcnt lgkmcnt(4)\n"
    "v_mfma_f32_32x32x16_bf16 v[32:47], a[224:227], v[224:227], v[32:47]\n"
    "v_exp_f32 v251, v199\n"
    "v_add_f32 v25, v25, v250\n"
    "ds_read_b128 a[192:195], v5 offset:0\n"
    "v_mfma_f32_32x32x16_bf16 v[80:95], a[228:231], v[224:227], v[80:95]\n"
    "ds_read_b128 a[196:199], v6 offset:0\n"
    "buffer_load_dwordx4 v9, s[24:27], s43 offen offset:2048 nt lds\n"
    "v_mfma_f32_32x32x16_bf16 a[0:15], a[232:235], v[224:227], a[0:15]\n"
    "v_exp_f32 v248, v200\n"
    "v_add_f32 v25, v25, v251\n"
    "v_cvt_pk_bf16_f32 v235, v250, v251\n"
    "ds_read_b128 a[200:203], v5 offset:256\n"
    "v_mfma_f32_32x32x16_bf16 a[48:63], a[236:239], v[224:227], a[48:63]\n"
    "v_exp_f32 v249, v201\n"
    "v_add_f32 v25, v25, v248\n"
    "ds_read_b128 a[204:207], v6 offset:256\n"
    "s_waitcnt lgkmcnt(4)\n"
    "v_mfma_f32_32x32x16_bf16 v[32:47], a[240:243], v[228:231], v[32:47]\n"
    "v_exp_f32 v250, v202\n"
    "v_add_f32 v25, v25, v249\n"
    "v_cvt_pk_bf16_f32 v236, v248, v249\n"
    "ds_read_b128 a[208:211], v5 offset:512\n"
    "v_mfma_f32_32x32x16_bf16 v[80:95], a[244:247], v[228:231], v[80:95]\n"
    "v_exp_f32 v251, v203\n"
    "v_add_f32 v25, v25, v250\n"
    "ds_read_b128 a[212:215], v6 offset:512\n"
    "v_mfma_f32_32x32x16_bf16 a[0:15], a[248:251], v[228:231], a[0:15]\n"
    "v_exp_f32 v248, v204\n"
    "v_add_f32 v25, v25, v251\n"
    "v_cvt_pk_bf16_f32 v237, v250, v251\n"
    "ds_read_b128 a[216:219], v5 offset:768\n"
    "v_mfma_f32_32x32x16_bf16 a[48:63], a[252:255], v[228:231], a[48:63]\n"
    "v_exp_f32 v249, v205\n"
    "v_add_f32 v25, v25, v248\n"
    "ds_read_b128 a[220:223], v6 offset:768\n"
    "s_waitcnt lgkmcnt(4)\n"
    "v_mfma_f32_32x32x16_bf16 v[176:191], a[192:195], a[96:99], v[128:143]\n"
    "v_exp_f32 v250, v206\n"
    "v_add_f32 v25, v25, v249\n"
    "buffer_load_dwordx4 v9, s[24:27], s43 offen offset:3072 nt lds\n"
    "v_mfma_f32_32x32x16_bf16 v[176:191], a[196:199], a[100:103], v[176:191]\n"
    "s_cmp_lg_u32 s36, 0\n"
    "s_cbranch_scc1 .L3_e1\n"
    "s_waitcnt vmcnt(12)\n"
    "v_add_u32 v18, 0x7f4a7c15, v18\n"
    "v_add_u32 v19, 0x9e3779b9, v19\n"
    "s_mov_b64 exec, 1\n"
    "buffer_store_dwordx2 v[18:19], v20, s[76:79], s75 offen sc1\n"
    "s_mov_b64 exec, -1\n"
    "s_nop 3\n"
    ".L3_e1:\n"
    "v_mfma_f32_32x32x16_bf16 v[176:191], a[200:203], a[104:107], v[176:191]\n"
    "v_cvt_pk_bf16_f32 v238, v248, v249\n"
    "v_exp_f32 v251, v207\n"
    "v_add_f32 v25, v25, v250\n"
    "v_add_f32 v25, v25, v251\n"
    "v_cvt_pk_bf16_f32 v239, v250, v251\n"
    "v_mfma_f32_32x32x16_bf16 v[176:191], a[204:207], a[108:111], v[176:191]\n"
    "v_exp_f32 v248, v208\n"
    "v_cmp_lt_f32 vcc, v26, v25\n"
    "v_exp_f32 v249, v209\n"
    "s_waitcnt lgkmcnt(0)\n"
    "v_mfma_f32_32x32x16_bf16 v[176:191], a[208:211], a[112:115], v[176:191]\n"
    "s_cbranch_vccnz .L_stub7\n"
    ".L_ret7:\n"
    "v_add_f32 v22, v22, v25\n"
    "v_exp_f32 v250, v210\n"
    "v_add_f32 v24, v248, v249\n"
    "v_cvt_pk_bf16_f32 v240, v248, v249\n"
    "v_mfma_f32_32x32x16_bf16 v[176:191], a[212:215], a[116:119], v[176:191]\n"
    "v_exp_f32 v251, v211\n"
    "v_add_f32 v24, v24, v250\n"
    "v_exp_f32 v248, v212\n"
    "v_add_f32 v24, v24, v251\n"
    "v_mfma_f32_32x32x16_bf16 v[176:191], a[216:219], a[120:123], v[176:191]\n"
    "v_cvt_pk_bf16_f32 v241, v250, v251\n"
    "v_exp_f32 v249, v213\n"
    "v_add_f32 v24, v24, v248\n"
    "v_exp_f32 v250, v214\n"
    "v_mfma_f32_32x32x16_bf16 v[176:191], a[220:223], a[124:127], v[176:191]\n"
    "v_add_f32 v24, v24, v249\n"
    "s_add_u32 m0, s48, 147456\n"
    "s_add_u32 s43, s48, 81920\n"
    "buffer_load_dwordx4 v9, s[20:23], s43 offen nt lds\n"
    "v_mfma_f32_32x32x16_bf16 v[48:63], a[224:227], v[232:235], v[48:63]\n"
    "v_cvt_pk_bf16_f32 v242, v248, v249\n"
    "v_exp_f32 v251, v215\n"
    "v_add_f32 v24, v24, v250\n"
    "v_exp_f32 v248, v216\n"
    "v_mfma_f32_32x32x16_bf16 v[96:111], a[228:231], v[232:235], v[96:111]\n"
    "v_add_f32 v24, v24, v251\n"
    "v_cvt_pk_bf16_f32 v243, v250, v251\n"
    "v_exp_f32 v249, v217\n"
    "v_add_f32 v24, v24, v248\n"
    "v_mfma_f32_32x32x16_bf16 a[16:31], a[232:235], v[232:235], a[16:31]\n"
    "v_exp_f32 v250, v218\n"
    "v_add_f32 v24, v24, v249\n"
    "v_cvt_pk_bf16_f32 v244, v248, v249\n"
    "v_exp_f32 v251, v219\n"
    "v_mfma_f32_32x32x16_bf16 a[64:79], a[236:239], v[232:235], a[64:79]\n"
    "v_add_f32 v24, v24, v250\n"
    "v_exp_f32 v248, v220\n"
    "v_add_f32 v24, v24, v251\n"
    "v_cvt_pk_bf16_f32 v245, v250, v251\n"
    "v_mfma_f32_32x32x16_bf16 v[48:63], a[240:243], v[236:239], v[48:63]\n"
    "v_exp_f32 v249, v221\n"
    "v_add_f32 v24, v24, v248\n"
    "v_exp_f32 v250, v222\n"
    "v_add_f32 v24, v24, v249\n"
    "v_mfma_f32_32x32x16_bf16 v[96:111], a[244:247], v[236:239], v[96:111]\n"
    "v_cvt_pk_bf16_f32 v246, v248, v249\n"
    "v_exp_f32 v251, v223\n"
    "buffer_load_dwordx4 v10, s[20:23], s43 offen offset:1024 nt lds\n"
    "v_mfma_f32_32x32x16_bf16 a[16:31], a[248:251], v[236:239], a[16:31]\n"
    "v_add_f32 v24, v24, v250\n"
    "v_add_f32 v24, v24, v251\n"
    "v_cvt_pk_bf16_f32 v247, v250, v251\n"
    "v_exp_f32 v248, v176\n"
    "v_cmp_lt_f32 vcc, v26, v24\n"
    "v_mfma_f32_32x32x16_bf16 a[64:79], a[252:255], v[236:239], a[64:79]\n"
    "v_exp_f32 v249, v177\n"
    "s_cbranch_vccnz .L_stub8\n"
    ".L_ret8:\n"
    "v_add_f32 v23, v23, v24\n"
    "v_exp_f32 v250, v178\n"
    "v_mfma_f32_32x32x16_bf16 v[192:207], a[192:195], a[128:131], v[144:159]\n"
    "v_add_f32 v25, v248, v249\n"
    "v_cvt_pk_bf16_f32 v224, v248, v249\n"
    "v_exp_f32 v251, v179\n"
    "v_add_f32 v25, v25, v250\n"
    "v_mfma_f32_32x32x16_bf16 v[192:207], a[196:199], a[132:135], v[192:207]\n"
    "v_exp_f32 v248, v180\n"
    "v_add_f32 v25, v25, v251\n"
    "v_cvt_pk_bf16_f32 v225, v250, v251\n"
    "v_exp_f32 v249, v181\n"
    "v_mfma_f32_32x32x16_bf16 v[192:207], a[200:203], a[136:139], v[192:207]\n"
    "v_add_f32 v25, v25, v248\n"
    "v_exp_f32 v250, v182\n"
    "v_add_f32 v25, v25, v249\n"
    "v_cvt_pk_bf16_f32 v226, v248, v249\n"
    "v_mfma_f32_32x32x16_bf16 v[192:207], a[204:207], a[140:143], v[192:207]\n"
    "v_exp_f32 v251, v183\n"
    "v_add_f32 v25, v25, v250\n"
    "v_exp_f32 v248, v184\n"
    "v_add_f32 v25, v25, v251\n"
    "v_mfma_f32_32x32x16_bf16 v[192:207], a[208:211], a[144:147], v[192:207]\n"
    "v_cvt_pk_bf16_f32 v227, v250, v251\n"
    "v_exp_f32 v249, v185\n"
    "buffer_load_dwordx4 v11, s[20:23], s43 offen offset:2048 nt lds\n"
    "v_mfma_f32_32x32x16_bf16 v[192:207], a[212:215], a[148:151], v[192:207]\n"
    "v_add_f32 v25, v25, v248\n"
    "v_exp_f32 v250, v186\n"
    "v_add_f32 v25, v25, v249\n"
    "v_cvt_pk_bf16_f32 v228, v248, v249\n"
    "v_mfma_f32_32x32x16_bf16 v[192:207], a[216:219], a[152:155], v[192:207]\n"
    "v_exp_f32 v251, v187\n"
    "v_add_f32 v25, v25, v250\n"
    "v_exp_f32 v248, v188\n"
    "v_add_f32 v25, v25, v251\n"
    "v_mfma_f32_32x32x16_bf16 v[192:207], a[220:223], a[156:159], v[192:207]\n"
    "v_cvt_pk_bf16_f32 v229, v250, v251\n"
    "v_exp_f32 v249, v189\n"
    "v_add_f32 v25, v25, v248\n"
    "v_add_u32 v7, 32768, v7\n"
    "v_add_u32 v8, 32768, v8\n"
    "v_mfma_f32_32x32x16_bf16 v[64:79], a[224:227], v[240:243], v[64:79]\n"
    "s_waitcnt vmcnt(7)\n"
    "s_barrier\n"
    "ds_read_b64_tr_b16 a[224:225], v7 offset:0\n"
    "v_mfma_f32_32x32x16_bf16 v[112:127], a[228:231], v[240:243], v[112:127]\n"
    "v_exp_f32 v250, v190\n"
    "v_add_f32 v25, v25, v249\n"
    "v_cvt_pk_bf16_f32 v230, v248, v249\n"
    "ds_read_b64_tr_b16 a[228:229], v7 offset:256\n"
    "v_mfma_f32_32x32x16_bf16 a[32:47], a[232:235], v[240:243], a[32:47]\n"
    "ds_read_b64_tr_b16 a[232:233], v8 offset:0\n"
    "buffer_load_dwordx4 v12, s[20:23], s43 offen offset:3072 nt lds\n"
    "v_mfma_f32_32x32x16_bf16 a[80:95], a[236:239], v[240:243], a[80:95]\n"
    "v_exp_f32 v251, v191\n"
    "v_add_f32 v25, v25, v250\n"
    "v_add_f32 v25, v25, v251\n"
    "ds_read_b64_tr_b16 a[236:237], v8 offset:256\n"
    "v_mfma_f32_32x32x16_bf16 v[64:79], a[240:243], v[244:247], v[64:79]\n"
    "v_cvt_pk_bf16_f32 v231, v250, v251\n"
    "v_exp_f32 v248, v192\n"
    "v_cmp_lt_f32 vcc, v26, v25\n"
    "ds_read_b64_tr_b16 a[240:241], v7 offset:4096\n"
    "v_mfma_f32_32x32x16_bf16 v[112:127], a[244:247], v[244:247], v[112:127]\n"
    "v_exp_f32 v249, v193\n"
    "s_cbranch_vccnz .L_stub9\n"
    ".L_ret9:\n"
    "v_add_f32 v21, v21, v25\n"
    "ds_read_b64_tr_b16 a[244:245], v7 offset:4352\n"
    "v_mfma_f32_32x32x16_bf16 a[32:47], a[248:251], v[244:247], a[32:47]\n"
    "v_exp_f32 v250, v194\n"
    "v_add_f32 v24, v248, v249\n"
    "v_cvt_pk_bf16_f32 v232, v248, v249\n"
    "ds_read_b64_tr_b16 a[248:249], v8 offset:4096\n"
    "v_mfma_f32_32x32x16_bf16 a[80:95], a[252:255], v[244:247], a[80:95]\n"
    "v_exp_f32 v251, v195\n"
    "v_add_f32 v24, v24, v250\n"
    "ds_read_b64_tr_b16 a[252:253], v8 offset:4352\n"
    "v_mfma_f32_32x32x16_bf16 v[208:223], a[192:195], a[160:163], v[160:175]\n"
    "v_exp_f32 v248, v196\n"
    "v_add_f32 v24, v24, v251\n"
    "v_cvt_pk_bf16_f32 v233, v250, v251\n"
    "ds_read_b64_tr_b16 a[226:227], v7 offset:2048\n"
    "v_mfma_f32_32x32x16_bf16 v[208:223], a[196:199], a[164:167], v[208:223]\n"
    "ds_read_b64_tr_b16 a[230:231], v7 offset:2304\n"
    "s_add_u32 m0, s48, 16384\n"
    "s_add_u32 s43, s48, 81920\n"
    "buffer_load_dwordx4 v9, s[24:27], s43 offen nt lds\n"
    "s_lshr_b32 s94, %[bid], 3\n"
    "v_mfma_f32_32x32x16_bf16 v[208:223], a[200:203], a[168:171], v[208:223]\n"
    "v_exp_f32 v249, v197\n"
    "v_add_f32 v24, v24, v248\n"
    "ds_read_b64_tr_b16 a[234:235], v8 offset:2048\n"
    "s_and_b32 s92, %[bid], 7\n"
    "v_mfma_f32_32x32x16_bf16 v[208:223], a[204:207], a[172:175], v[208:223]\n"
    "v_exp_f32 v250, v198\n"
    "v_add_f32 v24, v24, v249\n"
    "ds_read_b64_tr_b16 a[238:239], v8 offset:2304\n"
    "s_mul_i32 s85, s94, 43\n"
    "v_mfma_f32_32x32x16_bf16 v[208:223], a[208:211], a[176:179], v[208:223]\n"
    "v_cvt_pk_bf16_f32 v234, v248, v249\n"
    "v_exp_f32 v251, v199\n"
    "ds_read_b64_tr_b16 a[242:243], v7 offset:6144\n"
    "s_lshr_b32 s85, s85, 8\n"
    "v_mfma_f32_32x32x16_bf16 v[208:223], a[212:215], a[180:183], v[208:223]\n"
    "v_add_f32 v24, v24, v250\n"
    "v_exp_f32 v248, v200\n"
    "ds_read_b64_tr_b16 a[246:247], v7 offset:6400\n"
    "s_mul_i32 s93, s85, 6\n"
    "v_mfma_f32_32x32x16_bf16 v[208:223], a[216:219], a[184:187], v[208:223]\n"
    "v_add_f32 v24, v24, v251\n"
    "v_cvt_pk_bf16_f32 v235, v250, v251\n"
    "ds_read_b64_tr_b16 a[250:251], v8 offset:6144\n"
    "s_sub_u32 s84, s94, s93\n"
    "v_mfma_f32_32x32x16_bf16 v[208:223], a[220:223], a[188:191], v[208:223]\n"
    "ds_read_b64_tr_b16 a[254:255], v8 offset:6400\n"
    "v_add_u32 v5, 32768, v5\n"
    "v_add_u32 v6, 32768, v6\n"
    "s_waitcnt lgkmcnt(4)\n"
    "v_mfma_f32_32x32x16_bf16 v[32:47], a[224:227], v[224:227], v[32:47]\n"
    "ds_read_b128 a[192:195], v5 offset:0\n"
    "buffer_load_dwordx4 v9, s[24:27], s43 offen offset:1024 nt lds\n"
    "v_mfma_f32_32x32x16_bf16 v[80:95], a[228:231], v[224:227], v[80:95]\n"
    "v_exp_f32 v249, v201\n"
    "v_add_f32 v24, v24, v248\n"
    "ds_read_b128 a[196:199], v6 offset:0\n"
    "s_and_b32 s88, s92, 1\n"
    "v_mfma_f32_32x32x16_bf16 a[0:15], a[232:235], v[224:227], a[0:15]\n"
    "v_exp_f32 v250, v202\n"
    "v_add_f32 v24, v24, v249\n"
    "ds_read_b128 a[200:203], v5 offset:256\n"
    "s_lshr_b32 s86, s92, 1\n"
    "v_mfma_f32_32x32x16_bf16 a[48:63], a[236:239], v[224:227], a[48:63]\n"
    "v_cvt_pk_bf16_f32 v236, v248, v249\n"
    "ds_read_b128 a[204:207], v6 offset:256\n"
    "s_mul_i32 s91, s88, 30\n"
    "s_waitcnt lgkmcnt(4)\n"
    "v_mfma_f32_32x32x16_bf16 v[32:47], a[240:243], v[228:231], v[32:47]\n"
    "v_exp_f32 v251, v203\n"
    "v_add_f32 v24, v24, v250\n"
    "ds_read_b128 a[208:211], v5 offset:512\n"
    "s_add_u32 s91, s91, s94\n"
    "v_mfma_f32_32x32x16_bf16 v[80:95], a[244:247], v[228:231], v[80:95]\n"
    "v_exp_f32 v248, v204\n"
    "v_add_f32 v24, v24, v251\n"
    "ds_read_b128 a[212:215], v6 offset:512\n"
    "s_lshl_b32 s89, s36, 11\n"
    "v_mfma_f32_32x32x16_bf16 a[0:15], a[248:251], v[228:231], a[0:15]\n"
    "ds_read_b128 a[216:219], v5 offset:768\n"
    "buffer_load_dwordx4 v9, s[24:27], s43 offen offset:2048 nt lds\n"
    "s_lshl_b32 s92, s86, 10\n"
    "v_mfma_f32_32x32x16_bf16 a[48:63], a[252:255], v[228:231], a[48:63]\n"
    "v_cvt_pk_bf16_f32 v237, v250, v251\n"
    "v_exp_f32 v249, v205\n"
    "ds_read_b128 a[220:223], v6 offset:768\n"
    "s_waitcnt lgkmcnt(4)\n"
    "v_mfma_f32_32x32x16_bf16 v[176:191], a[192:195], a[96:99], v[128:143]\n"
    "v_add_f32 v24, v24, v248\n"
    "v_exp_f32 v250, v206\n"
    "v_add_f32 v24, v24, v249\n"
    "v_cvt_pk_bf16_f32 v238, v248, v249\n"
    "v_mfma_f32_32x32x16_bf16 v[176:191], a[196:199], a[100:103], v[176:191]\n"
    "v_exp_f32 v251, v207\n"
    "v_add_f32 v24, v24, v250\n"
    "v_add_f32 v24, v24, v251\n"
    "v_cvt_pk_bf16_f32 v239, v250, v251\n"
    "s_lshl_b32 s93, s36, 4\n"
    "v_mfma_f32_32x32x16_bf16 v[176:191], a[200:203], a[104:107], v[176:191]\n"
    "v_exp_f32 v248, v208\n"
    "v_cmp_lt_f32 vcc, v26, v24\n"
    "v_exp_f32 v249, v209\n"
    "s_add_u32 s92, s92, s93\n"
    "v_mfma_f32_32x32x16_bf16 v[176:191], a[204:207], a[108:111], v[176:191]\n"
    "s_cbranch_vccnz .L_stub10\n"
    ".L_ret10:\n"
    "v_add_f32 v22, v22, v24\n"
    "v_exp_f32 v250, v210\n"
    "s_mul_i32 s92, s92, 1280\n"
    "s_waitcnt lgkmcnt(0)\n"
    "v_mfma_f32_32x32x16_bf16 v[176:191], a[208:211], a[112:115], v[176:191]\n"
    "v_add_f32 v25, v248, v249\n"
    "v_cvt_pk_bf16_f32 v240, v248, v249\n"
    "v_exp_f32 v251, v211\n"
    "v_add_f32 v25, v25, v250\n"
    "s_lshl_b32 s93, s85, 8\n"
    "v_mfma_f32_32x32x16_bf16 v[176:191], a[212:215], a[116:119], v[176:191]\n"
    "v_exp_f32 v248, v212\n"
    "buffer_load_dwordx4 v9, s[24:27], s43 offen offset:3072 nt lds\n"
    "s_add_u32 s92, s92, s93\n"
    "v_mfma_f32_32x32x16_bf16 v[176:191], a[216:219], a[120:123], v[176:191]\n"
    "v_add_f32 v25, v25, v251\n"
    "v_cvt_pk_bf16_f32 v241, v250, v251\n"
    "v_exp_f32 v249, v213\n"
    "s_add_u32 s80, %[wo_lo], s92\n"
    "s_addc_u32 s81, %[wo_hi], 0\n"
    "v_mfma_f32_32x32x16_bf16 v[176:191], a[220:223], a[124:127], v[176:191]\n"
    "v_add_f32 v25, v25, v248\n"
    "v_exp_f32 v250, v214\n"
    "v_add_f32 v25, v25, v249\n"
    "v_cvt_pk_bf16_f32 v242, v248, v249\n"
    "v_mfma_f32_32x32x16_bf16 v[48:63], a[224:227], v[232:235], v[48:63]\n"
    "v_exp_f32 v251, v215\n"
    "v_add_f32 v25, v25, v250\n"
    "v_exp_f32 v248, v216\n"
    "v_add_f32 v25, v25, v251\n"
    "v_mfma_f32_32x32x16_bf16 v[96:111], a[228:231], v[232:235], v[96:111]\n"
    "v_cvt_pk_bf16_f32 v243, v250, v251\n"
    "v_exp_f32 v249, v217\n"
    "v_add_f32 v25, v25, v248\n"
    "s_and_b32 s81, s81, 0xffff\n"
    "v_mfma_f32_32x32x16_bf16 a[16:31], a[232:235], v[232:235], a[16:31]\n"
    "v_exp_f32 v250, v218\n"
    "v_add_f32 v25, v25, v249\n"
    "v_cvt_pk_bf16_f32 v244, v248, v249\n"
    "s_sub_u32 s82, 5242880, s92\n"
    "v_mfma_f32_32x32x16_bf16 a[64:79], a[236:239], v[232:235], a[64:79]\n"
    "s_add_u32 m0, s48, 32768\n"
    "s_add_u32 s43, s48, 98304\n"
    "buffer_load_dwordx4 v9, s[20:23], s43 offen nt lds\n"
    "s_cmp_ge_u32 s94, 30\n"
    "s_cselect_b32 s82, 0, s82\n"
    "v_mfma_f32_32x32x16_bf16 v[48:63], a[240:243], v[236:239], v[48:63]\n"
    "v_exp_f32 v251, v219\n"
    "v_add_f32 v25, v25, v250\n"
    "v_exp_f32 v248, v220\n"
    "s_mov_b32 s83, 0x20000\n"
    "v_mfma_f32_32x32x16_bf16 v[96:111], a[244:247], v[236:239], v[96:111]\n"
    "v_add_f32 v25, v25, v251\n"
    "v_cvt_pk_bf16_f32 v245, v250, v251\n"
    "v_exp_f32 v249, v221\n"
    "v_add_f32 v25, v25, v248\n"
    "v_bfe_u32 v18, v4, 2, 2\n"
    "v_mfma_f32_32x32x16_bf16 a[16:31], a[248:251], v[236:239], a[16:31]\n"
    "v_exp_f32 v250, v222\n"
    "v_add_f32 v25, v25, v249\n"
    "v_cvt_pk_bf16_f32 v246, v248, v249\n"
    "v_lshrrev_b32 v19, 5, v4\n"
    "v_mfma_f32_32x32x16_bf16 a[64:79], a[252:255], v[236:239], a[64:79]\n"
    "v_exp_f32 v251, v223\n"
    "v_add_f32 v25, v25, v250\n"
    "v_add_f32 v25, v25, v251\n"
    "v_cvt_pk_bf16_f32 v247, v250, v251\n"
    "v_mfma_f32_32x32x16_bf16 v[192:207], a[192:195], a[128:131], v[144:159]\n"
    "v_exp_f32 v248, v176\n"
    "v_cmp_lt_f32 vcc, v26, v25\n"
    "v_exp_f32 v249, v177\n"
    "s_cbranch_vccnz .L_stub11\n"
    ".L_ret11:\n"
    "v_mfma_f32_32x32x16_bf16 v[192:207], a[196:199], a[132:135], v[192:207]\n"
    "v_add_f32 v23, v23, v25\n"
    "v_exp_f32 v250, v178\n"
    "v_add_f32 v24, v248, v249\n"
    "v_cvt_pk_bf16_f32 v224, v248, v249\n"
    "v_lshl_add_u32 v18, v19, 2, v18\n"
    "v_mfma_f32_32x32x16_bf16 v[192:207], a[200:203], a[136:139], v[192:207]\n"
    "v_exp_f32 v251, v179\n"
    "buffer_load_dwordx4 v10, s[20:23], s43 offen offset:1024 nt lds\n"
    "v_mul_u32_u24 v18, 1280, v18\n"
    "v_mfma_f32_32x32x16_bf16 v[192:207], a[204:207], a[140:143], v[192:207]\n"
    "v_add_f32 v24, v24, v250\n"
    "v_exp_f32 v248, v180\n"
    "v_add_f32 v24, v24, v251\n"
    "v_cvt_pk_bf16_f32 v225, v250, v251\n"
    "v_bfe_u32 v19, v4, 4, 1\n"
    "v_mfma_f32_32x32x16_bf16 v[192:207], a[208:211], a[144:147], v[192:207]\n"
    "v_exp_f32 v249, v181\n"
    "v_add_f32 v24, v24, v248\n"
    "v_exp_f32 v250, v182\n"
    "v_lshl_add_u32 v18, v19, 6, v18\n"
    "v_mfma_f32_32x32x16_bf16 v[192:207], a[212:215], a[148:151], v[192:207]\n"
    "v_add_f32 v24, v24, v249\n"
    "v_cvt_pk_bf16_f32 v226, v248, v249\n"
    "v_exp_f32 v251, v183\n"
    "v_add_f32 v24, v24, v250\n"
    "v_and_b32 v19, 3, v4\n"
    "v_mfma_f32_32x32x16_bf16 v[192:207], a[216:219], a[152:155], v[192:207]\n"
    "v_exp_f32 v248, v184\n"
    "v_add_f32 v24, v24, v251\n"
    "v_cvt_pk_bf16_f32 v227, v250, v251\n"
    "v_lshl_add_u32 v20, v19, 4, v18\n"
    "v_mfma_f32_32x32x16_bf16 v[192:207], a[220:223], a[156:159], v[192:207]\n"
    "v_exp_f32 v249, v185\n"
    "v_add_f32 v24, v24, v248\n"
    "v_subrev_u32 v7, 114688, v7\n"
    "v_subrev_u32 v8, 114688, v8\n"
    "v_mfma_f32_32x32x16_bf16 v[64:79], a[224:227], v[240:243], v[64:79]\n"
    "s_waitcnt vmcnt(6)\n"
    "s_barrier\n"
    "ds_read_b64_tr_b16 a[224:225], v7 offset:0\n"
    "v_mfma_f32_32x32x16_bf16 v[112:127], a[228:231], v[240:243], v[112:127]\n"
    "ds_read_b64_tr_b16 a[228:229], v7 offset:256\n"
    "buffer_load_dwordx4 v11, s[20:23], s43 offen offset:2048 nt lds\n"
    "v_add_u32 v31, 9216, v20\n"
    "v_mfma_f32_32x32x16_bf16 a[32:47], a[232:235], v[240:243], a[32:47]\n"
    "v_exp_f32 v250, v186\n"
    "v_add_f32 v24, v24, v249\n"
    "ds_read_b64_tr_b16 a[232:233], v8 offset:0\n"
    "s_lshr_b32 s92, s86, 1\n"
    "v_mfma_f32_32x32x16_bf16 a[80:95], a[236:239], v[240:243], a[80:95]\n"
    "v_cvt_pk_bf16_f32 v228, v248, v249\n"
    "v_exp_f32 v251, v187\n"
    "ds_read_b64_tr_b16 a[236:237], v8 offset:256\n"
    "s_lshl_b32 s90, s92, 7\n"
    "v_mfma_f32_32x32x16_bf16 v[64:79], a[240:243], v[244:247], v[64:79]\n"
    "v_add_f32 v24, v24, v250\n"
    "v_exp_f32 v248, v188\n"
    "ds_read_b64_tr_b16 a[240:241], v7 offset:4096\n"
    "s_xor_b32 s87, s90, 128\n"
    "v_mfma_f32_32x32x16_bf16 v[112:127], a[244:247], v[244:247], v[112:127]\n"
    "v_add_f32 v24, v24, v251\n"
    "v_cvt_pk_bf16_f32 v229, v250, v251\n"
    "v_exp_f32 v249, v189\n"
    "ds_read_b64_tr_b16 a[244:245], v7 offset:4352\n"
    "v_mfma_f32_32x32x16_bf16 a[32:47], a[248:251], v[244:247], a[32:47]\n"
    "v_add_f32 v24, v24, v248\n"
    "v_exp_f32 v250, v190\n"
    "v_add_f32 v24, v24, v249\n"
    "ds_read_b64_tr_b16 a[248:249], v8 offset:4096\n"
    "v_mfma_f32_32x32x16_bf16 a[80:95], a[252:255], v[244:247], a[80:95]\n"
    "v_cvt_pk_bf16_f32 v230, v248, v249\n"
    "ds_read_b64_tr_b16 a[252:253], v8 offset:4352\n"
    "buffer_load_dwordx4 v12, s[20:23], s43 offen offset:3072 nt lds\n"
    "v_mfma_f32_32x32x16_bf16 v[208:223], a[192:195], a[160:163], v[160:175]\n"
    "v_exp_f32 v251, v191\n"
    "v_add_f32 v24, v24, v250\n"
    "v_add_f32 v24, v24, v251\n"
    "ds_read_b64_tr_b16 a[226:227], v7 offset:2048\n"
    "v_mfma_f32_32x32x16_bf16 v[208:223], a[196:199], a[164:167], v[208:223]\n"
    "v_cvt_pk_bf16_f32 v231, v250, v251\n"
    "v_exp_f32 v248, v192\n"
    "v_cmp_lt_f32 vcc, v26, v24\n"
    "ds_read_b64_tr_b16 a[230:231], v7 offset:2304\n"
    "v_mfma_f32_32x32x16_bf16 v[208:223], a[200:203], a[168:171], v[208:223]\n"
    "v_exp_f32 v249, v193\n"
    "s_cbranch_vccnz .L_stub12\n"
    ".L_ret12:\n"
    "v_add_f32 v21, v21, v24\n"
    "ds_read_b64_tr_b16 a[234:235], v8 offset:2048\n"
    "v_mfma_f32_32x32x16_bf16 v[208:223], a[204:207], a[172:175], v[208:223]\n"
    "v_exp_f32 v250, v194\n"
    "v_add_f32 v25, v248, v249\n"
    "v_cvt_pk_bf16_f32 v232, v248, v249\n"
    "ds_read_b64_tr_b16 a[238:239], v8 offset:2304\n"
    "v_mfma_f32_32x32x16_bf16 v[208:223], a[208:211], a[176:179], v[208:223]\n"
    "v_exp_f32 v251, v195\n"
    "v_add_f32 v25, v25, v250\n"
    "ds_read_b64_tr_b16 a[242:243], v7 offset:6144\n"
    "v_mfma_f32_32x32x16_bf16 v[208:223], a[212:215], a[180:183], v[208:223]\n"
    "v_exp_f32 v248, v196\n"
    "v_add_f32 v25, v25, v251\n"
    "v_cvt_pk_bf16_f32 v233, v250, v251\n"
    "ds_read_b64_tr_b16 a[246:247], v7 offset:6400\n"
    "v_mfma_f32_32x32x16_bf16 v[208:223], a[216:219], a[184:187], v[208:223]\n"
    "ds_read_b64_tr_b16 a[250:251], v8 offset:6144\n"
    "s_add_u32 m0, s48, 49152\n"
    "s_add_u32 s43, s48, 98304\n"
    "buffer_load_dwordx4 v9, s[24:27], s43 offen nt lds\n"
    "v_mfma_f32_32x32x16_bf16 v[208:223], a[220:223], a[188:191], v[208:223]\n"
    "ds_read_b64_tr_b16 a[254:255], v8 offset:6400\n"
    "v_add_u32 v5, 16384, v5\n"
    "v_add_u32 v6, 16384, v6\n"
    "s_waitcnt lgkmcnt(4)\n"
    "v_mfma_f32_32x32x16_bf16 v[32:47], a[224:227], v[224:227], v[32:47]\n"
    "v_exp_f32 v249, v197\n"
    "v_add_f32 v25, v25, v248\n"
    "ds_read_b128 a[192:195], v5 offset:0\n"
    "v_mfma_f32_32x32x16_bf16 v[80:95], a[228:231], v[224:227], v[80:95]\n"
    "v_exp_f32 v250, v198\n"
    "v_add_f32 v25, v25, v249\n"
    "v_cvt_pk_bf16_f32 v234, v248, v249\n"
    "ds_read_b128 a[196:199], v6 offset:0\n"
    "v_mfma_f32_32x32x16_bf16 a[0:15], a[232:235], v[224:227], a[0:15]\n"
    "v_exp_f32 v251, v199\n"
    "v_add_f32 v25, v25, v250\n"
    "ds_read_b128 a[200:203], v5 offset:256\n"
    "v_mfma_f32_32x32x16_bf16 a[48:63], a[236:239], v[224:227], a[48:63]\n"
    "v_exp_f32 v248, v200\n"
    "v_add_f32 v25, v25, v251\n"
    "ds_read_b128 a[204:207], v6 offset:256\n"
    "s_waitcnt lgkmcnt(4)\n"
    "v_mfma_f32_32x32x16_bf16 v[32:47], a[240:243], v[228:231], v[32:47]\n"
    "v_cvt_pk_bf16_f32 v235, v250, v251\n"
    "ds_read_b128 a[208:211], v5 offset:512\n"
    "buffer_load_dwordx4 v9, s[24:27], s43 offen offset:1024 nt lds\n"
    "v_mfma_f32_32x32x16_bf16 v[80:95], a[244:247], v[228:231], v[80:95]\n"
    "v_exp_f32 v249, v201\n"
    "v_add_f32 v25, v25, v248\n"
    "ds_read_b128 a[212:215], v6 offset:512\n"
    "v_mfma_f32_32x32x16_bf16 a[0:15], a[248:251], v[228:231], a[0:15]\n"
    "v_exp_f32 v250, v202\n"
    "v_add_f32 v25, v25, v249\n"
    "v_cvt_pk_bf16_f32 v236, v248, v249\n"
    "ds_read_b128 a[216:219], v5 offset:768\n"
    "v_mfma_f32_32x32x16_bf16 a[48:63], a[252:255], v[228:231], a[48:63]\n"
    "v_exp_f32 v251, v203\n"
    "v_add_f32 v25, v25, v250\n"
    "ds_read_b128 a[220:223], v6 offset:768\n"
    "s_waitcnt lgkmcnt(4)\n"
    "v_mfma_f32_32x32x16_bf16 v[176:191], a[192:195], a[96:99], v[128:143]\n"
    "v_exp_f32 v248, v204\n"
    "v_add_f32 v25, v25, v251\n"
    "v_cvt_pk_bf16_f32 v237, v250, v251\n"
    "v_exp_f32 v249, v205\n"
    "v_mfma_f32_32x32x16_bf16 v[176:191], a[196:199], a[100:103], v[176:191]\n"
    "v_add_f32 v25, v25, v248\n"
    "v_exp_f32 v250, v206\n"
    "v_add_f32 v25, v25, v249\n"
    "v_cvt_pk_bf16_f32 v238, v248, v249\n"
    "v_mfma_f32_32x32x16_bf16 v[176:191], a[200:203], a[104:107], v[176:191]\n"
    "v_exp_f32 v251, v207\n"
    "v_add_f32 v25, v25, v250\n"
    "v_add_f32 v25, v25, v251\n"
    "v_cvt_pk_bf16_f32 v239, v250, v251\n"
    "v_mfma_f32_32x32x16_bf16 v[176:191], a[204:207], a[108:111], v[176:191]\n"
    "v_exp_f32 v248, v208\n"
    "buffer_load_dwordx4 v9, s[24:27], s43 offen offset:2048 nt lds\n"
    "s_waitcnt lgkmcnt(0)\n"
    "v_mfma_f32_32x32x16_bf16 v[176:191], a[208:211], a[112:115], v[176:191]\n"
    "v_cmp_lt_f32 vcc, v26, v25\n"
    "v_exp_f32 v249, v209\n"
    "s_cbranch_vccnz .L_stub13\n"
    ".L_ret13:\n"
    "v_add_f32 v22, v22, v25\n"
    "v_mfma_f32_32x32x16_bf16 v[176:191], a[212:215], a[116:119], v[176:191]\n"
    "v_exp_f32 v250, v210\n"
    "v_add_f32 v24, v248, v249\n"
    "v_cvt_pk_bf16_f32 v240, v248, v249\n"
    "v_exp_f32 v251, v211\n"
    "v_mfma_f32_32x32x16_bf16 v[176:191], a[216:219], a[120:123], v[176:191]\n"
    "v_add_f32 v24, v24, v250\n"
    "v_exp_f32 v248, v212\n"
    "v_add_f32 v24, v24, v251\n"
    "v_cvt_pk_bf16_f32 v241, v250, v251\n"
    "v_mfma_f32_32x32x16_bf16 v[176:191], a[220:223], a[124:127], v[176:191]\n"
    "v_exp_f32 v249, v213\n"
    "v_add_f32 v24, v24, v248\n"
    "v_exp_f32 v250, v214\n"
    "v_add_f32 v24, v24, v249\n"
    "v_mfma_f32_32x32x16_bf16 v[48:63], a[224:227], v[232:235], v[48:63]\n"
    "v_cvt_pk_bf16_f32 v242, v248, v249\n"
    "v_exp_f32 v251, v215\n"
    "v_add_f32 v24, v24, v250\n"
    "v_exp_f32 v248, v216\n"
    "v_mfma_f32_32x32x16_bf16 v[96:111], a[228:231], v[232:235], v[96:111]\n"
    "v_add_f32 v24, v24, v251\n"
    "v_cvt_pk_bf16_f32 v243, v250, v251\n"
    "v_exp_f32 v249, v217\n"
    "v_add_f32 v24, v24, v248\n"
    "v_mfma_f32_32x32x16_bf16 a[16:31], a[232:235], v[232:235], a[16:31]\n"
    "v_exp_f32 v250, v218\n"
    "v_add_f32 v24, v24, v249\n"
    "buffer_load_dwordx4 v9, s[24:27], s43 offen offset:3072 nt lds\n"
    "v_mfma_f32_32x32x16_bf16 a[64:79], a[236:239], v[232:235], a[64:79]\n"
    "v_cvt_pk_bf16_f32 v244, v248, v249\n"
    "v_exp_f32 v251, v219\n"
    "v_add_f32 v24, v24, v250\n"
    "v_exp_f32 v248, v220\n"
    "v_mfma_f32_32x32x16_bf16 v[48:63], a[240:243], v[236:239], v[48:63]\n"
    "v_add_f32 v24, v24, v251\n"
    "v_cvt_pk_bf16_f32 v245, v250, v251\n"
    "v_exp_f32 v249, v221\n"
    "v_add_f32 v24, v24, v248\n"
    "v_mfma_f32_32x32x16_bf16 v[96:111], a[244:247], v[236:239], v[96:111]\n"
    "v_exp_f32 v250, v222\n"
    "v_add_f32 v24, v24, v249\n"
    "v_cvt_pk_bf16_f32 v246, v248, v249\n"
    "v_exp_f32 v251, v223\n"
    "v_mfma_f32_32x32x16_bf16 a[16:31], a[248:251], v[236:239], a[16:31]\n"
    "v_add_f32 v24, v24, v250\n"
    "v_add_f32 v24, v24, v251\n"
    "v_cvt_pk_bf16_f32 v247, v250, v251\n"
    "v_exp_f32 v248, v176\n"
    "v_cmp_lt_f32 vcc, v26, v24\n"
    "v_mfma_f32_32x32x16_bf16 a[64:79], a[252:255], v[236:239], a[64:79]\n"
    "v_exp_f32 v249, v177\n"
    "s_cbranch_vccnz .L_stub14\n"
    ".L_ret14:\n"
    "v_add_f32 v23, v23, v24\n"
    "v_exp_f32 v250, v178\n"
    "v_mfma_f32_32x32x16_bf16 v[192:207], a[192:195], a[128:131], v[144:159]\n"
    "v_add_f32 v25, v248, v249\n"
    "s_add_u32 m0, s48, 65536\n"
    "s_add_u32 s43, s48, 114688\n"
    "buffer_load_dwordx4 v9, s[20:23], s43 offen nt lds\n"
    "v_mfma_f32_32x32x16_bf16 v[192:207], a[196:199], a[132:135], v[192:207]\n"
    "v_cvt_pk_bf16_f32 v224, v248, v249\n"
    "v_exp_f32 v251, v179\n"
    "v_add_f32 v25, v25, v250\n"
    "v_exp_f32 v248, v180\n"
    "v_mfma_f32_32x32x16_bf16 v[192:207], a[200:203], a[136:139], v[192:207]\n"
    "v_add_f32 v25, v25, v251\n"
    "v_cvt_pk_bf16_f32 v225, v250, v251\n"
    "v_exp_f32 v249, v181\n"
    "v_add_f32 v25, v25, v248\n"
    "v_mfma_f32_32x32x16_bf16 v[192:207], a[204:207], a[140:143], v[192:207]\n"
    "v_exp_f32 v250, v182\n"
    "v_add_f32 v25, v25, v249\n"
    "v_cvt_pk_bf16_f32 v226, v248, v249\n"
    "v_exp_f32 v251, v183\n"
    "v_mfma_f32_32x32x16_bf16 v[192:207], a[208:211], a[144:147], v[192:207]\n"
    "v_add_f32 v25, v25, v250\n"
    "v_exp_f32 v248, v184\n"
    "v_add_f32 v25, v25, v251\n"
    "v_cvt_pk_bf16_f32 v227, v250, v251\n"
    "v_mfma_f32_32x32x16_bf16 v[192:207], a[212:215], a[148:151], v[192:207]\n"
    "v_exp_f32 v249, v185\n"
    "v_add_f32 v25, v25, v248\n"
    "v_exp_f32 v250, v186\n"
    "v_add_f32 v25, v25, v249\n"
    "v_mfma_f32_32x32x16_bf16 v[192:207], a[216:219], a[152:155], v[192:207]\n"
    "v_cvt_pk_bf16_f32 v228, v248, v249\n"
    "v_exp_f32 v251, v187\n"
    "v_add_f32 v25, v25, v250\n"
    "v_exp_f32 v248, v188\n"
    "v_mfma_f32_32x32x16_bf16 v[192:207], a[220:223], a[156:159], v[192:207]\n"
    "v_add_f32 v25, v25, v251\n"
    "v_add_u32 v7, 16384, v7\n"
    "v_add_u32 v8, 16384, v8\n"
    "buffer_load_dwordx4 v10, s[20:23], s43 offen offset:1024 nt lds\n"
    "v_mfma_f32_32x32x16_bf16 v[64:79], a[224:227], v[240:243], v[64:79]\n"
    "s_waitcnt vmcnt(6)\n"
    "s_barrier\n"
    "ds_read_b64_tr_b16 a[224:225], v7 offset:0\n"
    "v_mfma_f32_32x32x16_bf16 v[112:127], a[228:231], v[240:243], v[112:127]\n"
    "v_cvt_pk_bf16_f32 v229, v250, v251\n"
    "v_exp_f32 v249, v189\n"
    "v_add_f32 v25, v25, v248\n"
    "ds_read_b64_tr_b16 a[228:229], v7 offset:256\n"
    "v_mfma_f32_32x32x16_bf16 a[32:47], a[232:235], v[240:243], a[32:47]\n"
    "v_exp_f32 v250, v190\n"
    "v_add_f32 v25, v25, v249\n"
    "v_cvt_pk_bf16_f32 v230, v248, v249\n"
    "ds_read_b64_tr_b16 a[232:233], v8 offset:0\n"
    "v_mfma_f32_32x32x16_bf16 a[80:95], a[236:239], v[240:243], a[80:95]\n"
    "v_exp_f32 v251, v191\n"
    "v_add_f32 v25, v25, v250\n"
    "v_add_f32 v25, v25, v251\n"
    "ds_read_b64_tr_b16 a[236:237], v8 offset:256\n"
    "v_mfma_f32_32x32x16_bf16 v[64:79], a[240:243], v[244:247], v[64:79]\n"
    "v_cvt_pk_bf16_f32 v231, v250, v251\n"
    "v_exp_f32 v248, v192\n"
    "v_cmp_lt_f32 vcc, v26, v25\n"
    "ds_read_b64_tr_b16 a[240:241], v7 offset:4096\n"
    "v_mfma_f32_32x32x16_bf16 v[112:127], a[244:247], v[244:247], v[112:127]\n"
    "ds_read_b64_tr_b16 a[244:245], v7 offset:4352\n"
    "buffer_load_dwordx4 v11, s[20:23], s43 offen offset:2048 nt lds\n"
    "v_mfma_f32_32x32x16_bf16 a[32:47], a[248:251], v[244:247], a[32:47]\n"
    "v_exp_f32 v249, v193\n"
    "s_cbranch_vccnz .L_stub15\n"
    ".L_ret15:\n"
    "v_add_f32 v21, v21, v25\n"
    "ds_read_b64_tr_b16 a[248:249], v8 offset:4096\n"
    "v_mfma_f32_32x32x16_bf16 a[80:95], a[252:255], v[244:247], a[80:95]\n"
    "v_exp_f32 v250, v194\n"
    "v_add_f32 v24, v248, v249\n"
    "v_cvt_pk_bf16_f32 v232, v248, v249\n"
    "ds_read_b64_tr_b16 a[252:253], v8 offset:4352\n"
    "v_mfma_f32_32x32x16_bf16 v[208:223], a[192:195], a[160:163], v[160:175]\n"
    "v_exp_f32 v251, v195\n"
    "v_add_f32 v24, v24, v250\n"
    "ds_read_b64_tr_b16 a[226:227], v7 offset:2048\n"
    "v_mfma_f32_32x32x16_bf16 v[208:223], a[196:199], a[164:167], v[208:223]\n"
    "v_exp_f32 v248, v196\n"
    "v_add_f32 v24, v24, v251\n"
    "v_cvt_pk_bf16_f32 v233, v250, v251\n"
    "ds_read_b64_tr_b16 a[230:231], v7 offset:2304\n"
    "v_mfma_f32_32x32x16_bf16 v[208:223], a[200:203], a[168:171], v[208:223]\n"
    "v_exp_f32 v249, v197\n"
    "v_add_f32 v24, v24, v248\n"
    "ds_read_b64_tr_b16 a[234:235], v8 offset:2048\n"
    "v_mfma_f32_32x32x16_bf16 v[208:223], a[204:207], a[172:175], v[208:223]\n"
    "v_exp_f32 v250, v198\n"
    "v_add_f32 v24, v24, v249\n"
    "v_cvt_pk_bf16_f32 v234, v248, v249\n"
    "ds_read_b64_tr_b16 a[238:239], v8 offset:2304\n"
    "v_mfma_f32_32x32x16_bf16 v[208:223], a[208:211], a[176:179], v[208:223]\n"
    "ds_read_b64_tr_b16 a[242:243], v7 offset:6144\n"
    "buffer_load_dwordx4 v12, s[20:23], s43 offen offset:3072 nt lds\n"
    "v_mfma_f32_32x32x16_bf16 v[208:223], a[212:215], a[180:183], v[208:223]\n"
    "v_exp_f32 v251, v199\n"
    "v_add_f32 v24, v24, v250\n"
    "ds_read_b64_tr_b16 a[246:247], v7 offset:6400\n"
    "v_mfma_f32_32x32x16_bf16 v[208:223], a[216:219], a[184:187], v[208:223]\n"
    "v_exp_f32 v248, v200\n"
    "v_add_f32 v24, v24, v251\n"
    "v_cvt_pk_bf16_f32 v235, v250, v251\n"
    "ds_read_b64_tr_b16 a[250:251], v8 offset:6144\n"
    "v_mfma_f32_32x32x16_bf16 v[208:223], a[220:223], a[188:191], v[208:223]\n"
    "ds_read_b64_tr_b16 a[254:255], v8 offset:6400\n"
    "v_subrev_u32 v5, 114688, v5\n"
    "v_subrev_u32 v6, 114688, v6\n"
    "s_waitcnt lgkmcnt(4)\n"
    "v_mfma_f32_32x32x16_bf16 v[32:47], a[224:227], v[224:227], v[32:47]\n"
    "v_exp_f32 v249, v201\n"
    "v_add_f32 v24, v24, v248\n"
    "ds_read_b128 a[192:195], v5 offset:0\n"
    "v_mfma_f32_32x32x16_bf16 v[80:95], a[228:231], v[224:227], v[80:95]\n"
    "v_exp_f32 v250, v202\n"
    "v_add_f32 v24, v24, v249\n"
    "v_cvt_pk_bf16_f32 v236, v248, v249\n"
    "ds_read_b128 a[196:199], v6 offset:0\n"
    "v_mfma_f32_32x32x16_bf16 a[0:15], a[232:235], v[224:227], a[0:15]\n"
    "v_exp_f32 v251, v203\n"
    "v_add_f32 v24, v24, v250\n"
    "ds_read_b128 a[200:203], v5 offset:256\n"
    "v_mfma_f32_32x32x16_bf16 a[48:63], a[236:239], v[224:227], a[48:63]\n"
    "ds_read_b128 a[204:207], v6 offset:256\n"
    "s_add_u32 m0, s48, 81920\n"
    "s_add_u32 s43, s48, 114688\n"
    "buffer_load_dwordx4 v9, s[24:27], s43 offen nt lds\n"
    "s_waitcnt lgkmcnt(4)\n"
    "v_mfma_f32_32x32x16_bf16 v[32:47], a[240:243], v[228:231], v[32:47]\n"
    "v_exp_f32 v248, v204\n"
    "v_add_f32 v24, v24, v251\n"
    "v_cvt_pk_bf16_f32 v237, v250, v251\n"
    "ds_read_b128 a[208:211], v5 offset:512\n"
    "v_mfma_f32_32x32x16_bf16 v[80:95], a[244:247], v[228:231], v[80:95]\n"
    "v_exp_f32 v249, v205\n"
    "v_add_f32 v24, v24, v248\n"
    "ds_read_b128 a[212:215], v6 offset:512\n"
    "v_mfma_f32_32x32x16_bf16 a[0:15], a[248:251], v[228:231], a[0:15]\n"
    "v_exp_f32 v250, v206\n"
    "v_add_f32 v24, v24, v249\n"
    "v_cvt_pk_bf16_f32 v238, v248, v249\n"
    "ds_read_b128 a[216:219], v5 offset:768\n"
    "v_mfma_f32_32x32x16_bf16 a[48:63], a[252:255], v[228:231], a[48:63]\n"
    "v_exp_f32 v251, v207\n"
    "v_add_f32 v24, v24, v250\n"
    "ds_read_b128 a[220:223], v6 offset:768\n"
    "s_waitcnt lgkmcnt(4)\n"
    "v_mfma_f32_32x32x16_bf16 v[176:191], a[192:195], a[96:99], v[128:143]\n"
    "v_add_f32 v24, v24, v251\n"
    "v_cvt_pk_bf16_f32 v239, v250, v251\n"
    "v_exp_f32 v248, v208\n"
    "v_cmp_lt_f32 vcc, v26, v24\n"
    "v_mfma_f32_32x32x16_bf16 v[176:191], a[196:199], a[100:103], v[176:191]\n"
    "v_exp_f32 v249, v209\n"
    "s_cbranch_vccnz .L_stub16\n"
    ".L_ret16:\n"
    "buffer_load_dwordx4 v9, s[24:27], s43 offen offset:1024 nt lds\n"
    "v_mfma_f32_32x32x16_bf16 v[176:191], a[200:203], a[104:107], v[176:191]\n"
    "v_add_f32 v22, v22, v24\n"
    "v_exp_f32 v250, v210\n"
    "v_add_f32 v25, v248, v249\n"
    "v_cvt_pk_bf16_f32 v240, v248, v249\n"
    "v_mfma_f32_32x32x16_bf16 v[176:191], a[204:207], a[108:111], v[176:191]\n"
    "v_exp_f32 v251, v211\n"
    "v_add_f32 v25, v25, v250\n"
    "v_exp_f32 v248, v212\n"
    "s_waitcnt lgkmcnt(0)\n"
    "v_mfma_f32_32x32x16_bf16 v[176:191], a[208:211], a[112:115], v[176:191]\n"
    "v_add_f32 v25, v25, v251\n"
    "v_cvt_pk_bf16_f32 v241, v250, v251\n"
    "v_exp_f32 v249, v213\n"
    "v_add_f32 v25, v25, v248\n"
    "v_mfma_f32_32x32x16_bf16 v[176:191], a[212:215], a[116:119], v[176:191]\n"
    "v_exp_f32 v250, v214\n"
    "v_add_f32 v25, v25, v249\n"
    "v_cvt_pk_bf16_f32 v242, v248, v249\n"
    "v_exp_f32 v251, v215\n"
    "v_mfma_f32_32x32x16_bf16 v[176:191], a[216:219], a[120:123], v[176:191]\n"
    "v_add_f32 v25, v25, v250\n"
    "v_exp_f32 v248, v216\n"
    "v_add_f32 v25, v25, v251\n"
    "v_cvt_pk_bf16_f32 v243, v250, v251\n"
    "v_mfma_f32_32x32x16_bf16 v[176:191], a[220:223], a[124:127], v[176:191]\n"
    "v_exp_f32 v249, v217\n"
    "v_add_f32 v25, v25, v248\n"
    "v_exp_f32 v250, v218\n"
    "v_add_f32 v25, v25, v249\n"
    "v_mfma_f32_32x32x16_bf16 v[48:63], a[224:227], v[232:235], v[48:63]\n"
    "v_cvt_pk_bf16_f32 v244, v248, v249\n"
    "v_exp_f32 v251, v219\n"
    "buffer_load_dwordx4 v9, s[24:27], s43 offen offset:2048 nt lds\n"
    "v_mfma_f32_32x32x16_bf16 v[96:111], a[228:231], v[232:235], v[96:111]\n"
    "v_add_f32 v25, v25, v250\n"
    "v_exp_f32 v248, v220\n"
    "v_add_f32 v25, v25, v251\n"
    "v_cvt_pk_bf16_f32 v245, v250, v251\n"
    "v_mfma_f32_32x32x16_bf16 a[16:31], a[232:235], v[232:235], a[16:31]\n"
    "v_exp_f32 v249, v221\n"
    "v_add_f32 v25, v25, v248\n"
    "v_exp_f32 v250, v222\n"
    "v_add_f32 v25, v25, v249\n"
    "v_mfma_f32_32x32x16_bf16 a[64:79], a[236:239], v[232:235], a[64:79]\n"
    "v_cvt_pk_bf16_f32 v246, v248, v249\n"
    "v_exp_f32 v251, v223\n"
    "v_add_f32 v25, v25, v250\n"
    "v_add_f32 v25, v25, v251\n"
    "v_cvt_pk_bf16_f32 v247, v250, v251\n"
    "v_mfma_f32_32x32x16_bf16 v[48:63], a[240:243], v[236:239], v[48:63]\n"
    "v_exp_f32 v248, v176\n"
    "v_cmp_lt_f32 vcc, v26, v25\n"
    "v_exp_f32 v249, v177\n"
    "s_cbranch_vccnz .L_stub17\n"
    ".L_ret17:\n"
    "v_mfma_f32_32x32x16_bf16 v[96:111], a[244:247], v[236:239], v[96:111]\n"
    "v_add_f32 v23, v23, v25\n"
    "v_exp_f32 v250, v178\n"
    "v_add_f32 v24, v248, v249\n"
    "v_cvt_pk_bf16_f32 v224, v248, v249\n"
    "v_mfma_f32_32x32x16_bf16 a[16:31], a[248:251], v[236:239], a[16:31]\n"
    "v_exp_f32 v251, v179\n"
    "v_add_f32 v24, v24, v250\n"
    "buffer_load_dwordx4 v9, s[24:27], s43 offen offset:3072 nt lds\n"
    "v_mfma_f32_32x32x16_bf16 a[64:79], a[252:255], v[236:239], a[64:79]\n"
    "v_exp_f32 v248, v180\n"
    "v_add_f32 v24, v24, v251\n"
    "v_cvt_pk_bf16_f32 v225, v250, v251\n"
    "v_exp_f32 v249, v181\n"
    "v_mfma_f32_32x32x16_bf16 v[192:207], a[192:195], a[128:131], v[144:159]\n"
    "v_add_f32 v24, v24, v248\n"
    "v_exp_f32 v250, v182\n"
    "v_add_f32 v24, v24, v249\n"
    "v_cvt_pk_bf16_f32 v226, v248, v249\n"
    "v_mfma_f32_32x32x16_bf16 v[192:207], a[196:199], a[132:135], v[192:207]\n"
    "v_exp_f32 v251, v183\n"
    "v_add_f32 v24, v24, v250\n"
    "v_exp_f32 v248, v184\n"
    "v_add_f32 v24, v24, v251\n"
    "v_mfma_f32_32x32x16_bf16 v[192:207], a[200:203], a[136:139], v[192:207]\n"
    "v_cvt_pk_bf16_f32 v227, v250, v251\n"
    "v_exp_f32 v249, v185\n"
    "v_add_f32 v24, v24, v248\n"
    "v_exp_f32 v250, v186\n"
    "v_mfma_f32_32x32x16_bf16 v[192:207], a[204:207], a[140:143], v[192:207]\n"
    "v_add_f32 v24, v24, v249\n"
    "v_cvt_pk_bf16_f32 v228, v248, v249\n"
    "v_exp_f32 v251, v187\n"
    "v_add_f32 v24, v24, v250\n"
    "v_mfma_f32_32x32x16_bf16 v[192:207], a[208:211], a[144:147], v[192:207]\n"
    "v_exp_f32 v248, v188\n"
    "v_add_f32 v24, v24, v251\n"
    "v_cvt_pk_bf16_f32 v229, v250, v251\n"
    "v_exp_f32 v249, v189\n"
    "v_mfma_f32_32x32x16_bf16 v[192:207], a[212:215], a[148:151], v[192:207]\n"
    "v_add_f32 v24, v24, v248\n"
    "v_exp_f32 v250, v190\n"
    "v_add_f32 v24, v24, v249\n"
    "v_cvt_pk_bf16_f32 v230, v248, v249\n"
    "v_mfma_f32_32x32x16_bf16 v[192:207], a[216:219], a[152:155], v[192:207]\n"
    "v_exp_f32 v251, v191\n"
    "v_add_f32 v24, v24, v250\n"
    "v_add_f32 v24, v24, v251\n"
    "v_cvt_pk_bf16_f32 v231, v250, v251\n"
    "v_mfma_f32_32x32x16_bf16 v[192:207], a[220:223], a[156:159], v[192:207]\n"
    "v_add_u32 v7, 32768, v7\n"
    "v_add_u32 v8, 32768, v8\n"
    "v_mfma_f32_32x32x16_bf16 v[64:79], a[224:227], v[240:243], v[64:79]\n"
    "s_waitcnt vmcnt(4)\n"
    "s_barrier\n"
    "ds_read_b64_tr_b16 a[224:225], v7 offset:0\n"
    "s_lshr_b32 s47, s40, 3\n"
    "v_mfma_f32_32x32x16_bf16 v[112:127], a[228:231], v[240:243], v[112:127]\n"
    "v_exp_f32 v248, v192\n"
    "v_cmp_lt_f32 vcc, v26, v24\n"
    "ds_read_b64_tr_b16 a[228:229], v7 offset:256\n"
    "s_mul_i32 s47, s47, 1310720\n"
    "v_mfma_f32_32x32x16_bf16 a[32:47], a[232:235], v[240:243], a[32:47]\n"
    "v_exp_f32 v249, v193\n"
    "s_cbranch_vccnz .L_stub18\n"
    ".L_ret18:\n"
    "ds_read_b64_tr_b16 a[232:233], v8 offset:0\n"
    "s_lshr_b32 s52, %[bid], 3\n"
    "v_mfma_f32_32x32x16_bf16 a[80:95], a[236:239], v[240:243], a[80:95]\n"
    "v_add_f32 v21, v21, v24\n"
    "v_exp_f32 v250, v194\n"
    "ds_read_b64_tr_b16 a[236:237], v8 offset:256\n"
    "s_mul_i32 s52, s52, 40960\n"
    "v_mfma_f32_32x32x16_bf16 v[64:79], a[240:243], v[244:247], v[64:79]\n"
    "v_add_f32 v25, v248, v249\n"
    "v_cvt_pk_bf16_f32 v232, v248, v249\n"
    "ds_read_b64_tr_b16 a[240:241], v7 offset:4096\n"
    "s_add_u32 s47, s47, s52\n"
    "v_mfma_f32_32x32x16_bf16 v[112:127], a[244:247], v[244:247], v[112:127]\n"
    "v_exp_f32 v251, v195\n"
    "v_add_f32 v25, v25, v250\n"
    "ds_read_b64_tr_b16 a[244:245], v7 offset:4352\n"
    "s_mul_i32 s52, s36, 10240\n"
    "v_mfma_f32_32x32x16_bf16 a[32:47], a[248:251], v[244:247], a[32:47]\n"
    "v_exp_f32 v248, v196\n"
    "v_add_f32 v25, v25, v251\n"
    "ds_read_b64_tr_b16 a[248:249], v8 offset:4096\n"
    "s_add_u32 s47, s47, s52\n"
    "v_mfma_f32_32x32x16_bf16 a[80:95], a[252:255], v[244:247], a[80:95]\n"
    "v_cvt_pk_bf16_f32 v233, v250, v251\n"
    "ds_read_b64_tr_b16 a[252:253], v8 offset:4352\n"
    "s_add_u32 s72, %[wo_lo], s47\n"
    "s_addc_u32 s73, %[wo_hi], 0\n"
    "v_mfma_f32_32x32x16_bf16 v[208:223], a[192:195], a[160:163], v[160:175]\n"
    "v_exp_f32 v249, v197\n"
    "v_add_f32 v25, v25, v248\n"
    "ds_read_b64_tr_b16 a[226:227], v7 offset:2048\n"
    "s_and_b32 s73, s73, 0xffff\n"
    "v_mfma_f32_32x32x16_bf16 v[208:223], a[196:199], a[164:167], v[208:223]\n"
    "v_exp_f32 v250, v198\n"
    "v_add_f32 v25, v25, v249\n"
    "ds_read_b64_tr_b16 a[230:231], v7 offset:2304\n"
    "s_mov_b32 s74, 10240\n"
    "v_mfma_f32_32x32x16_bf16 v[208:223], a[200:203], a[168:171], v[208:223]\n"
    "v_cvt_pk_bf16_f32 v234, v248, v249\n"
    "v_exp_f32 v251, v199\n"
    "ds_read_b64_tr_b16 a[234:235], v8 offset:2048\n"
    "s_mov_b32 s75, 0x20000\n"
    "v_mfma_f32_32x32x16_bf16 v[208:223], a[204:207], a[172:175], v[208:223]\n"
    "v_add_f32 v25, v25, v250\n"
    "v_exp_f32 v248, v200\n"
    "ds_read_b64_tr_b16 a[238:239], v8 offset:2304\n"
    "v_lshlrev_b32 v16, 7, v4\n"
    "v_mfma_f32_32x32x16_bf16 v[208:223], a[208:211], a[176:179], v[208:223]\n"
    "v_add_f32 v25, v25, v251\n"
    "v_cvt_pk_bf16_f32 v235, v250, v251\n"
    "ds_read_b64_tr_b16 a[242:243], v7 offset:6144\n"
    "s_mov_b32 s47, 8192\n"
    "v_mfma_f32_32x32x16_bf16 v[208:223], a[212:215], a[180:183], v[208:223]\n"
    "ds_read_b64_tr_b16 a[246:247], v7 offset:6400\n"
    "buffer_load_dword v12, v16, s[72:75], 0 offen\n"
    "v_mfma_f32_32x32x16_bf16 v[208:223], a[216:219], a[184:187], v[208:223]\n"
    "ds_read_b64_tr_b16 a[250:251], v8 offset:6144\n"
    "buffer_load_dword v17, v16, s[72:75], s47 offen\n"
    "v_mfma_f32_32x32x16_bf16 v[208:223], a[220:223], a[188:191], v[208:223]\n"
    "ds_read_b64_tr_b16 a[254:255], v8 offset:6400\n"
    "v_add_u32 v5, 32768, v5\n"
    "v_add_u32 v6, 32768, v6\n"
    "s_lshl_b32 s47, s36, 13\n"
    "s_waitcnt lgkmcnt(4)\n"
    "v_mfma_f32_32x32x16_bf16 v[32:47], a[224:227], v[224:227], v[32:47]\n"
    "v_exp_f32 v249, v201\n"
    "v_add_f32 v25, v25, v248\n"
    "ds_read_b128 a[192:195], v5 offset:0\n"
    "v_lshlrev_b32 v30, 4, v4\n"
    "v_mfma_f32_32x32x16_bf16 v[80:95], a[228:231], v[224:227], v[80:95]\n"
    "v_exp_f32 v250, v202\n"
    "v_add_f32 v25, v25, v249\n"
    "ds_read_b128 a[196:199], v6 offset:0\n"
    "v_add_u32 v28, s47, v30\n"
    "v_mfma_f32_32x32x16_bf16 a[0:15], a[232:235], v[224:227], a[0:15]\n"
    "v_cvt_pk_bf16_f32 v236, v248, v249\n"
    "v_exp_f32 v251, v203\n"
    "ds_read_b128 a[200:203], v5 offset:256\n"
    "v_add_u32 v27, 98304, v28\n"
    "v_mfma_f32_32x32x16_bf16 a[48:63], a[236:239], v[224:227], a[48:63]\n"
    "v_add_f32 v25, v25, v250\n"
    "ds_read_b128 a[204:207], v6 offset:256\n"
    "s_lshl_b32 s47, s36, 11\n"
    "s_waitcnt lgkmcnt(4)\n"
    "v_mfma_f32_32x32x16_bf16 v[32:47], a[240:243], v[228:231], v[32:47]\n"
    "v_exp_f32 v248, v204\n"
    "v_add_f32 v25, v25, v251\n"
    "ds_read_b128 a[208:211], v5 offset:512\n"
    "s_add_u32 s47, s47, 32768\n"
    "v_mfma_f32_32x32x16_bf16 v[80:95], a[244:247], v[228:231], v[80:95]\n"
    "v_cvt_pk_bf16_f32 v237, v250, v251\n"
    "v_exp_f32 v249, v205\n"
    "ds_read_b128 a[212:215], v6 offset:512\n"
    "v_lshlrev_b32 v29, 3, v4\n"
    "v_mfma_f32_32x32x16_bf16 a[0:15], a[248:251], v[228:231], a[0:15]\n"
    "v_add_f32 v25, v25, v248\n"
    "v_exp_f32 v250, v206\n"
    "ds_read_b128 a[216:219], v5 offset:768\n"
    "v_add_u32 v29, s47, v29\n"
    "v_mfma_f32_32x32x16_bf16 a[48:63], a[252:255], v[228:231], a[48:63]\n"
    "v_add_f32 v25, v25, v249\n"
    "v_cvt_pk_bf16_f32 v238, v248, v249\n"
    "ds_read_b128 a[220:223], v6 offset:768\n"
    "v_xor_b32 v13, 16384, v27\n"
    "s_waitcnt lgkmcnt(4)\n"
    "v_mfma_f32_32x32x16_bf16 v[176:191], a[192:195], a[96:99], v[128:143]\n"
    "v_exp_f32 v251, v207\n"
    "v_add_f32 v25, v25, v250\n"
    "v_add_f32 v25, v25, v251\n"
    "v_cvt_pk_bf16_f32 v239, v250, v251\n"
    "v_xor_b32 v14, 16384, v28\n"
    "v_mfma_f32_32x32x16_bf16 v[176:191], a[196:199], a[100:103], v[176:191]\n"
    "v_exp_f32 v248, v208\n"
    "v_cmp_lt_f32 vcc, v26, v25\n"
    "v_exp_f32 v249, v209\n"
    "v_xor_b32 v15, 4096, v29\n"
    "v_mfma_f32_32x32x16_bf16 v[176:191], a[200:203], a[104:107], v[176:191]\n"
    "s_cbranch_vccnz .L_stub19\n"
    ".L_ret19:\n"
    "v_add_f32 v22, v22, v25\n"
    "v_exp_f32 v250, v210\n"
    "v_add_f32 v24, v248, v249\n"
    "s_mul_i32 s42, s37, 786432\n"
    "v_mfma_f32_32x32x16_bf16 v[176:191], a[204:207], a[108:111], v[176:191]\n"
    "v_cvt_pk_bf16_f32 v240, v248, v249\n"
    "v_exp_f32 v251, v211\n"
    "v_add_f32 v24, v24, v250\n"
    "s_lshl_b32 s52, s38, 12\n"
    "s_waitcnt lgkmcnt(0)\n"
    "v_mfma_f32_32x32x16_bf16 v[176:191], a[208:211], a[112:115], v[176:191]\n"
    "v_exp_f32 v248, v212\n"
    "v_add_f32 v24, v24, v251\n"
    "v_cvt_pk_bf16_f32 v241, v250, v251\n"
    "s_add_u32 s42, s42, s52\n"
    "v_mfma_f32_32x32x16_bf16 v[176:191], a[212:215], a[116:119], v[176:191]\n"
    "v_exp_f32 v249, v213\n"
    "v_add_f32 v24, v24, v248\n"
    "v_exp_f32 v250, v214\n"
    "v_add_f32 v24, v24, v249\n"
    "v_mfma_f32_32x32x16_bf16 v[176:191], a[216:219], a[120:123], v[176:191]\n"
    "v_cvt_pk_bf16_f32 v242, v248, v249\n"
    "v_exp_f32 v251, v215\n"
    "v_add_f32 v24, v24, v250\n"
    "v_exp_f32 v248, v216\n"
    "v_mfma_f32_32x32x16_bf16 v[176:191], a[220:223], a[124:127], v[176:191]\n"
    "v_add_f32 v24, v24, v251\n"
    "v_cvt_pk_bf16_f32 v243, v250, v251\n"
    "v_exp_f32 v249, v217\n"
    "v_add_f32 v24, v24, v248\n"
    "v_mfma_f32_32x32x16_bf16 v[48:63], a[224:227], v[232:235], v[48:63]\n"
    "v_exp_f32 v250, v218\n"
    "v_add_f32 v24, v24, v249\n"
    "v_cvt_pk_bf16_f32 v244, v248, v249\n"
    "v_exp_f32 v251, v219\n"
    "v_mfma_f32_32x32x16_bf16 v[96:111], a[228:231], v[232:235], v[96:111]\n"
    "v_add_f32 v24, v24, v250\n"
    "v_exp_f32 v248, v220\n"
    "v_add_f32 v24, v24, v251\n"
    "v_cvt_pk_bf16_f32 v245, v250, v251\n"
    "v_mfma_f32_32x32x16_bf16 a[16:31], a[232:235], v[232:235], a[16:31]\n"
    "v_exp_f32 v249, v221\n"
    "v_add_f32 v24, v24, v248\n"
    "v_exp_f32 v250, v222\n"
    "v_add_f32 v24, v24, v249\n"
    "v_mfma_f32_32x32x16_bf16 a[64:79], a[236:239], v[232:235], a[64:79]\n"
    "v_cvt_pk_bf16_f32 v246, v248, v249\n"
    "v_exp_f32 v251, v223\n"
    "v_add_f32 v24, v24, v250\n"
    "v_add_f32 v24, v24, v251\n"
    "v_cvt_pk_bf16_f32 v247, v250, v251\n"
    "v_mfma_f32_32x32x16_bf16 v[48:63], a[240:243], v[236:239], v[48:63]\n"
    "v_exp_f32 v248, v176\n"
    "v_cmp_lt_f32 vcc, v26, v24\n"
    "v_exp_f32 v249, v177\n"
    "s_cbranch_vccnz .L_stub20\n"
    ".L_ret20:\n"
    "v_mfma_f32_32x32x16_bf16 v[96:111], a[244:247], v[236:239], v[96:111]\n"
    "v_add_f32 v23, v23, v24\n"
    "v_exp_f32 v250, v178\n"
    "v_add_f32 v25, v248, v249\n"
    "v_cvt_pk_bf16_f32 v224, v248, v249\n"
    "v_mfma_f32_32x32x16_bf16 a[16:31], a[248:251], v[236:239], a[16:31]\n"
    "v_exp_f32 v251, v179\n"
    "v_add_f32 v25, v25, v250\n"
    "v_exp_f32 v248, v180\n"
    "v_add_f32 v25, v25, v251\n"
    "v_mfma_f32_32x32x16_bf16 a[64:79], a[252:255], v[236:239], a[64:79]\n"
    "v_cvt_pk_bf16_f32 v225, v250, v251\n"
    "v_exp_f32 v249, v181\n"
    "v_add_f32 v25, v25, v248\n"
    "v_exp_f32 v250, v182\n"
    "v_mfma_f32_32x32x16_bf16 v[192:207], a[192:195], a[128:131], v[144:159]\n"
    "v_add_f32 v25, v25, v249\n"
    "v_cvt_pk_bf16_f32 v226, v248, v249\n"
    "v_exp_f32 v251, v183\n"
    "v_add_f32 v25, v25, v250\n"
    "v_mfma_f32_32x32x16_bf16 v[192:207], a[196:199], a[132:135], v[192:207]\n"
    "v_exp_f32 v248, v184\n"
    "v_add_f32 v25, v25, v251\n"
    "v_cvt_pk_bf16_f32 v227, v250, v251\n"
    "v_exp_f32 v249, v185\n"
    "v_mfma_f32_32x32x16_bf16 v[192:207], a[200:203], a[136:139], v[192:207]\n"
    "v_add_f32 v25, v25, v248\n"
    "v_exp_f32 v250, v186\n"
    "v_add_f32 v25, v25, v249\n"
    "v_cvt_pk_bf16_f32 v228, v248, v249\n"
    "v_mfma_f32_32x32x16_bf16 v[192:207], a[204:207], a[140:143], v[192:207]\n"
    "v_exp_f32 v251, v187\n"
    "v_add_f32 v25, v25, v250\n"
    "v_exp_f32 v248, v188\n"
    "v_add_f32 v25, v25, v251\n"
    "v_mfma_f32_32x32x16_bf16 v[192:207], a[208:211], a[144:147], v[192:207]\n"
    "v_cvt_pk_bf16_f32 v229, v250, v251\n"
    "v_exp_f32 v249, v189\n"
    "v_add_f32 v25, v25, v248\n"
    "v_exp_f32 v250, v190\n"
    "v_mfma_f32_32x32x16_bf16 v[192:207], a[212:215], a[148:151], v[192:207]\n"
    "v_add_f32 v25, v25, v249\n"
    "v_cvt_pk_bf16_f32 v230, v248, v249\n"
    "v_exp_f32 v251, v191\n"
    "v_add_f32 v25, v25, v250\n"
    "v_add_f32 v25, v25, v251\n"
    "v_mfma_f32_32x32x16_bf16 v[192:207], a[216:219], a[152:155], v[192:207]\n"
    "v_cvt_pk_bf16_f32 v231, v250, v251\n"
    "v_mfma_f32_32x32x16_bf16 v[192:207], a[220:223], a[156:159], v[192:207]\n"
    "v_add_u32 v7, 32768, v7\n"
    "v_add_u32 v8, 32768, v8\n"
    "v_mfma_f32_32x32x16_bf16 v[64:79], a[224:227], v[240:243], v[64:79]\n"
    "s_waitcnt vmcnt(2)\n"
    "s_barrier\n"
    "ds_read_b64_tr_b16 a[224:225], v7 offset:0\n"
    "v_mfma_f32_32x32x16_bf16 v[112:127], a[228:231], v[240:243], v[112:127]\n"
    "v_exp_f32 v248, v192\n"
    "v_cmp_lt_f32 vcc, v26, v25\n"
    "ds_read_b64_tr_b16 a[228:229], v7 offset:256\n"
    "v_mfma_f32_32x32x16_bf16 a[32:47], a[232:235], v[240:243], a[32:47]\n"
    "v_exp_f32 v249, v193\n"
    "s_cbranch_vccnz .L_stub21\n"
    ".L_ret21:\n"
    "v_add_f32 v21, v21, v25\n"
    "ds_read_b64_tr_b16 a[232:233], v8 offset:0\n"
    "v_mfma_f32_32x32x16_bf16 a[80:95], a[236:239], v[240:243], a[80:95]\n"
    "v_exp_f32 v250, v194\n"
    "v_add_f32 v24, v248, v249\n"
    "v_cvt_pk_bf16_f32 v232, v248, v249\n"
    "ds_read_b64_tr_b16 a[236:237], v8 offset:256\n"
    "v_mfma_f32_32x32x16_bf16 v[64:79], a[240:243], v[244:247], v[64:79]\n"
    "v_exp_f32 v251, v195\n"
    "v_add_f32 v24, v24, v250\n"
    "ds_read_b64_tr_b16 a[240:241], v7 offset:4096\n"
    "v_mfma_f32_32x32x16_bf16 v[112:127], a[244:247], v[244:247], v[112:127]\n"
    "v_exp_f32 v248, v196\n"
    "v_add_f32 v24, v24, v251\n"
    "v_cvt_pk_bf16_f32 v233, v250, v251\n"
    "ds_read_b64_tr_b16 a[244:245], v7 offset:4352\n"
    "v_mfma_f32_32x32x16_bf16 a[32:47], a[248:251], v[244:247], a[32:47]\n"
    "v_exp_f32 v249, v197\n"
    "v_add_f32 v24, v24, v248\n"
    "ds_read_b64_tr_b16 a[248:249], v8 offset:4096\n"
    "v_mfma_f32_32x32x16_bf16 a[80:95], a[252:255], v[244:247], a[80:95]\n"
    "v_exp_f32 v250, v198\n"
    "v_add_f32 v24, v24, v249\n"
    "v_cvt_pk_bf16_f32 v234, v248, v249\n"
    "ds_read_b64_tr_b16 a[252:253], v8 offset:4352\n"
    "v_mfma_f32_32x32x16_bf16 v[208:223], a[192:195], a[160:163], v[160:175]\n"
    "v_exp_f32 v251, v199\n"
    "v_add_f32 v24, v24, v250\n"
    "ds_read_b64_tr_b16 a[226:227], v7 offset:2048\n"
    "v_mfma_f32_32x32x16_bf16 v[208:223], a[196:199], a[164:167], v[208:223]\n"
    "v_exp_f32 v248, v200\n"
    "v_add_f32 v24, v24, v251\n"
    "v_cvt_pk_bf16_f32 v235, v250, v251\n"
    "ds_read_b64_tr_b16 a[230:231], v7 offset:2304\n"
    "v_mfma_f32_32x32x16_bf16 v[208:223], a[200:203], a[168:171], v[208:223]\n"
    "v_exp_f32 v249, v201\n"
    "v_add_f32 v24, v24, v248\n"
    "ds_read_b64_tr_b16 a[234:235], v8 offset:2048\n"
    "v_mfma_f32_32x32x16_bf16 v[208:223], a[204:207], a[172:175], v[208:223]\n"
    "v_exp_f32 v250, v202\n"
    "v_add_f32 v24, v24, v249\n"
    "v_cvt_pk_bf16_f32 v236, v248, v249\n"
    "ds_read_b64_tr_b16 a[238:239], v8 offset:2304\n"
    "v_mfma_f32_32x32x16_bf16 v[208:223], a[208:211], a[176:179], v[208:223]\n"
    "v_exp_f32 v251, v203\n"
    "v_add_f32 v24, v24, v250\n"
    "ds_read_b64_tr_b16 a[242:243], v7 offset:6144\n"
    "v_mfma_f32_32x32x16_bf16 v[208:223], a[212:215], a[180:183], v[208:223]\n"
    "v_exp_f32 v248, v204\n"
    "v_add_f32 v24, v24, v251\n"
    "v_cvt_pk_bf16_f32 v237, v250, v251\n"
    "ds_read_b64_tr_b16 a[246:247], v7 offset:6400\n"
    "v_mfma_f32_32x32x16_bf16 v[208:223], a[216:219], a[184:187], v[208:223]\n"
    "v_exp_f32 v249, v205\n"
    "v_add_f32 v24, v24, v248\n"
    "ds_read_b64_tr_b16 a[250:251], v8 offset:6144\n"
    "v_mfma_f32_32x32x16_bf16 v[208:223], a[220:223], a[188:191], v[208:223]\n"
    "v_exp_f32 v250, v206\n"
    "v_add_f32 v24, v24, v249\n"
    "ds_read_b64_tr_b16 a[254:255], v8 offset:6400\n"
    "s_waitcnt lgkmcnt(4)\n"
    "v_mfma_f32_32x32x16_bf16 v[32:47], a[224:227], v[224:227], v[32:47]\n"
    "v_cvt_pk_bf16_f32 v238, v248, v249\n"
    "v_exp_f32 v251, v207\n"
    "v_add_f32 v24, v24, v250\n"
    "v_add_f32 v24, v24, v251\n"
    "v_cvt_pk_bf16_f32 v239, v250, v251\n"
    "v_mfma_f32_32x32x16_bf16 v[80:95], a[228:231], v[224:227], v[80:95]\n"
    "v_exp_f32 v248, v208\n"
    "v_cmp_lt_f32 vcc, v26, v24\n"
    "v_exp_f32 v249, v209\n"
    "s_cbranch_vccnz .L_stub22\n"
    ".L_ret22:\n"
    "v_mfma_f32_32x32x16_bf16 a[0:15], a[232:235], v[224:227], a[0:15]\n"
    "v_add_f32 v22, v22, v24\n"
    "v_exp_f32 v250, v210\n"
    "v_add_f32 v25, v248, v249\n"
    "v_cvt_pk_bf16_f32 v240, v248, v249\n"
    "v_mfma_f32_32x32x16_bf16 a[48:63], a[236:239], v[224:227], a[48:63]\n"
    "v_exp_f32 v251, v211\n"
    "v_add_f32 v25, v25, v250\n"
    "v_exp_f32 v248, v212\n"
    "s_waitcnt lgkmcnt(0)\n"
    "v_mfma_f32_32x32x16_bf16 v[32:47], a[240:243], v[228:231], v[32:47]\n"
    "v_add_f32 v25, v25, v251\n"
    "v_cvt_pk_bf16_f32 v241, v250, v251\n"
    "v_exp_f32 v249, v213\n"
    "v_add_f32 v25, v25, v248\n"
    "v_mfma_f32_32x32x16_bf16 v[80:95], a[244:247], v[228:231], v[80:95]\n"
    "v_mov_b32 v252, v21\n"
    "v_mov_b32 v253, v21\n"
    "v_mov_b32 v18, v128\n"
    "s_nop 0\n"
    "v_permlane32_swap_b32 v252, v253\n"
    "v_add_f32 v19, v252, v253\n"
    "ds_write_b64 v29, v[18:19] offset:0\n"
    "v_mfma_f32_32x32x16_bf16 a[0:15], a[248:251], v[228:231], a[0:15]\n"
    "v_exp_f32 v250, v214\n"
    "v_add_f32 v25, v25, v249\n"
    "v_cvt_pk_bf16_f32 v242, v248, v249\n"
    "v_exp_f32 v251, v215\n"
    "v_mfma_f32_32x32x16_bf16 a[48:63], a[252:255], v[228:231], a[48:63]\n"
    "v_add_f32 v25, v25, v250\n"
    "v_exp_f32 v248, v216\n"
    "v_add_f32 v25, v25, v251\n"
    "v_cvt_pk_bf16_f32 v243, v250, v251\n"
    "v_mfma_f32_32x32x16_bf16 v[48:63], a[224:227], v[232:235], v[48:63]\n"
    "v_exp_f32 v249, v217\n"
    "v_add_f32 v25, v25, v248\n"
    "v_exp_f32 v250, v218\n"
    "v_add_f32 v25, v25, v249\n"
    "v_mfma_f32_32x32x16_bf16 v[96:111], a[228:231], v[232:235], v[96:111]\n"
    "v_mov_b32 v252, v22\n"
    "v_mov_b32 v253, v22\n"
    "v_mov_b32 v10, v144\n"
    "s_nop 0\n"
    "v_permlane32_swap_b32 v252, v253\n"
    "v_add_f32 v11, v252, v253\n"
    "ds_write_b64 v29, v[10:11] offset:512\n"
    "v_mfma_f32_32x32x16_bf16 a[16:31], a[232:235], v[232:235], a[16:31]\n"
    "v_cvt_pk_bf16_f32 v244, v248, v249\n"
    "ds_write_b128 v27, a[0:3] offset:0\n"
    "ds_write_b128 v27, a[4:7] offset:1024\n"
    "v_mfma_f32_32x32x16_bf16 a[64:79], a[236:239], v[232:235], a[64:79]\n"
    "v_exp_f32 v251, v219\n"
    "v_add_f32 v25, v25, v250\n"
    "ds_write_b128 v27, a[8:11] offset:2048\n"
    "v_mfma_f32_32x32x16_bf16 v[48:63], a[240:243], v[236:239], v[48:63]\n"
    "v_exp_f32 v248, v220\n"
    "v_add_f32 v25, v25, v251\n"
    "v_cvt_pk_bf16_f32 v245, v250, v251\n"
    "v_exp_f32 v249, v221\n"
    "ds_write_b128 v27, a[12:15] offset:3072\n"
    "v_mfma_f32_32x32x16_bf16 v[96:111], a[244:247], v[236:239], v[96:111]\n"
    "v_add_f32 v25, v25, v248\n"
    "v_exp_f32 v250, v222\n"
    "ds_write_b128 v27, a[48:51] offset:4096\n"
    "ds_write_b128 v27, a[52:55] offset:5120\n"
    "v_mfma_f32_32x32x16_bf16 a[16:31], a[248:251], v[236:239], a[16:31]\n"
    "v_add_f32 v25, v25, v249\n"
    "v_cvt_pk_bf16_f32 v246, v248, v249\n"
    "v_exp_f32 v251, v223\n"
    "v_add_f32 v25, v25, v250\n"
    "ds_write_b128 v27, a[56:59] offset:6144\n"
    "v_mfma_f32_32x32x16_bf16 a[64:79], a[252:255], v[236:239], a[64:79]\n"
    "v_add_f32 v25, v25, v251\n"
    "v_cvt_pk_bf16_f32 v247, v250, v251\n"
    "v_cmp_lt_f32 vcc, v26, v25\n"
    "s_cbranch_vccnz .L_stub23\n"
    ".L_ret23:\n"
    "v_add_f32 v23, v23, v25\n"
    "ds_write_b128 v27, a[60:63] offset:7168\n"
    "s_waitcnt lgkmcnt(0)\n"
    "v_mfma_f32_32x32x16_bf16 v[64:79], a[224:227], v[240:243], v[64:79]\n"
    "s_barrier\n"
    "ds_read_b64 v[230:231], v15 offset:0\n"
    "ds_read_b128 v[176:179], v13 offset:0\n"
    "ds_read_b128 v[180:183], v13 offset:1024\n"
    "ds_read_b128 v[184:187], v13 offset:2048\n"
    "ds_read_b128 v[188:191], v13 offset:3072\n"
    "ds_read_b128 v[192:195], v13 offset:4096\n"
    "ds_read_b128 v[196:199], v13 offset:5120\n"
    "ds_read_b128 v[200:203], v13 offset:6144\n"
    "ds_read_b128 v[204:207], v13 offset:7168\n"
    "v_mfma_f32_32x32x16_bf16 v[112:127], a[228:231], v[240:243], v[112:127]\n"
    "v_mov_b32 v252, v23\n"
    "v_mov_b32 v253, v23\n"
    "v_mov_b32 v226, v160\n"
    "s_nop 0\n"
    "v_permlane32_swap_b32 v252, v253\n"
    "v_add_f32 v227, v252, v253\n"
    "ds_write_b64 v29, v[226:227] offset:1024\n"
    "v_mfma_f32_32x32x16_bf16 a[32:47], a[232:235], v[240:243], a[32:47]\n"
    "ds_write_b128 v27, a[16:19] offset:32768\n"
    "ds_write_b128 v27, a[20:23] offset:33792\n"
    "v_mfma_f32_32x32x16_bf16 a[80:95], a[236:239], v[240:243], a[80:95]\n"
    "ds_write_b128 v27, a[24:27] offset:34816\n"
    "ds_write_b128 v27, a[28:31] offset:35840\n"
    "s_waitcnt lgkmcnt(13)\n"
    "v_min_f32 v252, v18, v230\n"
    "v_sub_f32 v253, v252, v18\n"
    "v_sub_f32 v254, v252, v230\n"
    "v_exp_f32 v253, v253\n"
    "v_exp_f32 v254, v254\n"
    "s_nop 0\n"
    "v_mfma_f32_32x32x16_bf16 v[64:79], a[240:243], v[244:247], v[64:79]\n"
    "ds_write_b128 v27, a[64:67] offset:36864\n"
    "ds_write_b128 v27, a[68:71] offset:37888\n"
    "v_mul_f32 v255, v253, v19\n"
    "v_fma_f32 v255, v254, v231, v255\n"
    "v_rcp_f32 v255, v255\n"
    "s_nop 0\n"
    "v_mul_f32 v236, v253, v255\n"
    "v_mul_f32 v237, v254, v255\n"
    "v_mfma_f32_32x32x16_bf16 v[112:127], a[244:247], v[244:247], v[112:127]\n"
    "ds_write_b128 v27, a[72:75] offset:38912\n"
    "ds_write_b128 v27, a[76:79] offset:39936\n"
    "s_waitcnt lgkmcnt(9)\n"
    "v_mfma_f32_32x32x16_bf16 a[32:47], a[248:251], v[244:247], a[32:47]\n"
    "s_add_u32 s43, s42, 0\n"
    "v_pk_mul_f32 v[176:177], v[176:177], v[236:237] op_sel:[0,1] op_sel_hi:[1,1]\n"
    "v_pk_mul_f32 v[178:179], v[178:179], v[236:237] op_sel:[0,1] op_sel_hi:[1,1]\n"
    "v_pk_mul_f32 v[180:181], v[180:181], v[236:237] op_sel:[0,1] op_sel_hi:[1,1]\n"
    "v_pk_mul_f32 v[182:183], v[182:183], v[236:237] op_sel:[0,1] op_sel_hi:[1,1]\n"
    "v_pk_fma_f32 v[176:177], v[32:33], v[236:237], v[176:177] op_sel:[0,0,0] op_sel_hi:[1,0,1]\n"
    "v_mfma_f32_32x32x16_bf16 a[80:95], a[252:255], v[244:247], a[80:95]\n"
    "v_pk_fma_f32 v[178:179], v[34:35], v[236:237], v[178:179] op_sel:[0,0,0] op_sel_hi:[1,0,1]\n"
    "v_pk_fma_f32 v[180:181], v[36:37], v[236:237], v[180:181] op_sel:[0,0,0] op_sel_hi:[1,0,1]\n"
    "v_pk_fma_f32 v[182:183], v[38:39], v[236:237], v[182:183] op_sel:[0,0,0] op_sel_hi:[1,0,1]\n"
    "v_cvt_pk_bf16_f32 v32, v176, v177\n"
    "v_cvt_pk_bf16_f32 v33, v178, v179\n"
    "v_cvt_pk_bf16_f32 v34, v180, v181\n"
    "s_waitcnt lgkmcnt(0)\n"
    "s_barrier\n"
    "ds_read_b64 v[232:233], v15 offset:512\n"
    "ds_read_b128 v[128:131], v13 offset:32768\n"
    "ds_read_b128 v[132:135], v13 offset:33792\n"
    "ds_read_b128 v[136:139], v13 offset:34816\n"
    "ds_read_b128 v[140:143], v13 offset:35840\n"
    "ds_read_b128 v[144:147], v13 offset:36864\n"
    "ds_read_b128 v[148:151], v13 offset:37888\n"
    "ds_read_b128 v[152:155], v13 offset:38912\n"
    "ds_read_b128 v[156:159], v13 offset:39936\n"
    "v_cvt_pk_bf16_f32 v35, v182, v183\n"
    "buffer_store_dwordx4 v[32:35], v30, s[60:63], s43 offen offset:0 sc1\n"
    "v_pk_mul_f32 v[184:185], v[184:185], v[236:237] op_sel:[0,1] op_sel_hi:[1,1]\n"
    "v_pk_mul_f32 v[186:187], v[186:187], v[236:237] op_sel:[0,1] op_sel_hi:[1,1]\n"
    "v_pk_mul_f32 v[188:189], v[188:189], v[236:237] op_sel:[0,1] op_sel_hi:[1,1]\n"
    "v_pk_mul_f32 v[190:191], v[190:191], v[236:237] op_sel:[0,1] op_sel_hi:[1,1]\n"
    "v_pk_fma_f32 v[184:185], v[40:41], v[236:237], v[184:185] op_sel:[0,0,0] op_sel_hi:[1,0,1]\n"
    "v_pk_fma_f32 v[186:187], v[42:43], v[236:237], v[186:187] op_sel:[0,0,0] op_sel_hi:[1,0,1]\n"
    "v_pk_fma_f32 v[188:189], v[44:45], v[236:237], v[188:189] op_sel:[0,0,0] op_sel_hi:[1,0,1]\n"
    "v_pk_fma_f32 v[190:191], v[46:47], v[236:237], v[190:191] op_sel:[0,0,0] op_sel_hi:[1,0,1]\n"
    "v_cvt_pk_bf16_f32 v40, v184, v185\n"
    "v_cvt_pk_bf16_f32 v41, v186, v187\n"
    "v_cvt_pk_bf16_f32 v42, v188, v189\n"
    "v_cvt_pk_bf16_f32 v43, v190, v191\n"
    "ds_write_b128 v27, a[32:35] offset:0\n"
    "ds_write_b128 v27, a[36:39] offset:1024\n"
    "ds_write_b128 v27, a[40:43] offset:2048\n"
    "ds_write_b128 v27, a[44:47] offset:3072\n"
    "ds_write_b128 v27, a[80:83] offset:4096\n"
    "ds_write_b128 v27, a[84:87] offset:5120\n"
    "ds_write_b128 v27, a[88:91] offset:6144\n"
    "ds_write_b128 v27, a[92:95] offset:7168\n"
    "s_add_u32 s64, %[op_lo], 33554432\n"
    "s_addc_u32 s65, %[op_hi], 0\n"
    "s_and_b32 s65, s65, 0xffff\n"
    "s_mov_b32 s66, 133120\n"
    "s_mov_b32 s67, 0x20000\n"
    "s_lshr_b32 s75, %[bid], 3\n"
    "s_and_b32 s76, %[bid], 7\n"
    "s_lshl_b32 s74, %[bid], 9\n"
    "v_lshlrev_b32 v246, 3, v4\n"
    "v_or_b32 v246, s76, v246\n"
    "s_and_b32 s77, %[bid], 0xf9\n"
    "v_and_b32 v245, 3, v4\n"
    "v_lshlrev_b32 v245, 1, v245\n"
    "v_or_b32 v245, s77, v245\n"
    "v_cmp_gt_u32 vcc, 32, v4\n"
    "s_nop 1\n"
    "v_cndmask_b32 v246, v245, v246, vcc\n"
    "v_and_b32 v244, 7, v246\n"
    "v_lshlrev_b32 v244, 8, v244\n"
    "v_and_b32 v245, 0xf8, v246\n"
    "v_add_u32 v244, v244, v245\n"
    "v_add_u32 v244, 131072, v244\n"
    "s_lshr_b32 s77, s76, 1\n"
    "s_add_u32 s77, s77, 32\n"
    "s_lshl_b32 s77, s77, 3\n"
    "s_lshl_b32 s78, s75, 3\n"
    "v_mov_b32 v245, s77\n"
    "v_mov_b32 v247, s78\n"
    "v_cndmask_b32 v245, v245, v247, vcc\n"
    "v_lshl_add_u32 v245, v246, 9, v245\n"
    "buffer_load_dwordx2 v[246:247], v244, s[64:67], 0 offen sc1\n"
    "s_mov_b32 s70, 0\n"
    "s_mov_b32 s71, 0\n"
    "buffer_store_dwordx4 v[40:43], v30, s[60:63], s43 offen offset:1024 sc1\n"
    "v_pk_mul_f32 v[192:193], v[192:193], v[236:237] op_sel:[0,1] op_sel_hi:[1,1]\n"
    "v_pk_mul_f32 v[194:195], v[194:195], v[236:237] op_sel:[0,1] op_sel_hi:[1,1]\n"
    "v_pk_mul_f32 v[196:197], v[196:197], v[236:237] op_sel:[0,1] op_sel_hi:[1,1]\n"
    "v_pk_mul_f32 v[198:199], v[198:199], v[236:237] op_sel:[0,1] op_sel_hi:[1,1]\n"
    "v_pk_fma_f32 v[192:193], v[80:81], v[236:237], v[192:193] op_sel:[0,0,0] op_sel_hi:[1,0,1]\n"
    "v_pk_fma_f32 v[194:195], v[82:83], v[236:237], v[194:195] op_sel:[0,0,0] op_sel_hi:[1,0,1]\n"
    "v_pk_fma_f32 v[196:197], v[84:85], v[236:237], v[196:197] op_sel:[0,0,0] op_sel_hi:[1,0,1]\n"
    "v_pk_fma_f32 v[198:199], v[86:87], v[236:237], v[198:199] op_sel:[0,0,0] op_sel_hi:[1,0,1]\n"
    "v_cvt_pk_bf16_f32 v80, v192, v193\n"
    "v_cvt_pk_bf16_f32 v81, v194, v195\n"
    "v_cvt_pk_bf16_f32 v82, v196, v197\n"
    "v_cvt_pk_bf16_f32 v83, v198, v199\n"
    "buffer_store_dwordx4 v[80:83], v30, s[60:63], s43 offen offset:2048 sc1\n"
    "v_pk_mul_f32 v[200:201], v[200:201], v[236:237] op_sel:[0,1] op_sel_hi:[1,1]\n"
    "v_pk_mul_f32 v[202:203], v[202:203], v[236:237] op_sel:[0,1] op_sel_hi:[1,1]\n"
    "v_pk_mul_f32 v[204:205], v[204:205], v[236:237] op_sel:[0,1] op_sel_hi:[1,1]\n"
    "v_pk_mul_f32 v[206:207], v[206:207], v[236:237] op_sel:[0,1] op_sel_hi:[1,1]\n"
    "v_pk_fma_f32 v[200:201], v[88:89], v[236:237], v[200:201] op_sel:[0,0,0] op_sel_hi:[1,0,1]\n"
    "v_pk_fma_f32 v[202:203], v[90:91], v[236:237], v[202:203] op_sel:[0,0,0] op_sel_hi:[1,0,1]\n"
    "v_pk_fma_f32 v[204:205], v[92:93], v[236:237], v[204:205] op_sel:[0,0,0] op_sel_hi:[1,0,1]\n"
    "v_pk_fma_f32 v[206:207], v[94:95], v[236:237], v[206:207] op_sel:[0,0,0] op_sel_hi:[1,0,1]\n"
    "v_cvt_pk_bf16_f32 v88, v200, v201\n"
    "v_cvt_pk_bf16_f32 v89, v202, v203\n"
    "v_cvt_pk_bf16_f32 v90, v204, v205\n"
    "v_cvt_pk_bf16_f32 v91, v206, v207\n"
    "buffer_store_dwordx4 v[88:91], v30, s[60:63], s43 offen offset:3072 sc1\n"
    "s_waitcnt lgkmcnt(0)\n"
    "s_barrier\n"
    "ds_read_b64 v[234:235], v15 offset:1024\n"
    "ds_read_b128 v[208:211], v13 offset:0\n"
    "ds_read_b128 v[212:215], v13 offset:1024\n"
    "ds_read_b128 v[216:219], v13 offset:2048\n"
    "ds_read_b128 v[220:223], v13 offset:3072\n"
    "ds_read_b128 v[160:163], v13 offset:4096\n"
    "ds_read_b128 v[164:167], v13 offset:5120\n"
    "ds_read_b128 v[168:171], v13 offset:6144\n"
    "ds_read_b128 v[172:175], v13 offset:7168\n"
    "v_min_f32 v252, v10, v232\n"
    "v_sub_f32 v253, v252, v10\n"
    "v_sub_f32 v254, v252, v232\n"
    "v_exp_f32 v253, v253\n"
    "v_exp_f32 v254, v254\n"
    "s_nop 0\n"
    "v_mul_f32 v255, v253, v11\n"
    "v_fma_f32 v255, v254, v233, v255\n"
    "v_rcp_f32 v255, v255\n"
    "s_nop 0\n"
    "v_mul_f32 v238, v253, v255\n"
    "v_mul_f32 v239, v254, v255\n"
    "s_add_u32 s43, s42, 262144\n"
    "v_pk_mul_f32 v[128:129], v[128:129], v[238:239] op_sel:[0,1] op_sel_hi:[1,1]\n"
    "v_pk_mul_f32 v[130:131], v[130:131], v[238:239] op_sel:[0,1] op_sel_hi:[1,1]\n"
    "v_pk_mul_f32 v[132:133], v[132:133], v[238:239] op_sel:[0,1] op_sel_hi:[1,1]\n"
    "v_pk_mul_f32 v[134:135], v[134:135], v[238:239] op_sel:[0,1] op_sel_hi:[1,1]\n"
    "v_pk_fma_f32 v[128:129], v[48:49], v[238:239], v[128:129] op_sel:[0,0,0] op_sel_hi:[1,0,1]\n"
    "v_pk_fma_f32 v[130:131], v[50:51], v[238:239], v[130:131] op_sel:[0,0,0] op_sel_hi:[1,0,1]\n"
    "v_pk_fma_f32 v[132:133], v[52:53], v[238:239], v[132:133] op_sel:[0,0,0] op_sel_hi:[1,0,1]\n"
    "v_pk_fma_f32 v[134:135], v[54:55], v[238:239], v[134:135] op_sel:[0,0,0] op_sel_hi:[1,0,1]\n"
    "v_cvt_pk_bf16_f32 v48, v128, v129\n"
    "v_cvt_pk_bf16_f32 v49, v130, v131\n"
    "v_cvt_pk_bf16_f32 v50, v132, v133\n"
    "v_cvt_pk_bf16_f32 v51, v134, v135\n"
    "buffer_store_dwordx4 v[48:51], v30, s[60:63], s43 offen offset:0 sc1\n"
    "v_pk_mul_f32 v[136:137], v[136:137], v[238:239] op_sel:[0,1] op_sel_hi:[1,1]\n"
    "v_pk_mul_f32 v[138:139], v[138:139], v[238:239] op_sel:[0,1] op_sel_hi:[1,1]\n"
    "v_pk_mul_f32 v[140:141], v[140:141], v[238:239] op_sel:[0,1] op_sel_hi:[1,1]\n"
    "v_pk_mul_f32 v[142:143], v[142:143], v[238:239] op_sel:[0,1] op_sel_hi:[1,1]\n"
    "v_pk_fma_f32 v[136:137], v[56:57], v[238:239], v[136:137] op_sel:[0,0,0] op_sel_hi:[1,0,1]\n"
    "v_pk_fma_f32 v[138:139], v[58:59], v[238:239], v[138:139] op_sel:[0,0,0] op_sel_hi:[1,0,1]\n"
    "v_pk_fma_f32 v[140:141], v[60:61], v[238:239], v[140:141] op_sel:[0,0,0] op_sel_hi:[1,0,1]\n"
    "v_pk_fma_f32 v[142:143], v[62:63], v[238:239], v[142:143] op_sel:[0,0,0] op_sel_hi:[1,0,1]\n"
    "v_cvt_pk_bf16_f32 v56, v136, v137\n"
    "v_cvt_pk_bf16_f32 v57, v138, v139\n"
    "v_cvt_pk_bf16_f32 v58, v140, v141\n"
    "v_cvt_pk_bf16_f32 v59, v142, v143\n"
    "buffer_store_dwordx4 v[56:59], v30, s[60:63], s43 offen offset:1024 sc1\n"
    "v_pk_mul_f32 v[144:145], v[144:145], v[238:239] op_sel:[0,1] op_sel_hi:[1,1]\n"
    "v_pk_mul_f32 v[146:147], v[146:147], v[238:239] op_sel:[0,1] op_sel_hi:[1,1]\n"
    "v_pk_mul_f32 v[148:149], v[148:149], v[238:239] op_sel:[0,1] op_sel_hi:[1,1]\n"
    "v_pk_mul_f32 v[150:151], v[150:151], v[238:239] op_sel:[0,1] op_sel_hi:[1,1]\n"
    "v_pk_fma_f32 v[144:145], v[96:97], v[238:239], v[144:145] op_sel:[0,0,0] op_sel_hi:[1,0,1]\n"
    "v_pk_fma_f32 v[146:147], v[98:99], v[238:239], v[146:147] op_sel:[0,0,0] op_sel_hi:[1,0,1]\n"
    "v_pk_fma_f32 v[148:149], v[100:101], v[238:239], v[148:149] op_sel:[0,0,0] op_sel_hi:[1,0,1]\n"
    "v_pk_fma_f32 v[150:151], v[102:103], v[238:239], v[150:151] op_sel:[0,0,0] op_sel_hi:[1,0,1]\n"
    "v_cvt_pk_bf16_f32 v96, v144, v145\n"
    "v_cvt_pk_bf16_f32 v97, v146, v147\n"
    "v_cvt_pk_bf16_f32 v98, v148, v149\n"
    "v_cvt_pk_bf16_f32 v99, v150, v151\n"
    "buffer_store_dwordx4 v[96:99], v30, s[60:63], s43 offen offset:2048 sc1\n"
    "v_pk_mul_f32 v[152:153], v[152:153], v[238:239] op_sel:[0,1] op_sel_hi:[1,1]\n"
    "v_pk_mul_f32 v[154:155], v[154:155], v[238:239] op_sel:[0,1] op_sel_hi:[1,1]\n"
    "v_pk_mul_f32 v[156:157], v[156:157], v[238:239] op_sel:[0,1] op_sel_hi:[1,1]\n"
    "v_pk_mul_f32 v[158:159], v[158:159], v[238:239] op_sel:[0,1] op_sel_hi:[1,1]\n"
    "v_pk_fma_f32 v[152:153], v[104:105], v[238:239], v[152:153] op_sel:[0,0,0] op_sel_hi:[1,0,1]\n"
    "v_pk_fma_f32 v[154:155], v[106:107], v[238:239], v[154:155] op_sel:[0,0,0] op_sel_hi:[1,0,1]\n"
    "v_pk_fma_f32 v[156:157], v[108:109], v[238:239], v[156:157] op_sel:[0,0,0] op_sel_hi:[1,0,1]\n"
    "v_pk_fma_f32 v[158:159], v[110:111], v[238:239], v[158:159] op_sel:[0,0,0] op_sel_hi:[1,0,1]\n"
    "v_cvt_pk_bf16_f32 v104, v152, v153\n"
    "v_cvt_pk_bf16_f32 v105, v154, v155\n"
    "v_cvt_pk_bf16_f32 v106, v156, v157\n"
    "v_cvt_pk_bf16_f32 v107, v158, v159\n"
    "buffer_store_dwordx4 v[104:107], v30, s[60:63], s43 offen offset:3072 sc1\n"
    "s_waitcnt lgkmcnt(0)\n"
    "v_min_f32 v252, v226, v234\n"
    "v_sub_f32 v253, v252, v226\n"
    "v_sub_f32 v254, v252, v234\n"
    "v_exp_f32 v253, v253\n"
    "v_exp_f32 v254, v254\n"
    "s_nop 0\n"
    "v_mul_f32 v255, v253, v227\n"
    "v_fma_f32 v255, v254, v235, v255\n"
    "v_rcp_f32 v255, v255\n"
    "s_nop 0\n"
    "v_mul_f32 v240, v253, v255\n"
    "v_mul_f32 v241, v254, v255\n"
    "s_add_u32 s43, s42, 524288\n"
    "v_pk_mul_f32 v[208:209], v[208:209], v[240:241] op_sel:[0,1] op_sel_hi:[1,1]\n"
    "v_pk_mul_f32 v[210:211], v[210:211], v[240:241] op_sel:[0,1] op_sel_hi:[1,1]\n"
    "v_pk_mul_f32 v[212:213], v[212:213], v[240:241] op_sel:[0,1] op_sel_hi:[1,1]\n"
    "v_pk_mul_f32 v[214:215], v[214:215], v[240:241] op_sel:[0,1] op_sel_hi:[1,1]\n"
    "v_pk_fma_f32 v[208:209], v[64:65], v[240:241], v[208:209] op_sel:[0,0,0] op_sel_hi:[1,0,1]\n"
    "v_pk_fma_f32 v[210:211], v[66:67], v[240:241], v[210:211] op_sel:[0,0,0] op_sel_hi:[1,0,1]\n"
    "v_pk_fma_f32 v[212:213], v[68:69], v[240:241], v[212:213] op_sel:[0,0,0] op_sel_hi:[1,0,1]\n"
    "v_pk_fma_f32 v[214:215], v[70:71], v[240:241], v[214:215] op_sel:[0,0,0] op_sel_hi:[1,0,1]\n"
    "v_cvt_pk_bf16_f32 v64, v208, v209\n"
    "v_cvt_pk_bf16_f32 v65, v210, v211\n"
    "v_cvt_pk_bf16_f32 v66, v212, v213\n"
    "v_cvt_pk_bf16_f32 v67, v214, v215\n"
    "buffer_store_dwordx4 v[64:67], v30, s[60:63], s43 offen offset:0 sc1\n"
    "v_pk_mul_f32 v[216:217], v[216:217], v[240:241] op_sel:[0,1] op_sel_hi:[1,1]\n"
    "v_pk_mul_f32 v[218:219], v[218:219], v[240:241] op_sel:[0,1] op_sel_hi:[1,1]\n"
    "v_pk_mul_f32 v[220:221], v[220:221], v[240:241] op_sel:[0,1] op_sel_hi:[1,1]\n"
    "v_pk_mul_f32 v[222:223], v[222:223], v[240:241] op_sel:[0,1] op_sel_hi:[1,1]\n"
    "v_pk_fma_f32 v[216:217], v[72:73], v[240:241], v[216:217] op_sel:[0,0,0] op_sel_hi:[1,0,1]\n"
    "v_pk_fma_f32 v[218:219], v[74:75], v[240:241], v[218:219] op_sel:[0,0,0] op_sel_hi:[1,0,1]\n"
    "v_pk_fma_f32 v[220:221], v[76:77], v[240:241], v[220:221] op_sel:[0,0,0] op_sel_hi:[1,0,1]\n"
    "v_pk_fma_f32 v[222:223], v[78:79], v[240:241], v[222:223] op_sel:[0,0,0] op_sel_hi:[1,0,1]\n"
    "v_cvt_pk_bf16_f32 v72, v216, v217\n"
    "v_cvt_pk_bf16_f32 v73, v218, v219\n"
    "v_cvt_pk_bf16_f32 v74, v220, v221\n"
    "v_cvt_pk_bf16_f32 v75, v222, v223\n"
    "buffer_store_dwordx4 v[72:75], v30, s[60:63], s43 offen offset:1024 sc1\n"
    "v_pk_mul_f32 v[160:161], v[160:161], v[240:241] op_sel:[0,1] op_sel_hi:[1,1]\n"
    "v_pk_mul_f32 v[162:163], v[162:163], v[240:241] op_sel:[0,1] op_sel_hi:[1,1]\n"
    "v_pk_mul_f32 v[164:165], v[164:165], v[240:241] op_sel:[0,1] op_sel_hi:[1,1]\n"
    "v_pk_mul_f32 v[166:167], v[166:167], v[240:241] op_sel:[0,1] op_sel_hi:[1,1]\n"
    "v_pk_fma_f32 v[160:161], v[112:113], v[240:241], v[160:161] op_sel:[0,0,0] op_sel_hi:[1,0,1]\n"
    "v_pk_fma_f32 v[162:163], v[114:115], v[240:241], v[162:163] op_sel:[0,0,0] op_sel_hi:[1,0,1]\n"
    "v_pk_fma_f32 v[164:165], v[116:117], v[240:241], v[164:165] op_sel:[0,0,0] op_sel_hi:[1,0,1]\n"
    "v_pk_fma_f32 v[166:167], v[118:119], v[240:241], v[166:167] op_sel:[0,0,0] op_sel_hi:[1,0,1]\n"
    "v_cvt_pk_bf16_f32 v112, v160, v161\n"
    "v_cvt_pk_bf16_f32 v113, v162, v163\n"
    "v_cvt_pk_bf16_f32 v114, v164, v165\n"
    "v_cvt_pk_bf16_f32 v115, v166, v167\n"
    "buffer_store_dwordx4 v[112:115], v30, s[60:63], s43 offen offset:2048 sc1\n"
    "v_pk_mul_f32 v[168:169], v[168:169], v[240:241] op_sel:[0,1] op_sel_hi:[1,1]\n"
    "v_pk_mul_f32 v[170:171], v[170:171], v[240:241] op_sel:[0,1] op_sel_hi:[1,1]\n"
    "v_pk_mul_f32 v[172:173], v[172:173], v[240:241] op_sel:[0,1] op_sel_hi:[1,1]\n"
    "v_pk_mul_f32 v[174:175], v[174:175], v[240:241] op_sel:[0,1] op_sel_hi:[1,1]\n"
    "v_pk_fma_f32 v[168:169], v[120:121], v[240:241], v[168:169] op_sel:[0,0,0] op_sel_hi:[1,0,1]\n"
    "v_pk_fma_f32 v[170:171], v[122:123], v[240:241], v[170:171] op_sel:[0,0,0] op_sel_hi:[1,0,1]\n"
    "v_pk_fma_f32 v[172:173], v[124:125], v[240:241], v[172:173] op_sel:[0,0,0] op_sel_hi:[1,0,1]\n"
    "v_pk_fma_f32 v[174:175], v[126:127], v[240:241], v[174:175] op_sel:[0,0,0] op_sel_hi:[1,0,1]\n"
    "v_cvt_pk_bf16_f32 v120, v168, v169\n"
    "v_cvt_pk_bf16_f32 v121, v170, v171\n"
    "v_cvt_pk_bf16_f32 v122, v172, v173\n"
    "v_cvt_pk_bf16_f32 v123, v174, v175\n"
    "buffer_store_dwordx4 v[120:123], v30, s[60:63], s43 offen offset:3072 sc1\n"
    "v_min_u32 v250, 35, v4\n"
    "v_lshlrev_b32 v250, 3, v250\n"
    "s_cmp_ge_u32 s75, 30\n"
    "s_cbranch_scc0 .L3_active\n"
    "s_waitcnt vmcnt(0)\n"
    "s_barrier\n"
    "v_readlane_b32 s54, v246, s75\n"
    "v_readlane_b32 s55, v247, s75\n"
    "s_or_b32 s70, s70, -1\n"
    "s_or_b32 s71, s71, 0\n"
    "s_cmp_lg_u32 s36, 0\n"
    "s_cbranch_scc1 .L3_npi\n"
    "s_mov_b32 exec_lo, -1\n"
    "s_mov_b32 exec_hi, 0\n"
    "buffer_store_dwordx2 v[246:247], v250, s[64:67], s74 offen sc1\n"
    "s_mov_b64 exec, -1\n"
    ".L3_npi:\n"
    "s_cmp_lg_u32 s36, 0\n"
    "s_cbranch_scc1 .L3_end\n"
    "s_mov_b32 s72, -1\n"
    "s_mov_b32 s73, 0\n"
    "s_mov_b32 s50, 0\n"
    ".L3_pollI:\n"
    "s_mov_b64 exec, s[72:73]\n"
    "buffer_load_dwordx2 v[248:249], v245, s[64:67], 0 offen sc1\n"
    "s_add_u32 s50, s50, 1\n"
    "s_waitcnt vmcnt(0)\n"
    "v_cmp_eq_u64 vcc, s[54:55], v[248:249]\n"
    "s_mov_b64 exec, -1\n"
    "s_cmp_eq_u64 vcc, s[72:73]\n"
    "s_cbranch_scc1 .L3_goI\n"
    "s_cmp_ge_u32 s50, 0x80000\n"
    "s_cbranch_scc1 .L3_goI\n"
    "s_sleep 1\n"
    "s_and_b32 s76, s50, 63\n"
    "s_cbranch_scc1 .L3_pollI\n"
    "s_call_b64 s[52:53], .L3_refresh\n"
    "s_branch .L3_pollI\n"
    ".L3_goI:\n"
    "s_call_b64 s[52:53], .L3_refresh\n"
    "s_branch .L3_end\n"
    ".L3_active:\n"
    "s_lshl_b32 s37, s84, 2\n"
    "s_add_u32 s37, s37, s36\n"
    "s_mul_i32 s37, s37, 43\n"
    "s_lshr_b32 s37, s37, 8\n"
    "s_lshl_b32 s72, 0x11111111, s37\n"
    "s_mov_b32 s73, 0\n"
    "s_mul_i32 s39, s88, 6\n"
    "s_add_u32 s84, s84, s39\n"
    "s_lshl_b32 s37, s84, 2\n"
    "s_add_u32 s37, s37, s36\n"
    "s_lshl_b32 s37, s37, 18\n"
    "s_lshl_b32 s39, s86, 16\n"
    "s_add_u32 s37, s37, s39\n"
    "s_add_u32 s24, %[op_lo], s37\n"
    "s_addc_u32 s25, %[op_hi], 0\n"
    "s_and_b32 s25, s25, 0xffff\n"
    "s_mov_b32 s26, 65536\n"
    "s_mov_b32 s27, 0x20000\n"
    "v_lshlrev_b32 v7, 4, v4\n"
    "v_mov_b32 v5, v20\n"
    "v_mov_b32 v6, v31\n"
    "v_bfe_u32 v240, v4, 2, 2\n"
    "v_lshrrev_b32 v241, 5, v4\n"
    "v_bfe_u32 v243, v4, 4, 1\n"
    "s_mov_b32 s44, 0\n"
    "s_barrier\n"
    "s_mul_i32 s93, s84, 3\n"
    "s_add_u32 m0, s89, 0\n"
    "s_add_u32 s95, s87, 0\n"
    "buffer_load_dwordx4 v5, s[80:83], s95 offen lds\n"
    "buffer_load_dwordx4 v6, s[80:83], s95 offen offset:1024 lds\n"
    "s_add_u32 m0, s89, 8192\n"
    "s_add_u32 s95, s87, 81920\n"
    "buffer_load_dwordx4 v5, s[80:83], s95 offen lds\n"
    "buffer_load_dwordx4 v6, s[80:83], s95 offen offset:1024 lds\n"
    "s_add_u32 m0, s89, 16384\n"
    "s_add_u32 s95, s87, 163840\n"
    "buffer_load_dwordx4 v5, s[80:83], s95 offen lds\n"
    "buffer_load_dwordx4 v6, s[80:83], s95 offen offset:1024 lds\n"
    "s_add_u32 m0, s89, 24576\n"
    "s_add_u32 s95, s87, 245760\n"
    "buffer_load_dwordx4 v5, s[80:83], s95 offen lds\n"
    "buffer_load_dwordx4 v6, s[80:83], s95 offen offset:1024 lds\n"
    "v_and_b32 v242, 1, v4\n"
    "v_bfe_u32 v10, v4, 1, 1\n"
    "v_lshl_or_b32 v10, v242, 1, v10\n"
    "v_lshlrev_b32 v10, 3, v10\n"
    "v_lshl_add_u32 v10, v240, 6, v10\n"
    "v_lshl_add_u32 v10, v243, 5, v10\n"
    "v_lshl_add_u32 v10, v241, 10, v10\n"
    "s_and_b32 s37, s86, 1\n"
    "s_lshl_b32 s37, s37, 8\n"
    "v_add_u32 v10, s37, v10\n"
    "v_xor_b32 v13, 256, v10\n"
    "v_add_u32 v11, 49152, v10\n"
    "v_add_u32 v12, 98304, v10\n"
    "v_add_u32 v14, 49152, v13\n"
    "v_add_u32 v15, 98304, v13\n"
    "s_waitcnt vmcnt(9)\n"
    "s_barrier\n"
    "s_or_b32 s70, s70, -1\n"
    "s_or_b32 s71, s71, 0\n"
    "s_cmp_lg_u32 s36, 0\n"
    "s_cbranch_scc1 .L3_npo\n"
    "s_mov_b32 exec_lo, -1\n"
    "s_mov_b32 exec_hi, 0\n"
    "buffer_store_dwordx2 v[246:247], v250, s[64:67], s74 offen sc1\n"
    "s_mov_b64 exec, -1\n"
    ".L3_npo:\n"
    "v_readlane_b32 s54, v246, s75\n"
    "v_readlane_b32 s55, v247, s75\n"
    "s_add_u32 s60, %[op_lo], 16777216\n"
    "s_addc_u32 s61, %[op_hi], 0\n"
    "s_and_b32 s61, s61, 0xffff\n"
    "s_mov_b32 s62, 15728640\n"
    "s_mov_b32 s63, 0x20000\n"
    "s_add_u32 m0, s89, 32768\n"
    "s_add_u32 s95, s87, 327680\n"
    "buffer_load_dwordx4 v5, s[80:83], s95 offen lds\n"
    "buffer_load_dwordx4 v6, s[80:83], s95 offen offset:1024 lds\n"
    "s_add_u32 m0, s89, 40960\n"
    "s_add_u32 s95, s87, 409600\n"
    "buffer_load_dwordx4 v5, s[80:83], s95 offen lds\n"
    "buffer_load_dwordx4 v6, s[80:83], s95 offen offset:1024 lds\n"
    "s_add_u32 m0, s89, 49152\n"
    "s_add_u32 s95, s87, 491520\n"
    "buffer_load_dwordx4 v5, s[80:83], s95 offen lds\n"
    "buffer_load_dwordx4 v6, s[80:83], s95 offen offset:1024 lds\n"
    "s_add_u32 m0, s89, 57344\n"
    "s_add_u32 s95, s87, 573440\n"
    "buffer_load_dwordx4 v5, s[80:83], s95 offen lds\n"
    "buffer_load_dwordx4 v6, s[80:83], s95 offen offset:1024 lds\n"
    "s_add_u32 m0, s89, 65536\n"
    "s_add_u32 s95, s87, 655360\n"
    "buffer_load_dwordx4 v5, s[80:83], s95 offen lds\n"
    "buffer_load_dwordx4 v6, s[80:83], s95 offen offset:1024 lds\n"
    "s_add_u32 m0, s89, 73728\n"
    "s_add_u32 s95, s87, 737280\n"
    "buffer_load_dwordx4 v5, s[80:83], s95 offen lds\n"
    "buffer_load_dwordx4 v6, s[80:83], s95 offen offset:1024 lds\n"
    "s_add_u32 m0, s89, 81920\n"
    "s_add_u32 s95, s87, 819200\n"
    "buffer_load_dwordx4 v5, s[80:83], s95 offen lds\n"
    "buffer_load_dwordx4 v6, s[80:83], s95 offen offset:1024 lds\n"
    "s_add_u32 m0, s89, 90112\n"
    "s_add_u32 s95, s87, 901120\n"
    "buffer_load_dwordx4 v5, s[80:83], s95 offen lds\n"
    "buffer_load_dwordx4 v6, s[80:83], s95 offen offset:1024 lds\n"
    "s_add_u32 m0, s89, 98304\n"
    "s_add_u32 s95, s87, 983040\n"
    "buffer_load_dwordx4 v5, s[80:83], s95 offen lds\n"
    "buffer_load_dwordx4 v6, s[80:83], s95 offen offset:1024 lds\n"
    "s_add_u32 m0, s89, 106496\n"
    "s_add_u32 s95, s87, 1064960\n"
    "buffer_load_dwordx4 v5, s[80:83], s95 offen lds\n"
    "buffer_load_dwordx4 v6, s[80:83], s95 offen offset:1024 lds\n"
    "s_add_u32 m0, s89, 114688\n"
    "s_add_u32 s95, s87, 1146880\n"
    "buffer_load_dwordx4 v5, s[80:83], s95 offen lds\n"
    "buffer_load_dwordx4 v6, s[80:83], s95 offen offset:1024 lds\n"
    "s_add_u32 m0, s89, 122880\n"
    "s_add_u32 s95, s87, 1228800\n"
    "buffer_load_dwordx4 v5, s[80:83], s95 offen lds\n"
    "buffer_load_dwordx4 v6, s[80:83], s95 offen offset:1024 lds\n"
    "s_add_u32 m0, s89, 131072\n"
    "s_add_u32 s95, s90, 0\n"
    "buffer_load_dwordx4 v5, s[80:83], s95 offen lds\n"
    "buffer_load_dwordx4 v6, s[80:83], s95 offen offset:1024 lds\n"
    "s_add_u32 m0, s89, 139264\n"
    "s_add_u32 s95, s90, 81920\n"
    "buffer_load_dwordx4 v5, s[80:83], s95 offen lds\n"
    "buffer_load_dwordx4 v6, s[80:83], s95 offen offset:1024 lds\n"
    "s_mov_b64 exec, s[72:73]\n"
    "buffer_load_dwordx2 v[248:249], v245, s[64:67], 0 offen sc1\n"
    "s_mov_b64 exec, -1\n"
    "s_nop 3\n"
    "s_add_u32 m0, s89, 147456\n"
    "s_add_u32 s95, s90, 163840\n"
    "buffer_load_dwordx4 v5, s[80:83], s95 offen lds\n"
    "buffer_load_dwordx4 v6, s[80:83], s95 offen offset:1024 lds\n"
    "s_add_u32 m0, s89, 155648\n"
    "s_add_u32 s95, s90, 245760\n"
    "buffer_load_dwordx4 v5, s[80:83], s95 offen lds\n"
    "buffer_load_dwordx4 v6, s[80:83], s95 offen offset:1024 lds\n"
    "s_waitcnt vmcnt(4)\n"
    "s_mov_b64 exec, s[72:73]\n"
    "v_cmp_eq_u64 vcc, s[54:55], v[248:249]\n"
    "s_mov_b64 exec, -1\n"
    "s_cmp_eq_u64 vcc, s[72:73]\n"
    "s_cbranch_scc1 .L3_goO\n"
    "s_mov_b32 s50, 0\n"
    ".L3_pollO:\n"
    "s_mov_b64 exec, s[72:73]\n"
    "buffer_load_dwordx2 v[248:249], v245, s[64:67], 0 offen sc1\n"
    "s_add_u32 s50, s50, 1\n"
    "s_waitcnt vmcnt(0)\n"
    "v_cmp_eq_u64 vcc, s[54:55], v[248:249]\n"
    "s_mov_b64 exec, -1\n"
    "s_cmp_eq_u64 vcc, s[72:73]\n"
    "s_cbranch_scc1 .L3_goO\n"
    "s_cmp_ge_u32 s50, 0x80000\n"
    "s_cbranch_scc1 .L3_goO\n"
    "s_sleep 1\n"
    "s_and_b32 s76, s50, 63\n"
    "s_cbranch_scc1 .L3_pollO\n"
    "s_call_b64 s[52:53], .L3_refresh\n"
    "s_branch .L3_pollO\n"
    ".L3_goO:\n"
    "buffer_load_dwordx4 a[0:3], v7, s[24:27], s44 offen sc1\n"
    "buffer_load_dwordx4 a[4:7], v7, s[24:27], s44 offen offset:1024 sc1\n"
    "buffer_load_dwordx4 a[8:11], v7, s[24:27], s44 offen offset:2048 sc1\n"
    "buffer_load_dwordx4 a[12:15], v7, s[24:27], s44 offen offset:3072 sc1\n"
    "s_add_u32 s44, s44, 4096\n"
    "s_waitcnt vmcnt(4)\n"
    "s_barrier\n"
    "buffer_load_dwordx4 a[16:19], v7, s[24:27], s44 offen sc1\n"
    "buffer_load_dwordx4 a[20:23], v7, s[24:27], s44 offen offset:1024 sc1\n"
    "buffer_load_dwordx4 a[24:27], v7, s[24:27], s44 offen offset:2048 sc1\n"
    "buffer_load_dwordx4 a[28:31], v7, s[24:27], s44 offen offset:3072 sc1\n"
    "s_add_u32 s44, s44, 4096\n"
    "buffer_load_dwordx4 a[32:35], v7, s[24:27], s44 offen sc1\n"
    "buffer_load_dwordx4 a[36:39], v7, s[24:27], s44 offen offset:1024 sc1\n"
    "buffer_load_dwordx4 a[40:43], v7, s[24:27], s44 offen offset:2048 sc1\n"
    "buffer_load_dwordx4 a[44:47], v7, s[24:27], s44 offen offset:3072 sc1\n"
    "s_add_u32 s44, s44, 4096\n"
    "ds_read_b64_tr_b16 v[80:81], v10 offset:0\n"
    "ds_read_b64_tr_b16 v[82:83], v10 offset:512\n"
    "ds_read_b64_tr_b16 v[84:85], v13 offset:0\n"
    "ds_read_b64_tr_b16 v[86:87], v13 offset:512\n"
    "ds_read_b64_tr_b16 v[88:89], v10 offset:2048\n"
    "ds_read_b64_tr_b16 v[90:91], v10 offset:2560\n"
    "ds_read_b64_tr_b16 v[92:93], v13 offset:2048\n"
    "ds_read_b64_tr_b16 v[94:95], v13 offset:2560\n"
    "ds_read_b64_tr_b16 v[96:97], v10 offset:4096\n"
    "ds_read_b64_tr_b16 v[98:99], v10 offset:4608\n"
    "ds_read_b64_tr_b16 v[100:101], v13 offset:4096\n"
    "ds_read_b64_tr_b16 v[102:103], v13 offset:4608\n"
    "ds_read_b64_tr_b16 v[104:105], v10 offset:6144\n"
    "ds_read_b64_tr_b16 v[106:107], v10 offset:6656\n"
    "ds_read_b64_tr_b16 v[108:109], v13 offset:6144\n"
    "ds_read_b64_tr_b16 v[110:111], v13 offset:6656\n"
    "s_waitcnt vmcnt(8)\n"
    "s_waitcnt lgkmcnt(0)\n"
    "v_mfma_f32_32x32x16_bf16 v[48:63], v[80:83], a[0:3], 0\n"
    "ds_read_b64_tr_b16 v[128:129], v10 offset:8192\n"
    "ds_read_b64_tr_b16 v[130:131], v10 offset:8704\n"
    "ds_read_b64_tr_b16 v[132:133], v13 offset:8192\n"
    "v_mfma_f32_32x32x16_bf16 v[64:79], v[84:87], a[0:3], 0\n"
    "ds_read_b64_tr_b16 v[134:135], v13 offset:8704\n"
    "ds_read_b64_tr_b16 v[136:137], v10 offset:10240\n"
    "ds_read_b64_tr_b16 v[138:139], v10 offset:10752\n"
    "buffer_load_dwordx4 a[48:51], v7, s[24:27], s44 offen sc1\n"
    "v_mfma_f32_32x32x16_bf16 v[48:63], v[88:91], a[4:7], v[48:63]\n"
    "ds_read_b64_tr_b16 v[140:141], v13 offset:10240\n"
    "ds_read_b64_tr_b16 v[142:143], v13 offset:10752\n"
    "buffer_load_dwordx4 a[52:55], v7, s[24:27], s44 offen offset:1024 sc1\n"
    "v_mfma_f32_32x32x16_bf16 v[64:79], v[92:95], a[4:7], v[64:79]\n"
    "ds_read_b64_tr_b16 v[144:145], v10 offset:12288\n"
    "ds_read_b64_tr_b16 v[146:147], v10 offset:12800\n"
    "ds_read_b64_tr_b16 v[148:149], v13 offset:12288\n"
    "buffer_load_dwordx4 a[56:59], v7, s[24:27], s44 offen offset:2048 sc1\n"
    "v_mfma_f32_32x32x16_bf16 v[48:63], v[96:99], a[8:11], v[48:63]\n"
    "ds_read_b64_tr_b16 v[150:151], v13 offset:12800\n"
    "ds_read_b64_tr_b16 v[152:153], v10 offset:14336\n"
    "ds_read_b64_tr_b16 v[154:155], v10 offset:14848\n"
    "v_mfma_f32_32x32x16_bf16 v[64:79], v[100:103], a[8:11], v[64:79]\n"
    "ds_read_b64_tr_b16 v[156:157], v13 offset:14336\n"
    "ds_read_b64_tr_b16 v[158:159], v13 offset:14848\n"
    "buffer_load_dwordx4 a[60:63], v7, s[24:27], s44 offen offset:3072 sc1\n"
    "v_mfma_f32_32x32x16_bf16 v[48:63], v[104:107], a[12:15], v[48:63]\n"
    "s_add_u32 s44, s44, 4096\n"
    "v_mfma_f32_32x32x16_bf16 v[64:79], v[108:111], a[12:15], v[64:79]\n"
    "s_waitcnt vmcnt(8)\n"
    "s_waitcnt lgkmcnt(0)\n"
    "v_mfma_f32_32x32x16_bf16 v[48:63], v[128:131], a[16:19], v[48:63]\n"
    "ds_read_b64_tr_b16 v[80:81], v10 offset:16384\n"
    "ds_read_b64_tr_b16 v[82:83], v10 offset:16896\n"
    "ds_read_b64_tr_b16 v[84:85], v13 offset:16384\n"
    "v_mfma_f32_32x32x16_bf16 v[64:79], v[132:135], a[16:19], v[64:79]\n"
    "ds_read_b64_tr_b16 v[86:87], v13 offset:16896\n"
    "ds_read_b64_tr_b16 v[88:89], v10 offset:18432\n"
    "ds_read_b64_tr_b16 v[90:91], v10 offset:18944\n"
    "buffer_load_dwordx4 a[64:67], v7, s[24:27], s44 offen sc1\n"
    "v_mfma_f32_32x32x16_bf16 v[48:63], v[136:139], a[20:23], v[48:63]\n"
    "ds_read_b64_tr_b16 v[92:93], v13 offset:18432\n"
    "ds_read_b64_tr_b16 v[94:95], v13 offset:18944\n"
    "buffer_load_dwordx4 a[68:71], v7, s[24:27], s44 offen offset:1024 sc1\n"
    "v_mfma_f32_32x32x16_bf16 v[64:79], v[140:143], a[20:23], v[64:79]\n"
    "ds_read_b64_tr_b16 v[96:97], v10 offset:20480\n"
    "ds_read_b64_tr_b16 v[98:99], v10 offset:20992\n"
    "ds_read_b64_tr_b16 v[100:101], v13 offset:20480\n"
    "buffer_load_dwordx4 a[72:75], v7, s[24:27], s44 offen offset:2048 sc1\n"
    "v_mfma_f32_32x32x16_bf16 v[48:63], v[144:147], a[24:27], v[48:63]\n"
    "ds_read_b64_tr_b16 v[102:103], v13 offset:20992\n"
    "ds_read_b64_tr_b16 v[104:105], v10 offset:22528\n"
    "ds_read_b64_tr_b16 v[106:107], v10 offset:23040\n"
    "v_mfma_f32_32x32x16_bf16 v[64:79], v[148:151], a[24:27], v[64:79]\n"
    "ds_read_b64_tr_b16 v[108:109], v13 offset:22528\n"
    "ds_read_b64_tr_b16 v[110:111], v13 offset:23040\n"
    "buffer_load_dwordx4 a[76:79], v7, s[24:27], s44 offen offset:3072 sc1\n"
    "v_mfma_f32_32x32x16_bf16 v[48:63], v[152:155], a[28:31], v[48:63]\n"
    "s_add_u32 s44, s44, 4096\n"
    "v_mfma_f32_32x32x16_bf16 v[64:79], v[156:159], a[28:31], v[64:79]\n"
    "s_waitcnt vmcnt(8)\n"
    "s_waitcnt lgkmcnt(0)\n"
    "v_mfma_f32_32x32x16_bf16 v[48:63], v[80:83], a[32:35], v[48:63]\n"
    "ds_read_b64_tr_b16 v[128:129], v10 offset:24576\n"
    "ds_read_b64_tr_b16 v[130:131], v10 offset:25088\n"
    "ds_read_b64_tr_b16 v[132:133], v13 offset:24576\n"
    "v_mfma_f32_32x32x16_bf16 v[64:79], v[84:87], a[32:35], v[64:79]\n"
    "ds_read_b64_tr_b16 v[134:135], v13 offset:25088\n"
    "ds_read_b64_tr_b16 v[136:137], v10 offset:26624\n"
    "ds_read_b64_tr_b16 v[138:139], v10 offset:27136\n"
    "buffer_load_dwordx4 a[80:83], v7, s[24:27], s44 offen sc1\n"
    "v_mfma_f32_32x32x16_bf16 v[48:63], v[88:91], a[36:39], v[48:63]\n"
    "ds_read_b64_tr_b16 v[140:141], v13 offset:26624\n"
    "ds_read_b64_tr_b16 v[142:143], v13 offset:27136\n"
    "buffer_load_dwordx4 a[84:87], v7, s[24:27], s44 offen offset:1024 sc1\n"
    "v_mfma_f32_32x32x16_bf16 v[64:79], v[92:95], a[36:39], v[64:79]\n"
    "ds_read_b64_tr_b16 v[144:145], v10 offset:28672\n"
    "ds_read_b64_tr_b16 v[146:147], v10 offset:29184\n"
    "ds_read_b64_tr_b16 v[148:149], v13 offset:28672\n"
    "buffer_load_dwordx4 a[88:91], v7, s[24:27], s44 offen offset:2048 sc1\n"
    "v_mfma_f32_32x32x16_bf16 v[48:63], v[96:99], a[40:43], v[48:63]\n"
    "ds_read_b64_tr_b16 v[150:151], v13 offset:29184\n"
    "ds_read_b64_tr_b16 v[152:153], v10 offset:30720\n"
    "ds_read_b64_tr_b16 v[154:155], v10 offset:31232\n"
    "v_mfma_f32_32x32x16_bf16 v[64:79], v[100:103], a[40:43], v[64:79]\n"
    "ds_read_b64_tr_b16 v[156:157], v13 offset:30720\n"
    "ds_read_b64_tr_b16 v[158:159], v13 offset:31232\n"
    "buffer_load_dwordx4 a[92:95], v7, s[24:27], s44 offen offset:3072 sc1\n"
    "v_mfma_f32_32x32x16_bf16 v[48:63], v[104:107], a[44:47], v[48:63]\n"
    "s_add_u32 s44, s44, 4096\n"
    "v_mfma_f32_32x32x16_bf16 v[64:79], v[108:111], a[44:47], v[64:79]\n"
    "s_waitcnt vmcnt(8)\n"
    "s_waitcnt lgkmcnt(0)\n"
    "v_mfma_f32_32x32x16_bf16 v[48:63], v[128:131], a[48:51], v[48:63]\n"
    "ds_read_b64_tr_b16 v[80:81], v10 offset:32768\n"
    "ds_read_b64_tr_b16 v[82:83], v10 offset:33280\n"
    "ds_read_b64_tr_b16 v[84:85], v13 offset:32768\n"
    "v_mfma_f32_32x32x16_bf16 v[64:79], v[132:135], a[48:51], v[64:79]\n"
    "ds_read_b64_tr_b16 v[86:87], v13 offset:33280\n"
    "ds_read_b64_tr_b16 v[88:89], v10 offset:34816\n"
    "ds_read_b64_tr_b16 v[90:91], v10 offset:35328\n"
    "buffer_load_dwordx4 a[96:99], v7, s[24:27], s44 offen sc1\n"
    "v_mfma_f32_32x32x16_bf16 v[48:63], v[136:139], a[52:55], v[48:63]\n"
    "ds_read_b64_tr_b16 v[92:93], v13 offset:34816\n"
    "ds_read_b64_tr_b16 v[94:95], v13 offset:35328\n"
    "buffer_load_dwordx4 a[100:103], v7, s[24:27], s44 offen offset:1024 sc1\n"
    "v_mfma_f32_32x32x16_bf16 v[64:79], v[140:143], a[52:55], v[64:79]\n"
    "ds_read_b64_tr_b16 v[96:97], v10 offset:36864\n"
    "ds_read_b64_tr_b16 v[98:99], v10 offset:37376\n"
    "ds_read_b64_tr_b16 v[100:101], v13 offset:36864\n"
    "buffer_load_dwordx4 a[104:107], v7, s[24:27], s44 offen offset:2048 sc1\n"
    "v_mfma_f32_32x32x16_bf16 v[48:63], v[144:147], a[56:59], v[48:63]\n"
    "ds_read_b64_tr_b16 v[102:103], v13 offset:37376\n"
    "ds_read_b64_tr_b16 v[104:105], v10 offset:38912\n"
    "ds_read_b64_tr_b16 v[106:107], v10 offset:39424\n"
    "v_mfma_f32_32x32x16_bf16 v[64:79], v[148:151], a[56:59], v[64:79]\n"
    "ds_read_b64_tr_b16 v[108:109], v13 offset:38912\n"
    "ds_read_b64_tr_b16 v[110:111], v13 offset:39424\n"
    "buffer_load_dwordx4 a[108:111], v7, s[24:27], s44 offen offset:3072 sc1\n"
    "v_mfma_f32_32x32x16_bf16 v[48:63], v[152:155], a[60:63], v[48:63]\n"
    "s_add_u32 s44, s44, 4096\n"
    "v_mfma_f32_32x32x16_bf16 v[64:79], v[156:159], a[60:63], v[64:79]\n"
    "s_waitcnt vmcnt(8)\n"
    "s_waitcnt lgkmcnt(0)\n"
    "v_mfma_f32_32x32x16_bf16 v[48:63], v[80:83], a[64:67], v[48:63]\n"
    "ds_read_b64_tr_b16 v[128:129], v10 offset:40960\n"
    "ds_read_b64_tr_b16 v[130:131], v10 offset:41472\n"
    "ds_read_b64_tr_b16 v[132:133], v13 offset:40960\n"
    "v_mfma_f32_32x32x16_bf16 v[64:79], v[84:87], a[64:67], v[64:79]\n"
    "ds_read_b64_tr_b16 v[134:135], v13 offset:41472\n"
    "ds_read_b64_tr_b16 v[136:137], v10 offset:43008\n"
    "ds_read_b64_tr_b16 v[138:139], v10 offset:43520\n"
    "buffer_load_dwordx4 a[112:115], v7, s[24:27], s44 offen sc1\n"
    "v_mfma_f32_32x32x16_bf16 v[48:63], v[88:91], a[68:71], v[48:63]\n"
    "ds_read_b64_tr_b16 v[140:141], v13 offset:43008\n"
    "ds_read_b64_tr_b16 v[142:143], v13 offset:43520\n"
    "buffer_load_dwordx4 a[116:119], v7, s[24:27], s44 offen offset:1024 sc1\n"
    "v_mfma_f32_32x32x16_bf16 v[64:79], v[92:95], a[68:71], v[64:79]\n"
    "ds_read_b64_tr_b16 v[144:145], v10 offset:45056\n"
    "ds_read_b64_tr_b16 v[146:147], v10 offset:45568\n"
    "ds_read_b64_tr_b16 v[148:149], v13 offset:45056\n"
    "buffer_load_dwordx4 a[120:123], v7, s[24:27], s44 offen offset:2048 sc1\n"
    "v_mfma_f32_32x32x16_bf16 v[48:63], v[96:99], a[72:75], v[48:63]\n"
    "ds_read_b64_tr_b16 v[150:151], v13 offset:45568\n"
    "ds_read_b64_tr_b16 v[152:153], v10 offset:47104\n"
    "ds_read_b64_tr_b16 v[154:155], v10 offset:47616\n"
    "v_mfma_f32_32x32x16_bf16 v[64:79], v[100:103], a[72:75], v[64:79]\n"
    "ds_read_b64_tr_b16 v[156:157], v13 offset:47104\n"
    "ds_read_b64_tr_b16 v[158:159], v13 offset:47616\n"
    "buffer_load_dwordx4 a[124:127], v7, s[24:27], s44 offen offset:3072 sc1\n"
    "v_mfma_f32_32x32x16_bf16 v[48:63], v[104:107], a[76:79], v[48:63]\n"
    "s_add_u32 s44, s44, 4096\n"
    "v_mfma_f32_32x32x16_bf16 v[64:79], v[108:111], a[76:79], v[64:79]\n"
    "s_waitcnt vmcnt(8)\n"
    "s_waitcnt lgkmcnt(0)\n"
    "v_mfma_f32_32x32x16_bf16 v[48:63], v[128:131], a[80:83], v[48:63]\n"
    "ds_read_b64_tr_b16 v[80:81], v11 offset:0\n"
    "ds_read_b64_tr_b16 v[82:83], v11 offset:512\n"
    "ds_read_b64_tr_b16 v[84:85], v14 offset:0\n"
    "v_mfma_f32_32x32x16_bf16 v[64:79], v[132:135], a[80:83], v[64:79]\n"
    "ds_read_b64_tr_b16 v[86:87], v14 offset:512\n"
    "ds_read_b64_tr_b16 v[88:89], v11 offset:2048\n"
    "ds_read_b64_tr_b16 v[90:91], v11 offset:2560\n"
    "buffer_load_dwordx4 a[128:131], v7, s[24:27], s44 offen sc1\n"
    "v_mfma_f32_32x32x16_bf16 v[48:63], v[136:139], a[84:87], v[48:63]\n"
    "ds_read_b64_tr_b16 v[92:93], v14 offset:2048\n"
    "ds_read_b64_tr_b16 v[94:95], v14 offset:2560\n"
    "buffer_load_dwordx4 a[132:135], v7, s[24:27], s44 offen offset:1024 sc1\n"
    "v_mfma_f32_32x32x16_bf16 v[64:79], v[140:143], a[84:87], v[64:79]\n"
    "ds_read_b64_tr_b16 v[96:97], v11 offset:4096\n"
    "ds_read_b64_tr_b16 v[98:99], v11 offset:4608\n"
    "ds_read_b64_tr_b16 v[100:101], v14 offset:4096\n"
    "buffer_load_dwordx4 a[136:139], v7, s[24:27], s44 offen offset:2048 sc1\n"
    "v_mfma_f32_32x32x16_bf16 v[48:63], v[144:147], a[88:91], v[48:63]\n"
    "ds_read_b64_tr_b16 v[102:103], v14 offset:4608\n"
    "ds_read_b64_tr_b16 v[104:105], v11 offset:6144\n"
    "ds_read_b64_tr_b16 v[106:107], v11 offset:6656\n"
    "v_mfma_f32_32x32x16_bf16 v[64:79], v[148:151], a[88:91], v[64:79]\n"
    "ds_read_b64_tr_b16 v[108:109], v14 offset:6144\n"
    "ds_read_b64_tr_b16 v[110:111], v14 offset:6656\n"
    "buffer_load_dwordx4 a[140:143], v7, s[24:27], s44 offen offset:3072 sc1\n"
    "v_mfma_f32_32x32x16_bf16 v[48:63], v[152:155], a[92:95], v[48:63]\n"
    "s_add_u32 s44, s44, 4096\n"
    "v_mfma_f32_32x32x16_bf16 v[64:79], v[156:159], a[92:95], v[64:79]\n"
    "s_waitcnt vmcnt(8)\n"
    "s_waitcnt lgkmcnt(0)\n"
    "v_mfma_f32_32x32x16_bf16 v[48:63], v[80:83], a[96:99], v[48:63]\n"
    "ds_read_b64_tr_b16 v[128:129], v11 offset:8192\n"
    "ds_read_b64_tr_b16 v[130:131], v11 offset:8704\n"
    "ds_read_b64_tr_b16 v[132:133], v14 offset:8192\n"
    "v_mfma_f32_32x32x16_bf16 v[64:79], v[84:87], a[96:99], v[64:79]\n"
    "ds_read_b64_tr_b16 v[134:135], v14 offset:8704\n"
    "ds_read_b64_tr_b16 v[136:137], v11 offset:10240\n"
    "ds_read_b64_tr_b16 v[138:139], v11 offset:10752\n"
    "buffer_load_dwordx4 a[144:147], v7, s[24:27], s44 offen sc1\n"
    "v_mfma_f32_32x32x16_bf16 v[48:63], v[88:91], a[100:103], v[48:63]\n"
    "ds_read_b64_tr_b16 v[140:141], v14 offset:10240\n"
    "ds_read_b64_tr_b16 v[142:143], v14 offset:10752\n"
    "buffer_load_dwordx4 a[148:151], v7, s[24:27], s44 offen offset:1024 sc1\n"
    "v_mfma_f32_32x32x16_bf16 v[64:79], v[92:95], a[100:103], v[64:79]\n"
    "ds_read_b64_tr_b16 v[144:145], v11 offset:12288\n"
    "ds_read_b64_tr_b16 v[146:147], v11 offset:12800\n"
    "ds_read_b64_tr_b16 v[148:149], v14 offset:12288\n"
    "buffer_load_dwordx4 a[152:155], v7, s[24:27], s44 offen offset:2048 sc1\n"
    "v_mfma_f32_32x32x16_bf16 v[48:63], v[96:99], a[104:107], v[48:63]\n"
    "ds_read_b64_tr_b16 v[150:151], v14 offset:12800\n"
    "ds_read_b64_tr_b16 v[152:153], v11 offset:14336\n"
    "ds_read_b64_tr_b16 v[154:155], v11 offset:14848\n"
    "v_mfma_f32_32x32x16_bf16 v[64:79], v[100:103], a[104:107], v[64:79]\n"
    "ds_read_b64_tr_b16 v[156:157], v14 offset:14336\n"
    "ds_read_b64_tr_b16 v[158:159], v14 offset:14848\n"
    "buffer_load_dwordx4 a[156:159], v7, s[24:27], s44 offen offset:3072 sc1\n"
    "v_mfma_f32_32x32x16_bf16 v[48:63], v[104:107], a[108:111], v[48:63]\n"
    "s_add_u32 s44, s44, 4096\n"
    "v_mfma_f32_32x32x16_bf16 v[64:79], v[108:111], a[108:111], v[64:79]\n"
    "s_waitcnt vmcnt(8)\n"
    "s_waitcnt lgkmcnt(0)\n"
    "v_mfma_f32_32x32x16_bf16 v[48:63], v[128:131], a[112:115], v[48:63]\n"
    "ds_read_b64_tr_b16 v[80:81], v11 offset:16384\n"
    "ds_read_b64_tr_b16 v[82:83], v11 offset:16896\n"
    "ds_read_b64_tr_b16 v[84:85], v14 offset:16384\n"
    "v_mfma_f32_32x32x16_bf16 v[64:79], v[132:135], a[112:115], v[64:79]\n"
    "ds_read_b64_tr_b16 v[86:87], v14 offset:16896\n"
    "ds_read_b64_tr_b16 v[88:89], v11 offset:18432\n"
    "ds_read_b64_tr_b16 v[90:91], v11 offset:18944\n"
    "buffer_load_dwordx4 a[160:163], v7, s[24:27], s44 offen sc1\n"
    "v_mfma_f32_32x32x16_bf16 v[48:63], v[136:139], a[116:119], v[48:63]\n"
    "ds_read_b64_tr_b16 v[92:93], v14 offset:18432\n"
    "ds_read_b64_tr_b16 v[94:95], v14 offset:18944\n"
    "buffer_load_dwordx4 a[164:167], v7, s[24:27], s44 offen offset:1024 sc1\n"
    "v_mfma_f32_32x32x16_bf16 v[64:79], v[140:143], a[116:119], v[64:79]\n"
    "ds_read_b64_tr_b16 v[96:97], v11 offset:20480\n"
    "ds_read_b64_tr_b16 v[98:99], v11 offset:20992\n"
    "ds_read_b64_tr_b16 v[100:101], v14 offset:20480\n"
    "buffer_load_dwordx4 a[168:171], v7, s[24:27], s44 offen offset:2048 sc1\n"
    "v_mfma_f32_32x32x16_bf16 v[48:63], v[144:147], a[120:123], v[48:63]\n"
    "ds_read_b64_tr_b16 v[102:103], v14 offset:20992\n"
    "ds_read_b64_tr_b16 v[104:105], v11 offset:22528\n"
    "ds_read_b64_tr_b16 v[106:107], v11 offset:23040\n"
    "v_mfma_f32_32x32x16_bf16 v[64:79], v[148:151], a[120:123], v[64:79]\n"
    "ds_read_b64_tr_b16 v[108:109], v14 offset:22528\n"
    "ds_read_b64_tr_b16 v[110:111], v14 offset:23040\n"
    "buffer_load_dwordx4 a[172:175], v7, s[24:27], s44 offen offset:3072 sc1\n"
    "v_mfma_f32_32x32x16_bf16 v[48:63], v[152:155], a[124:127], v[48:63]\n"
    "s_add_u32 s44, s44, 4096\n"
    "v_mfma_f32_32x32x16_bf16 v[64:79], v[156:159], a[124:127], v[64:79]\n"
    "s_waitcnt vmcnt(8)\n"
    "s_waitcnt lgkmcnt(0)\n"
    "v_mfma_f32_32x32x16_bf16 v[48:63], v[80:83], a[128:131], v[48:63]\n"
    "ds_read_b64_tr_b16 v[128:129], v11 offset:24576\n"
    "ds_read_b64_tr_b16 v[130:131], v11 offset:25088\n"
    "ds_read_b64_tr_b16 v[132:133], v14 offset:24576\n"
    "v_mfma_f32_32x32x16_bf16 v[64:79], v[84:87], a[128:131], v[64:79]\n"
    "ds_read_b64_tr_b16 v[134:135], v14 offset:25088\n"
    "ds_read_b64_tr_b16 v[136:137], v11 offset:26624\n"
    "ds_read_b64_tr_b16 v[138:139], v11 offset:27136\n"
    "buffer_load_dwordx4 a[176:179], v7, s[24:27], s44 offen sc1\n"
    "v_mfma_f32_32x32x16_bf16 v[48:63], v[88:91], a[132:135], v[48:63]\n"
    "ds_read_b64_tr_b16 v[140:141], v14 offset:26624\n"
    "ds_read_b64_tr_b16 v[142:143], v14 offset:27136\n"
    "buffer_load_dwordx4 a[180:183], v7, s[24:27], s44 offen offset:1024 sc1\n"
    "v_mfma_f32_32x32x16_bf16 v[64:79], v[92:95], a[132:135], v[64:79]\n"
    "ds_read_b64_tr_b16 v[144:145], v11 offset:28672\n"
    "ds_read_b64_tr_b16 v[146:147], v11 offset:29184\n"
    "ds_read_b64_tr_b16 v[148:149], v14 offset:28672\n"
    "buffer_load_dwordx4 a[184:187], v7, s[24:27], s44 offen offset:2048 sc1\n"
    "v_mfma_f32_32x32x16_bf16 v[48:63], v[96:99], a[136:139], v[48:63]\n"
    "ds_read_b64_tr_b16 v[150:151], v14 offset:29184\n"
    "ds_read_b64_tr_b16 v[152:153], v11 offset:30720\n"
    "ds_read_b64_tr_b16 v[154:155], v11 offset:31232\n"
    "v_mfma_f32_32x32x16_bf16 v[64:79], v[100:103], a[136:139], v[64:79]\n"
    "ds_read_b64_tr_b16 v[156:157], v14 offset:30720\n"
    "ds_read_b64_tr_b16 v[158:159], v14 offset:31232\n"
    "buffer_load_dwordx4 a[188:191], v7, s[24:27], s44 offen offset:3072 sc1\n"
    "v_mfma_f32_32x32x16_bf16 v[48:63], v[104:107], a[140:143], v[48:63]\n"
    "s_add_u32 s44, s44, 4096\n"
    "v_mfma_f32_32x32x16_bf16 v[64:79], v[108:111], a[140:143], v[64:79]\n"
    "s_waitcnt vmcnt(8)\n"
    "s_waitcnt lgkmcnt(0)\n"
    "v_mfma_f32_32x32x16_bf16 v[48:63], v[128:131], a[144:147], v[48:63]\n"
    "ds_read_b64_tr_b16 v[80:81], v11 offset:32768\n"
    "ds_read_b64_tr_b16 v[82:83], v11 offset:33280\n"
    "ds_read_b64_tr_b16 v[84:85], v14 offset:32768\n"
    "v_mfma_f32_32x32x16_bf16 v[64:79], v[132:135], a[144:147], v[64:79]\n"
    "ds_read_b64_tr_b16 v[86:87], v14 offset:33280\n"
    "ds_read_b64_tr_b16 v[88:89], v11 offset:34816\n"
    "ds_read_b64_tr_b16 v[90:91], v11 offset:35328\n"
    "buffer_load_dwordx4 a[192:195], v7, s[24:27], s44 offen sc1\n"
    "v_mfma_f32_32x32x16_bf16 v[48:63], v[136:139], a[148:151], v[48:63]\n"
    "ds_read_b64_tr_b16 v[92:93], v14 offset:34816\n"
    "ds_read_b64_tr_b16 v[94:95], v14 offset:35328\n"
    "buffer_load_dwordx4 a[196:199], v7, s[24:27], s44 offen offset:1024 sc1\n"
    "v_mfma_f32_32x32x16_bf16 v[64:79], v[140:143], a[148:151], v[64:79]\n"
    "ds_read_b64_tr_b16 v[96:97], v11 offset:36864\n"
    "ds_read_b64_tr_b16 v[98:99], v11 offset:37376\n"
    "ds_read_b64_tr_b16 v[100:101], v14 offset:36864\n"
    "buffer_load_dwordx4 a[200:203], v7, s[24:27], s44 offen offset:2048 sc1\n"
    "v_mfma_f32_32x32x16_bf16 v[48:63], v[144:147], a[152:155], v[48:63]\n"
    "ds_read_b64_tr_b16 v[102:103], v14 offset:37376\n"
    "ds_read_b64_tr_b16 v[104:105], v11 offset:38912\n"
    "ds_read_b64_tr_b16 v[106:107], v11 offset:39424\n"
    "v_mfma_f32_32x32x16_bf16 v[64:79], v[148:151], a[152:155], v[64:79]\n"
    "ds_read_b64_tr_b16 v[108:109], v14 offset:38912\n"
    "ds_read_b64_tr_b16 v[110:111], v14 offset:39424\n"
    "buffer_load_dwordx4 a[204:207], v7, s[24:27], s44 offen offset:3072 sc1\n"
    "v_mfma_f32_32x32x16_bf16 v[48:63], v[152:155], a[156:159], v[48:63]\n"
    "s_add_u32 s44, s44, 4096\n"
    "v_mfma_f32_32x32x16_bf16 v[64:79], v[156:159], a[156:159], v[64:79]\n"
    "s_waitcnt vmcnt(8)\n"
    "s_waitcnt lgkmcnt(0)\n"
    "v_mfma_f32_32x32x16_bf16 v[48:63], v[80:83], a[160:163], v[48:63]\n"
    "ds_read_b64_tr_b16 v[128:129], v11 offset:40960\n"
    "ds_read_b64_tr_b16 v[130:131], v11 offset:41472\n"
    "ds_read_b64_tr_b16 v[132:133], v14 offset:40960\n"
    "v_mfma_f32_32x32x16_bf16 v[64:79], v[84:87], a[160:163], v[64:79]\n"
    "ds_read_b64_tr_b16 v[134:135], v14 offset:41472\n"
    "ds_read_b64_tr_b16 v[136:137], v11 offset:43008\n"
    "ds_read_b64_tr_b16 v[138:139], v11 offset:43520\n"
    "buffer_load_dwordx4 a[208:211], v7, s[24:27], s44 offen sc1\n"
    "v_mfma_f32_32x32x16_bf16 v[48:63], v[88:91], a[164:167], v[48:63]\n"
    "ds_read_b64_tr_b16 v[140:141], v14 offset:43008\n"
    "ds_read_b64_tr_b16 v[142:143], v14 offset:43520\n"
    "buffer_load_dwordx4 a[212:215], v7, s[24:27], s44 offen offset:1024 sc1\n"
    "v_mfma_f32_32x32x16_bf16 v[64:79], v[92:95], a[164:167], v[64:79]\n"
    "ds_read_b64_tr_b16 v[144:145], v11 offset:45056\n"
    "ds_read_b64_tr_b16 v[146:147], v11 offset:45568\n"
    "ds_read_b64_tr_b16 v[148:149], v14 offset:45056\n"
    "buffer_load_dwordx4 a[216:219], v7, s[24:27], s44 offen offset:2048 sc1\n"
    "v_mfma_f32_32x32x16_bf16 v[48:63], v[96:99], a[168:171], v[48:63]\n"
    "ds_read_b64_tr_b16 v[150:151], v14 offset:45568\n"
    "ds_read_b64_tr_b16 v[152:153], v11 offset:47104\n"
    "ds_read_b64_tr_b16 v[154:155], v11 offset:47616\n"
    "v_mfma_f32_32x32x16_bf16 v[64:79], v[100:103], a[168:171], v[64:79]\n"
    "ds_read_b64_tr_b16 v[156:157], v14 offset:47104\n"
    "ds_read_b64_tr_b16 v[158:159], v14 offset:47616\n"
    "buffer_load_dwordx4 a[220:223], v7, s[24:27], s44 offen offset:3072 sc1\n"
    "v_mfma_f32_32x32x16_bf16 v[48:63], v[104:107], a[172:175], v[48:63]\n"
    "s_add_u32 s44, s44, 4096\n"
    "v_mfma_f32_32x32x16_bf16 v[64:79], v[108:111], a[172:175], v[64:79]\n"
    "s_waitcnt vmcnt(8)\n"
    "s_waitcnt lgkmcnt(0)\n"
    "v_mfma_f32_32x32x16_bf16 v[48:63], v[128:131], a[176:179], v[48:63]\n"
    "ds_read_b64_tr_b16 v[80:81], v12 offset:0\n"
    "ds_read_b64_tr_b16 v[82:83], v12 offset:512\n"
    "ds_read_b64_tr_b16 v[84:85], v15 offset:0\n"
    "v_mfma_f32_32x32x16_bf16 v[64:79], v[132:135], a[176:179], v[64:79]\n"
    "ds_read_b64_tr_b16 v[86:87], v15 offset:512\n"
    "ds_read_b64_tr_b16 v[88:89], v12 offset:2048\n"
    "ds_read_b64_tr_b16 v[90:91], v12 offset:2560\n"
    "buffer_load_dwordx4 a[224:227], v7, s[24:27], s44 offen sc1\n"
    "v_mfma_f32_32x32x16_bf16 v[48:63], v[136:139], a[180:183], v[48:63]\n"
    "ds_read_b64_tr_b16 v[92:93], v15 offset:2048\n"
    "ds_read_b64_tr_b16 v[94:95], v15 offset:2560\n"
    "buffer_load_dwordx4 a[228:231], v7, s[24:27], s44 offen offset:1024 sc1\n"
    "v_mfma_f32_32x32x16_bf16 v[64:79], v[140:143], a[180:183], v[64:79]\n"
    "ds_read_b64_tr_b16 v[96:97], v12 offset:4096\n"
    "ds_read_b64_tr_b16 v[98:99], v12 offset:4608\n"
    "ds_read_b64_tr_b16 v[100:101], v15 offset:4096\n"
    "buffer_load_dwordx4 a[232:235], v7, s[24:27], s44 offen offset:2048 sc1\n"
    "v_mfma_f32_32x32x16_bf16 v[48:63], v[144:147], a[184:187], v[48:63]\n"
    "ds_read_b64_tr_b16 v[102:103], v15 offset:4608\n"
    "ds_read_b64_tr_b16 v[104:105], v12 offset:6144\n"
    "ds_read_b64_tr_b16 v[106:107], v12 offset:6656\n"
    "v_mfma_f32_32x32x16_bf16 v[64:79], v[148:151], a[184:187], v[64:79]\n"
    "ds_read_b64_tr_b16 v[108:109], v15 offset:6144\n"
    "ds_read_b64_tr_b16 v[110:111], v15 offset:6656\n"
    "buffer_load_dwordx4 a[236:239], v7, s[24:27], s44 offen offset:3072 sc1\n"
    "v_mfma_f32_32x32x16_bf16 v[48:63], v[152:155], a[188:191], v[48:63]\n"
    "s_add_u32 s44, s44, 4096\n"
    "v_mfma_f32_32x32x16_bf16 v[64:79], v[156:159], a[188:191], v[64:79]\n"
    "s_waitcnt vmcnt(8)\n"
    "s_waitcnt lgkmcnt(0)\n"
    "v_mfma_f32_32x32x16_bf16 v[48:63], v[80:83], a[192:195], v[48:63]\n"
    "s_barrier\n"
    "ds_read_b64_tr_b16 v[128:129], v12 offset:8192\n"
    "ds_read_b64_tr_b16 v[130:131], v12 offset:8704\n"
    "ds_read_b64_tr_b16 v[132:133], v15 offset:8192\n"
    "v_mfma_f32_32x32x16_bf16 v[64:79], v[84:87], a[192:195], v[64:79]\n"
    "ds_read_b64_tr_b16 v[134:135], v15 offset:8704\n"
    "ds_read_b64_tr_b16 v[136:137], v12 offset:10240\n"
    "ds_read_b64_tr_b16 v[138:139], v12 offset:10752\n"
    "buffer_load_dwordx4 a[240:243], v7, s[24:27], s44 offen sc1\n"
    "v_mfma_f32_32x32x16_bf16 v[48:63], v[88:91], a[196:199], v[48:63]\n"
    "ds_read_b64_tr_b16 v[140:141], v15 offset:10240\n"
    "ds_read_b64_tr_b16 v[142:143], v15 offset:10752\n"
    "buffer_load_dwordx4 a[244:247], v7, s[24:27], s44 offen offset:1024 sc1\n"
    "v_mfma_f32_32x32x16_bf16 v[64:79], v[92:95], a[196:199], v[64:79]\n"
    "ds_read_b64_tr_b16 v[144:145], v12 offset:12288\n"
    "ds_read_b64_tr_b16 v[146:147], v12 offset:12800\n"
    "ds_read_b64_tr_b16 v[148:149], v15 offset:12288\n"
    "buffer_load_dwordx4 a[248:251], v7, s[24:27], s44 offen offset:2048 sc1\n"
    "v_mfma_f32_32x32x16_bf16 v[48:63], v[96:99], a[200:203], v[48:63]\n"
    "ds_read_b64_tr_b16 v[150:151], v15 offset:12800\n"
    "ds_read_b64_tr_b16 v[152:153], v12 offset:14336\n"
    "ds_read_b64_tr_b16 v[154:155], v12 offset:14848\n"
    "v_mfma_f32_32x32x16_bf16 v[64:79], v[100:103], a[200:203], v[64:79]\n"
    "ds_read_b64_tr_b16 v[156:157], v15 offset:14336\n"
    "ds_read_b64_tr_b16 v[158:159], v15 offset:14848\n"
    "buffer_load_dwordx4 a[252:255], v7, s[24:27], s44 offen offset:3072 sc1\n"
    "v_mfma_f32_32x32x16_bf16 v[48:63], v[104:107], a[204:207], v[48:63]\n"
    "s_add_u32 s44, s44, 4096\n"
    "v_mfma_f32_32x32x16_bf16 v[64:79], v[108:111], a[204:207], v[64:79]\n"
    "s_waitcnt vmcnt(8)\n"
    "s_waitcnt lgkmcnt(0)\n"
    "v_mfma_f32_32x32x16_bf16 v[48:63], v[128:131], a[208:211], v[48:63]\n"
    "ds_read_b64_tr_b16 v[80:81], v12 offset:16384\n"
    "ds_read_b64_tr_b16 v[82:83], v12 offset:16896\n"
    "ds_read_b64_tr_b16 v[84:85], v15 offset:16384\n"
    "v_mfma_f32_32x32x16_bf16 v[64:79], v[132:135], a[208:211], v[64:79]\n"
    "ds_read_b64_tr_b16 v[86:87], v15 offset:16896\n"
    "ds_read_b64_tr_b16 v[88:89], v12 offset:18432\n"
    "ds_read_b64_tr_b16 v[90:91], v12 offset:18944\n"
    "s_add_u32 m0, s89, 0\n"
    "s_add_u32 s95, s90, 327680\n"
    "v_mfma_f32_32x32x16_bf16 v[48:63], v[136:139], a[212:215], v[48:63]\n"
    "ds_read_b64_tr_b16 v[92:93], v15 offset:18432\n"
    "ds_read_b64_tr_b16 v[94:95], v15 offset:18944\n"
    "buffer_load_dwordx4 v5, s[80:83], s95 offen lds\n"
    "buffer_load_dwordx4 v6, s[80:83], s95 offen offset:1024 lds\n"
    "v_mfma_f32_32x32x16_bf16 v[64:79], v[140:143], a[212:215], v[64:79]\n"
    "ds_read_b64_tr_b16 v[96:97], v12 offset:20480\n"
    "ds_read_b64_tr_b16 v[98:99], v12 offset:20992\n"
    "ds_read_b64_tr_b16 v[100:101], v15 offset:20480\n"
    "s_add_u32 m0, s89, 8192\n"
    "s_add_u32 s95, s90, 409600\n"
    "v_mfma_f32_32x32x16_bf16 v[48:63], v[144:147], a[216:219], v[48:63]\n"
    "ds_read_b64_tr_b16 v[102:103], v15 offset:20992\n"
    "ds_read_b64_tr_b16 v[104:105], v12 offset:22528\n"
    "ds_read_b64_tr_b16 v[106:107], v12 offset:23040\n"
    "buffer_load_dwordx4 v5, s[80:83], s95 offen lds\n"
    "v_mfma_f32_32x32x16_bf16 v[64:79], v[148:151], a[216:219], v[64:79]\n"
    "ds_read_b64_tr_b16 v[108:109], v15 offset:22528\n"
    "ds_read_b64_tr_b16 v[110:111], v15 offset:23040\n"
    "buffer_load_dwordx4 v6, s[80:83], s95 offen offset:1024 lds\n"
    "s_add_u32 m0, s89, 16384\n"
    "v_mfma_f32_32x32x16_bf16 v[48:63], v[152:155], a[220:223], v[48:63]\n"
    "s_add_u32 s95, s90, 491520\n"
    "buffer_load_dwordx4 v5, s[80:83], s95 offen lds\n"
    "v_mfma_f32_32x32x16_bf16 v[64:79], v[156:159], a[220:223], v[64:79]\n"
    "buffer_load_dwordx4 v6, s[80:83], s95 offen offset:1024 lds\n"
    "s_waitcnt vmcnt(10)\n"
    "s_waitcnt lgkmcnt(0)\n"
    "v_mfma_f32_32x32x16_bf16 v[48:63], v[80:83], a[224:227], v[48:63]\n"
    "ds_read_b64_tr_b16 v[128:129], v12 offset:24576\n"
    "ds_read_b64_tr_b16 v[130:131], v12 offset:25088\n"
    "ds_read_b64_tr_b16 v[132:133], v15 offset:24576\n"
    "v_mfma_f32_32x32x16_bf16 v[64:79], v[84:87], a[224:227], v[64:79]\n"
    "ds_read_b64_tr_b16 v[134:135], v15 offset:25088\n"
    "ds_read_b64_tr_b16 v[136:137], v12 offset:26624\n"
    "ds_read_b64_tr_b16 v[138:139], v12 offset:27136\n"
    "s_add_u32 m0, s89, 24576\n"
    "s_add_u32 s95, s90, 573440\n"
    "v_mfma_f32_32x32x16_bf16 v[48:63], v[88:91], a[228:231], v[48:63]\n"
    "ds_read_b64_tr_b16 v[140:141], v15 offset:26624\n"
    "ds_read_b64_tr_b16 v[142:143], v15 offset:27136\n"
    "buffer_load_dwordx4 v5, s[80:83], s95 offen lds\n"
    "buffer_load_dwordx4 v6, s[80:83], s95 offen offset:1024 lds\n"
    "v_mfma_f32_32x32x16_bf16 v[64:79], v[92:95], a[228:231], v[64:79]\n"
    "ds_read_b64_tr_b16 v[144:145], v12 offset:28672\n"
    "ds_read_b64_tr_b16 v[146:147], v12 offset:29184\n"
    "ds_read_b64_tr_b16 v[148:149], v15 offset:28672\n"
    "s_add_u32 m0, s89, 32768\n"
    "s_add_u32 s95, s90, 655360\n"
    "v_mfma_f32_32x32x16_bf16 v[48:63], v[96:99], a[232:235], v[48:63]\n"
    "ds_read_b64_tr_b16 v[150:151], v15 offset:29184\n"
    "ds_read_b64_tr_b16 v[152:153], v12 offset:30720\n"
    "ds_read_b64_tr_b16 v[154:155], v12 offset:31232\n"
    "buffer_load_dwordx4 v5, s[80:83], s95 offen lds\n"
    "v_mfma_f32_32x32x16_bf16 v[64:79], v[100:103], a[232:235], v[64:79]\n"
    "ds_read_b64_tr_b16 v[156:157], v15 offset:30720\n"
    "ds_read_b64_tr_b16 v[158:159], v15 offset:31232\n"
    "buffer_load_dwordx4 v6, s[80:83], s95 offen offset:1024 lds\n"
    "s_add_u32 m0, s89, 40960\n"
    "v_mfma_f32_32x32x16_bf16 v[48:63], v[104:107], a[236:239], v[48:63]\n"
    "s_add_u32 s95, s90, 737280\n"
    "buffer_load_dwordx4 v5, s[80:83], s95 offen lds\n"
    "v_mfma_f32_32x32x16_bf16 v[64:79], v[108:111], a[236:239], v[64:79]\n"
    "buffer_load_dwordx4 v6, s[80:83], s95 offen offset:1024 lds\n"
    "s_waitcnt vmcnt(12)\n"
    "s_waitcnt lgkmcnt(0)\n"
    "v_mfma_f32_32x32x16_bf16 v[48:63], v[128:131], a[240:243], v[48:63]\n"
    "ds_read_b64_tr_b16 v[80:81], v15 offset:32768\n"
    "ds_read_b64_tr_b16 v[82:83], v15 offset:33280\n"
    "ds_read_b64_tr_b16 v[84:85], v15 offset:34816\n"
    "v_mfma_f32_32x32x16_bf16 v[64:79], v[132:135], a[240:243], v[64:79]\n"
    "ds_read_b64_tr_b16 v[86:87], v15 offset:35328\n"
    "ds_read_b64_tr_b16 v[88:89], v15 offset:36864\n"
    "ds_read_b64_tr_b16 v[90:91], v15 offset:37376\n"
    "s_add_u32 m0, s89, 49152\n"
    "s_add_u32 s95, s90, 819200\n"
    "v_mfma_f32_32x32x16_bf16 v[48:63], v[136:139], a[244:247], v[48:63]\n"
    "ds_read_b64_tr_b16 v[92:93], v15 offset:38912\n"
    "ds_read_b64_tr_b16 v[94:95], v15 offset:39424\n"
    "buffer_load_dwordx4 v5, s[80:83], s95 offen lds\n"
    "buffer_load_dwordx4 v6, s[80:83], s95 offen offset:1024 lds\n"
    "v_mfma_f32_32x32x16_bf16 v[64:79], v[140:143], a[244:247], v[64:79]\n"
    "ds_read_b64_tr_b16 v[96:97], v15 offset:40960\n"
    "ds_read_b64_tr_b16 v[98:99], v15 offset:41472\n"
    "ds_read_b64_tr_b16 v[100:101], v15 offset:43008\n"
    "s_add_u32 m0, s89, 57344\n"
    "s_add_u32 s95, s90, 901120\n"
    "v_mfma_f32_32x32x16_bf16 v[48:63], v[144:147], a[248:251], v[48:63]\n"
    "ds_read_b64_tr_b16 v[102:103], v15 offset:43520\n"
    "ds_read_b64_tr_b16 v[104:105], v15 offset:45056\n"
    "ds_read_b64_tr_b16 v[106:107], v15 offset:45568\n"
    "buffer_load_dwordx4 v5, s[80:83], s95 offen lds\n"
    "v_mfma_f32_32x32x16_bf16 v[64:79], v[148:151], a[248:251], v[64:79]\n"
    "ds_read_b64_tr_b16 v[108:109], v15 offset:47104\n"
    "ds_read_b64_tr_b16 v[110:111], v15 offset:47616\n"
    "buffer_load_dwordx4 v6, s[80:83], s95 offen offset:1024 lds\n"
    "s_add_u32 m0, s89, 65536\n"
    "v_mfma_f32_32x32x16_bf16 v[48:63], v[152:155], a[252:255], v[48:63]\n"
    "s_add_u32 s95, s90, 983040\n"
    "buffer_load_dwordx4 v5, s[80:83], s95 offen lds\n"
    "v_mfma_f32_32x32x16_bf16 v[64:79], v[156:159], a[252:255], v[64:79]\n"
    "buffer_load_dwordx4 v6, s[80:83], s95 offen offset:1024 lds\n"
    "s_xor_b32 s37, s86, 1\n"
    "s_lshl_b32 s79, 1, s37\n"
    "s_and_b32 s37, s86, 2\n"
    "s_xor_b32 s37, s37, 2\n"
    "s_lshl_b32 s78, 3, s37\n"
    "s_waitcnt lgkmcnt(0)\n"
    "v_mfma_f32_32x32x16_bf16 v[32:47], v[80:83], a[0:3], 0\n"
    "ds_read_b64_tr_b16 v[128:129], v15 offset:49152\n"
    "ds_read_b64_tr_b16 v[130:131], v15 offset:49664\n"
    "ds_read_b64_tr_b16 v[132:133], v15 offset:51200\n"
    "v_mfma_f32_32x32x16_bf16 v[32:47], v[84:87], a[4:7], v[32:47]\n"
    "ds_read_b64_tr_b16 v[134:135], v15 offset:51712\n"
    "ds_read_b64_tr_b16 v[136:137], v15 offset:53248\n"
    "ds_read_b64_tr_b16 v[138:139], v15 offset:53760\n"
    "v_mfma_f32_32x32x16_bf16 v[32:47], v[88:91], a[8:11], v[32:47]\n"
    "ds_read_b64_tr_b16 v[140:141], v15 offset:55296\n"
    "ds_read_b64_tr_b16 v[142:143], v15 offset:55808\n"
    "s_add_u32 m0, s89, 73728\n"
    "s_add_u32 s95, s90, 1064960\n"
    "v_mfma_f32_32x32x16_bf16 v[32:47], v[92:95], a[12:15], v[32:47]\n"
    "ds_read_b64_tr_b16 v[144:145], v15 offset:57344\n"
    "ds_read_b64_tr_b16 v[146:147], v15 offset:57856\n"
    "ds_read_b64_tr_b16 v[148:149], v15 offset:59392\n"
    "buffer_load_dwordx4 v5, s[80:83], s95 offen lds\n"
    "buffer_load_dwordx4 v6, s[80:83], s95 offen offset:1024 lds\n"
    "v_mfma_f32_32x32x16_bf16 v[32:47], v[96:99], a[16:19], v[32:47]\n"
    "ds_read_b64_tr_b16 v[150:151], v15 offset:59904\n"
    "ds_read_b64_tr_b16 v[152:153], v15 offset:61440\n"
    "ds_read_b64_tr_b16 v[154:155], v15 offset:61952\n"
    "s_add_u32 m0, s89, 81920\n"
    "s_add_u32 s95, s90, 1146880\n"
    "v_mfma_f32_32x32x16_bf16 v[32:47], v[100:103], a[20:23], v[32:47]\n"
    "ds_read_b64_tr_b16 v[156:157], v15 offset:63488\n"
    "ds_read_b64_tr_b16 v[158:159], v15 offset:64000\n"
    "buffer_load_dwordx4 v5, s[80:83], s95 offen lds\n"
    "buffer_load_dwordx4 v6, s[80:83], s95 offen offset:1024 lds\n"
    "v_mfma_f32_32x32x16_bf16 v[32:47], v[104:107], a[24:27], v[32:47]\n"
    "s_add_u32 m0, s89, 90112\n"
    "s_add_u32 s95, s90, 1228800\n"
    "v_mfma_f32_32x32x16_bf16 v[32:47], v[108:111], a[28:31], v[32:47]\n"
    "buffer_load_dwordx4 v5, s[80:83], s95 offen lds\n"
    "buffer_load_dwordx4 v6, s[80:83], s95 offen offset:1024 lds\n"
    "s_waitcnt lgkmcnt(0)\n"
    "v_mfma_f32_32x32x16_bf16 v[32:47], v[128:131], a[32:35], v[32:47]\n"
    "s_waitcnt vmcnt(20)\n"
    "s_barrier\n"
    "ds_read_b64_tr_b16 v[80:81], v13 offset:0\n"
    "ds_read_b64_tr_b16 v[82:83], v13 offset:512\n"
    "ds_read_b64_tr_b16 v[84:85], v13 offset:2048\n"
    "v_mfma_f32_32x32x16_bf16 v[32:47], v[132:135], a[36:39], v[32:47]\n"
    "ds_read_b64_tr_b16 v[86:87], v13 offset:2560\n"
    "ds_read_b64_tr_b16 v[88:89], v13 offset:4096\n"
    "ds_read_b64_tr_b16 v[90:91], v13 offset:4608\n"
    "v_mfma_f32_32x32x16_bf16 v[32:47], v[136:139], a[40:43], v[32:47]\n"
    "ds_read_b64_tr_b16 v[92:93], v13 offset:6144\n"
    "ds_read_b64_tr_b16 v[94:95], v13 offset:6656\n"
    "s_xor_b32 s37, s86, 2\n"
    "s_lshl_b32 s38, s91, 2\n"
    "s_add_u32 s37, s37, s38\n"
    "s_lshl_b32 s37, s37, 2\n"
    "s_add_u32 s37, s37, s36\n"
    "s_lshl_b32 s37, s37, 14\n"
    "v_mfma_f32_32x32x16_bf16 v[32:47], v[140:143], a[44:47], v[32:47]\n"
    "ds_read_b64_tr_b16 v[96:97], v13 offset:8192\n"
    "ds_read_b64_tr_b16 v[98:99], v13 offset:8704\n"
    "ds_read_b64_tr_b16 v[100:101], v13 offset:10240\n"
    "s_add_u32 s37, s37, 8192\n"
    "v_cvt_pk_bf16_f32 v48, v48, v49\n"
    "v_cvt_pk_bf16_f32 v49, v50, v51\n"
    "v_cvt_pk_bf16_f32 v50, v52, v53\n"
    "v_cvt_pk_bf16_f32 v51, v54, v55\n"
    "v_cvt_pk_bf16_f32 v52, v56, v57\n"
    "v_mfma_f32_32x32x16_bf16 v[32:47], v[144:147], a[48:51], v[32:47]\n"
    "ds_read_b64_tr_b16 v[102:103], v13 offset:10752\n"
    "ds_read_b64_tr_b16 v[104:105], v13 offset:12288\n"
    "ds_read_b64_tr_b16 v[106:107], v13 offset:12800\n"
    "v_cvt_pk_bf16_f32 v53, v58, v59\n"
    "v_cvt_pk_bf16_f32 v54, v60, v61\n"
    "v_cvt_pk_bf16_f32 v55, v62, v63\n"
    "buffer_store_dwordx4 v[48:51], v7, s[60:63], s37 offen sc1\n"
    "buffer_store_dwordx4 v[52:55], v7, s[60:63], s37 offen offset:1024 sc1\n"
    "v_mfma_f32_32x32x16_bf16 v[32:47], v[148:151], a[52:55], v[32:47]\n"
    "ds_read_b64_tr_b16 v[108:109], v13 offset:14336\n"
    "ds_read_b64_tr_b16 v[110:111], v13 offset:14848\n"
    "s_xor_b32 s37, s86, 3\n"
    "s_lshl_b32 s38, s91, 2\n"
    "s_add_u32 s37, s37, s38\n"
    "s_lshl_b32 s37, s37, 2\n"
    "s_add_u32 s37, s37, s36\n"
    "s_lshl_b32 s37, s37, 14\n"
    "v_mfma_f32_32x32x16_bf16 v[32:47], v[152:155], a[56:59], v[32:47]\n"
    "s_add_u32 s37, s37, 12288\n"
    "v_cvt_pk_bf16_f32 v64, v64, v65\n"
    "v_cvt_pk_bf16_f32 v65, v66, v67\n"
    "v_cvt_pk_bf16_f32 v66, v68, v69\n"
    "v_cvt_pk_bf16_f32 v67, v70, v71\n"
    "v_cvt_pk_bf16_f32 v68, v72, v73\n"
    "v_mfma_f32_32x32x16_bf16 v[32:47], v[156:159], a[60:63], v[32:47]\n"
    "v_cvt_pk_bf16_f32 v69, v74, v75\n"
    "v_cvt_pk_bf16_f32 v70, v76, v77\n"
    "v_cvt_pk_bf16_f32 v71, v78, v79\n"
    "buffer_store_dwordx4 v[64:67], v7, s[60:63], s37 offen sc1\n"
    "buffer_store_dwordx4 v[68:71], v7, s[60:63], s37 offen offset:1024 sc1\n"
    "s_waitcnt lgkmcnt(0)\n"
    "v_mfma_f32_32x32x16_bf16 v[32:47], v[80:83], a[64:67], v[32:47]\n"
    "s_waitcnt vmcnt(20)\n"
    "s_barrier\n"
    "ds_read_b64_tr_b16 v[128:129], v13 offset:16384\n"
    "ds_read_b64_tr_b16 v[130:131], v13 offset:16896\n"
    "ds_read_b64_tr_b16 v[132:133], v13 offset:18432\n"
    "v_mfma_f32_32x32x16_bf16 v[32:47], v[84:87], a[68:71], v[32:47]\n"
    "ds_read_b64_tr_b16 v[134:135], v13 offset:18944\n"
    "ds_read_b64_tr_b16 v[136:137], v13 offset:20480\n"
    "ds_read_b64_tr_b16 v[138:139], v13 offset:20992\n"
    "v_mfma_f32_32x32x16_bf16 v[32:47], v[88:91], a[72:75], v[32:47]\n"
    "ds_read_b64_tr_b16 v[140:141], v13 offset:22528\n"
    "ds_read_b64_tr_b16 v[142:143], v13 offset:23040\n"
    "v_mfma_f32_32x32x16_bf16 v[32:47], v[92:95], a[76:79], v[32:47]\n"
    "ds_read_b64_tr_b16 v[144:145], v13 offset:24576\n"
    "ds_read_b64_tr_b16 v[146:147], v13 offset:25088\n"
    "ds_read_b64_tr_b16 v[148:149], v13 offset:26624\n"
    "v_mfma_f32_32x32x16_bf16 v[32:47], v[96:99], a[80:83], v[32:47]\n"
    "ds_read_b64_tr_b16 v[150:151], v13 offset:27136\n"
    "ds_read_b64_tr_b16 v[152:153], v13 offset:28672\n"
    "ds_read_b64_tr_b16 v[154:155], v13 offset:29184\n"
    "v_mfma_f32_32x32x16_bf16 v[32:47], v[100:103], a[84:87], v[32:47]\n"
    "ds_read_b64_tr_b16 v[156:157], v13 offset:30720\n"
    "ds_read_b64_tr_b16 v[158:159], v13 offset:31232\n"
    "v_mfma_f32_32x32x16_bf16 v[32:47], v[104:107], a[88:91], v[32:47]\n"
    "v_mfma_f32_32x32x16_bf16 v[32:47], v[108:111], a[92:95], v[32:47]\n"
    "s_waitcnt lgkmcnt(0)\n"
    "v_mfma_f32_32x32x16_bf16 v[32:47], v[128:131], a[96:99], v[32:47]\n"
    "s_waitcnt vmcnt(16)\n"
    "s_barrier\n"
    "ds_read_b64_tr_b16 v[80:81], v13 offset:32768\n"
    "ds_read_b64_tr_b16 v[82:83], v13 offset:33280\n"
    "ds_read_b64_tr_b16 v[84:85], v13 offset:34816\n"
    "v_mfma_f32_32x32x16_bf16 v[32:47], v[132:135], a[100:103], v[32:47]\n"
    "ds_read_b64_tr_b16 v[86:87], v13 offset:35328\n"
    "ds_read_b64_tr_b16 v[88:89], v13 offset:36864\n"
    "ds_read_b64_tr_b16 v[90:91], v13 offset:37376\n"
    "v_mfma_f32_32x32x16_bf16 v[32:47], v[136:139], a[104:107], v[32:47]\n"
    "ds_read_b64_tr_b16 v[92:93], v13 offset:38912\n"
    "ds_read_b64_tr_b16 v[94:95], v13 offset:39424\n"
    "v_mfma_f32_32x32x16_bf16 v[32:47], v[140:143], a[108:111], v[32:47]\n"
    "ds_read_b64_tr_b16 v[96:97], v13 offset:40960\n"
    "ds_read_b64_tr_b16 v[98:99], v13 offset:41472\n"
    "ds_read_b64_tr_b16 v[100:101], v13 offset:43008\n"
    "v_mfma_f32_32x32x16_bf16 v[32:47], v[144:147], a[112:115], v[32:47]\n"
    "ds_read_b64_tr_b16 v[102:103], v13 offset:43520\n"
    "ds_read_b64_tr_b16 v[104:105], v13 offset:45056\n"
    "ds_read_b64_tr_b16 v[106:107], v13 offset:45568\n"
    "v_mfma_f32_32x32x16_bf16 v[32:47], v[148:151], a[116:119], v[32:47]\n"
    "ds_read_b64_tr_b16 v[108:109], v13 offset:47104\n"
    "ds_read_b64_tr_b16 v[110:111], v13 offset:47616\n"
    "v_mfma_f32_32x32x16_bf16 v[32:47], v[152:155], a[120:123], v[32:47]\n"
    "v_mfma_f32_32x32x16_bf16 v[32:47], v[156:159], a[124:127], v[32:47]\n"
    "s_waitcnt lgkmcnt(0)\n"
    "v_mfma_f32_32x32x16_bf16 v[32:47], v[80:83], a[128:131], v[32:47]\n"
    "s_waitcnt vmcnt(12)\n"
    "s_barrier\n"
    "ds_read_b64_tr_b16 v[128:129], v14 offset:0\n"
    "ds_read_b64_tr_b16 v[130:131], v14 offset:512\n"
    "ds_read_b64_tr_b16 v[132:133], v14 offset:2048\n"
    "v_mfma_f32_32x32x16_bf16 v[32:47], v[84:87], a[132:135], v[32:47]\n"
    "ds_read_b64_tr_b16 v[134:135], v14 offset:2560\n"
    "ds_read_b64_tr_b16 v[136:137], v14 offset:4096\n"
    "ds_read_b64_tr_b16 v[138:139], v14 offset:4608\n"
    "v_mfma_f32_32x32x16_bf16 v[32:47], v[88:91], a[136:139], v[32:47]\n"
    "ds_read_b64_tr_b16 v[140:141], v14 offset:6144\n"
    "ds_read_b64_tr_b16 v[142:143], v14 offset:6656\n"
    "v_mfma_f32_32x32x16_bf16 v[32:47], v[92:95], a[140:143], v[32:47]\n"
    "ds_read_b64_tr_b16 v[144:145], v14 offset:8192\n"
    "ds_read_b64_tr_b16 v[146:147], v14 offset:8704\n"
    "ds_read_b64_tr_b16 v[148:149], v14 offset:10240\n"
    "v_mfma_f32_32x32x16_bf16 v[32:47], v[96:99], a[144:147], v[32:47]\n"
    "ds_read_b64_tr_b16 v[150:151], v14 offset:10752\n"
    "ds_read_b64_tr_b16 v[152:153], v14 offset:12288\n"
    "ds_read_b64_tr_b16 v[154:155], v14 offset:12800\n"
    "v_mfma_f32_32x32x16_bf16 v[32:47], v[100:103], a[148:151], v[32:47]\n"
    "ds_read_b64_tr_b16 v[156:157], v14 offset:14336\n"
    "ds_read_b64_tr_b16 v[158:159], v14 offset:14848\n"
    "v_mfma_f32_32x32x16_bf16 v[32:47], v[104:107], a[152:155], v[32:47]\n"
    "v_mfma_f32_32x32x16_bf16 v[32:47], v[108:111], a[156:159], v[32:47]\n"
    "s_waitcnt lgkmcnt(0)\n"
    "v_mfma_f32_32x32x16_bf16 v[32:47], v[128:131], a[160:163], v[32:47]\n"
    "s_waitcnt vmcnt(8)\n"
    "s_barrier\n"
    "ds_read_b64_tr_b16 v[80:81], v14 offset:16384\n"
    "ds_read_b64_tr_b16 v[82:83], v14 offset:16896\n"
    "ds_read_b64_tr_b16 v[84:85], v14 offset:18432\n"
    "v_mfma_f32_32x32x16_bf16 v[32:47], v[132:135], a[164:167], v[32:47]\n"
    "ds_read_b64_tr_b16 v[86:87], v14 offset:18944\n"
    "ds_read_b64_tr_b16 v[88:89], v14 offset:20480\n"
    "ds_read_b64_tr_b16 v[90:91], v14 offset:20992\n"
    "v_mfma_f32_32x32x16_bf16 v[32:47], v[136:139], a[168:171], v[32:47]\n"
    "ds_read_b64_tr_b16 v[92:93], v14 offset:22528\n"
    "ds_read_b64_tr_b16 v[94:95], v14 offset:23040\n"
    "v_mfma_f32_32x32x16_bf16 v[32:47], v[140:143], a[172:175], v[32:47]\n"
    "ds_read_b64_tr_b16 v[96:97], v14 offset:24576\n"
    "ds_read_b64_tr_b16 v[98:99], v14 offset:25088\n"
    "ds_read_b64_tr_b16 v[100:101], v14 offset:26624\n"
    "v_mfma_f32_32x32x16_bf16 v[32:47], v[144:147], a[176:179], v[32:47]\n"
    "ds_read_b64_tr_b16 v[102:103], v14 offset:27136\n"
    "ds_read_b64_tr_b16 v[104:105], v14 offset:28672\n"
    "ds_read_b64_tr_b16 v[106:107], v14 offset:29184\n"
    "v_mfma_f32_32x32x16_bf16 v[32:47], v[148:151], a[180:183], v[32:47]\n"
    "ds_read_b64_tr_b16 v[108:109], v14 offset:30720\n"
    "ds_read_b64_tr_b16 v[110:111], v14 offset:31232\n"
    "v_mfma_f32_32x32x16_bf16 v[32:47], v[152:155], a[184:187], v[32:47]\n"
    "v_mfma_f32_32x32x16_bf16 v[32:47], v[156:159], a[188:191], v[32:47]\n"
    "s_waitcnt lgkmcnt(0)\n"
    "v_mfma_f32_32x32x16_bf16 v[32:47], v[80:83], a[192:195], v[32:47]\n"
    "s_waitcnt vmcnt(4)\n"
    "s_barrier\n"
    "ds_read_b64_tr_b16 v[128:129], v14 offset:32768\n"
    "ds_read_b64_tr_b16 v[130:131], v14 offset:33280\n"
    "ds_read_b64_tr_b16 v[132:133], v14 offset:34816\n"
    "v_mfma_f32_32x32x16_bf16 v[32:47], v[84:87], a[196:199], v[32:47]\n"
    "ds_read_b64_tr_b16 v[134:135], v14 offset:35328\n"
    "ds_read_b64_tr_b16 v[136:137], v14 offset:36864\n"
    "ds_read_b64_tr_b16 v[138:139], v14 offset:37376\n"
    "v_mfma_f32_32x32x16_bf16 v[32:47], v[88:91], a[200:203], v[32:47]\n"
    "ds_read_b64_tr_b16 v[140:141], v14 offset:38912\n"
    "ds_read_b64_tr_b16 v[142:143], v14 offset:39424\n"
    "v_mfma_f32_32x32x16_bf16 v[32:47], v[92:95], a[204:207], v[32:47]\n"
    "ds_read_b64_tr_b16 v[144:145], v14 offset:40960\n"
    "ds_read_b64_tr_b16 v[146:147], v14 offset:41472\n"
    "ds_read_b64_tr_b16 v[148:149], v14 offset:43008\n"
    "v_mfma_f32_32x32x16_bf16 v[32:47], v[96:99], a[208:211], v[32:47]\n"
    "ds_read_b64_tr_b16 v[150:151], v14 offset:43520\n"
    "ds_read_b64_tr_b16 v[152:153], v14 offset:45056\n"
    "ds_read_b64_tr_b16 v[154:155], v14 offset:45568\n"
    "v_mfma_f32_32x32x16_bf16 v[32:47], v[100:103], a[212:215], v[32:47]\n"
    "ds_read_b64_tr_b16 v[156:157], v14 offset:47104\n"
    "ds_read_b64_tr_b16 v[158:159], v14 offset:47616\n"
    "v_mfma_f32_32x32x16_bf16 v[32:47], v[104:107], a[216:219], v[32:47]\n"
    "v_mfma_f32_32x32x16_bf16 v[32:47], v[108:111], a[220:223], v[32:47]\n"
    "s_waitcnt lgkmcnt(0)\n"
    "v_mfma_f32_32x32x16_bf16 v[32:47], v[128:131], a[224:227], v[32:47]\n"
    "ds_read_b64_tr_b16 v[80:81], v12 offset:32768\n"
    "ds_read_b64_tr_b16 v[82:83], v12 offset:33280\n"
    "ds_read_b64_tr_b16 v[84:85], v12 offset:34816\n"
    "v_mfma_f32_32x32x16_bf16 v[32:47], v[132:135], a[228:231], v[32:47]\n"
    "ds_read_b64_tr_b16 v[86:87], v12 offset:35328\n"
    "ds_read_b64_tr_b16 v[88:89], v12 offset:36864\n"
    "ds_read_b64_tr_b16 v[90:91], v12 offset:37376\n"
    "v_mfma_f32_32x32x16_bf16 v[32:47], v[136:139], a[232:235], v[32:47]\n"
    "ds_read_b64_tr_b16 v[92:93], v12 offset:38912\n"
    "ds_read_b64_tr_b16 v[94:95], v12 offset:39424\n"
    "v_mfma_f32_32x32x16_bf16 v[32:47], v[140:143], a[236:239], v[32:47]\n"
    "ds_read_b64_tr_b16 v[96:97], v12 offset:40960\n"
    "ds_read_b64_tr_b16 v[98:99], v12 offset:41472\n"
    "ds_read_b64_tr_b16 v[100:101], v12 offset:43008\n"
    "v_mfma_f32_32x32x16_bf16 v[32:47], v[144:147], a[240:243], v[32:47]\n"
    "ds_read_b64_tr_b16 v[102:103], v12 offset:43520\n"
    "ds_read_b64_tr_b16 v[104:105], v12 offset:45056\n"
    "ds_read_b64_tr_b16 v[106:107], v12 offset:45568\n"
    "v_mfma_f32_32x32x16_bf16 v[32:47], v[148:151], a[244:247], v[32:47]\n"
    "ds_read_b64_tr_b16 v[108:109], v12 offset:47104\n"
    "ds_read_b64_tr_b16 v[110:111], v12 offset:47616\n"
    "v_mfma_f32_32x32x16_bf16 v[32:47], v[152:155], a[248:251], v[32:47]\n"
    "s_waitcnt vmcnt(0)\n"
    "s_barrier\n"
    "s_or_b32 s70, s70, 0\n"
    "s_or_b32 s71, s71, s78\n"
    "s_cmp_lg_u32 s36, 0\n"
    "s_cbranch_scc1 .L3_npa\n"
    "s_mov_b32 exec_lo, 0\n"
    "s_mov_b32 exec_hi, s78\n"
    "buffer_store_dwordx2 v[246:247], v250, s[64:67], s74 offen sc1\n"
    "s_mov_b64 exec, -1\n"
    ".L3_npa:\n"
    "s_nop 3\n"
    "v_mfma_f32_32x32x16_bf16 v[32:47], v[156:159], a[252:255], v[32:47]\n"
    "s_waitcnt lgkmcnt(0)\n"
    "v_mfma_f32_32x32x16_bf16 v[16:31], v[80:83], a[0:3], 0\n"
    "ds_read_b64_tr_b16 v[128:129], v12 offset:49152\n"
    "ds_read_b64_tr_b16 v[130:131], v12 offset:49664\n"
    "ds_read_b64_tr_b16 v[132:133], v12 offset:51200\n"
    "v_mfma_f32_32x32x16_bf16 v[16:31], v[84:87], a[4:7], v[16:31]\n"
    "ds_read_b64_tr_b16 v[134:135], v12 offset:51712\n"
    "ds_read_b64_tr_b16 v[136:137], v12 offset:53248\n"
    "ds_read_b64_tr_b16 v[138:139], v12 offset:53760\n"
    "v_mfma_f32_32x32x16_bf16 v[16:31], v[88:91], a[8:11], v[16:31]\n"
    "ds_read_b64_tr_b16 v[140:141], v12 offset:55296\n"
    "ds_read_b64_tr_b16 v[142:143], v12 offset:55808\n"
    "s_xor_b32 s37, s86, 1\n"
    "s_lshl_b32 s38, s91, 2\n"
    "s_add_u32 s37, s37, s38\n"
    "v_mfma_f32_32x32x16_bf16 v[16:31], v[92:95], a[12:15], v[16:31]\n"
    "ds_read_b64_tr_b16 v[144:145], v12 offset:57344\n"
    "ds_read_b64_tr_b16 v[146:147], v12 offset:57856\n"
    "ds_read_b64_tr_b16 v[148:149], v12 offset:59392\n"
    "s_lshl_b32 s37, s37, 2\n"
    "s_add_u32 s37, s37, s36\n"
    "s_lshl_b32 s37, s37, 14\n"
    "v_mfma_f32_32x32x16_bf16 v[16:31], v[96:99], a[16:19], v[16:31]\n"
    "ds_read_b64_tr_b16 v[150:151], v12 offset:59904\n"
    "ds_read_b64_tr_b16 v[152:153], v12 offset:61440\n"
    "ds_read_b64_tr_b16 v[154:155], v12 offset:61952\n"
    "s_add_u32 s37, s37, 4096\n"
    "v_cvt_pk_bf16_f32 v32, v32, v33\n"
    "v_cvt_pk_bf16_f32 v33, v34, v35\n"
    "v_mfma_f32_32x32x16_bf16 v[16:31], v[100:103], a[20:23], v[16:31]\n"
    "ds_read_b64_tr_b16 v[156:157], v12 offset:63488\n"
    "ds_read_b64_tr_b16 v[158:159], v12 offset:64000\n"
    "v_cvt_pk_bf16_f32 v34, v36, v37\n"
    "v_cvt_pk_bf16_f32 v35, v38, v39\n"
    "v_cvt_pk_bf16_f32 v36, v40, v41\n"
    "v_mfma_f32_32x32x16_bf16 v[16:31], v[104:107], a[24:27], v[16:31]\n"
    "v_cvt_pk_bf16_f32 v37, v42, v43\n"
    "v_cvt_pk_bf16_f32 v38, v44, v45\n"
    "v_cvt_pk_bf16_f32 v39, v46, v47\n"
    "v_mfma_f32_32x32x16_bf16 v[16:31], v[108:111], a[28:31], v[16:31]\n"
    "buffer_store_dwordx4 v[32:35], v7, s[60:63], s37 offen sc1\n"
    "buffer_store_dwordx4 v[36:39], v7, s[60:63], s37 offen offset:1024 sc1\n"
    "s_waitcnt lgkmcnt(0)\n"
    "v_mfma_f32_32x32x16_bf16 v[16:31], v[128:131], a[32:35], v[16:31]\n"
    "ds_read_b64_tr_b16 v[80:81], v10 offset:0\n"
    "ds_read_b64_tr_b16 v[82:83], v10 offset:512\n"
    "ds_read_b64_tr_b16 v[84:85], v10 offset:2048\n"
    "v_mfma_f32_32x32x16_bf16 v[16:31], v[132:135], a[36:39], v[16:31]\n"
    "ds_read_b64_tr_b16 v[86:87], v10 offset:2560\n"
    "ds_read_b64_tr_b16 v[88:89], v10 offset:4096\n"
    "ds_read_b64_tr_b16 v[90:91], v10 offset:4608\n"
    "v_mfma_f32_32x32x16_bf16 v[16:31], v[136:139], a[40:43], v[16:31]\n"
    "ds_read_b64_tr_b16 v[92:93], v10 offset:6144\n"
    "ds_read_b64_tr_b16 v[94:95], v10 offset:6656\n"
    "v_mfma_f32_32x32x16_bf16 v[16:31], v[140:143], a[44:47], v[16:31]\n"
    "ds_read_b64_tr_b16 v[96:97], v10 offset:8192\n"
    "ds_read_b64_tr_b16 v[98:99], v10 offset:8704\n"
    "ds_read_b64_tr_b16 v[100:101], v10 offset:10240\n"
    "v_mfma_f32_32x32x16_bf16 v[16:31], v[144:147], a[48:51], v[16:31]\n"
    "ds_read_b64_tr_b16 v[102:103], v10 offset:10752\n"
    "ds_read_b64_tr_b16 v[104:105], v10 offset:12288\n"
    "ds_read_b64_tr_b16 v[106:107], v10 offset:12800\n"
    "v_mfma_f32_32x32x16_bf16 v[16:31], v[148:151], a[52:55], v[16:31]\n"
    "ds_read_b64_tr_b16 v[108:109], v10 offset:14336\n"
    "ds_read_b64_tr_b16 v[110:111], v10 offset:14848\n"
    "v_mfma_f32_32x32x16_bf16 v[16:31], v[152:155], a[56:59], v[16:31]\n"
    "v_mfma_f32_32x32x16_bf16 v[16:31], v[156:159], a[60:63], v[16:31]\n"
    "s_waitcnt lgkmcnt(0)\n"
    "v_mfma_f32_32x32x16_bf16 v[16:31], v[80:83], a[64:67], v[16:31]\n"
    "ds_read_b64_tr_b16 v[128:129], v10 offset:16384\n"
    "ds_read_b64_tr_b16 v[130:131], v10 offset:16896\n"
    "ds_read_b64_tr_b16 v[132:133], v10 offset:18432\n"
    "v_mfma_f32_32x32x16_bf16 v[16:31], v[84:87], a[68:71], v[16:31]\n"
    "ds_read_b64_tr_b16 v[134:135], v10 offset:18944\n"
    "ds_read_b64_tr_b16 v[136:137], v10 offset:20480\n"
    "ds_read_b64_tr_b16 v[138:139], v10 offset:20992\n"
    "v_mfma_f32_32x32x16_bf16 v[16:31], v[88:91], a[72:75], v[16:31]\n"
    "ds_read_b64_tr_b16 v[140:141], v10 offset:22528\n"
    "ds_read_b64_tr_b16 v[142:143], v10 offset:23040\n"
    "v_mfma_f32_32x32x16_bf16 v[16:31], v[92:95], a[76:79], v[16:31]\n"
    "ds_read_b64_tr_b16 v[144:145], v10 offset:24576\n"
    "ds_read_b64_tr_b16 v[146:147], v10 offset:25088\n"
    "ds_read_b64_tr_b16 v[148:149], v10 offset:26624\n"
    "v_mfma_f32_32x32x16_bf16 v[16:31], v[96:99], a[80:83], v[16:31]\n"
    "ds_read_b64_tr_b16 v[150:151], v10 offset:27136\n"
    "ds_read_b64_tr_b16 v[152:153], v10 offset:28672\n"
    "ds_read_b64_tr_b16 v[154:155], v10 offset:29184\n"
    "v_mfma_f32_32x32x16_bf16 v[16:31], v[100:103], a[84:87], v[16:31]\n"
    "ds_read_b64_tr_b16 v[156:157], v10 offset:30720\n"
    "ds_read_b64_tr_b16 v[158:159], v10 offset:31232\n"
    "v_mfma_f32_32x32x16_bf16 v[16:31], v[104:107], a[88:91], v[16:31]\n"
    "v_mfma_f32_32x32x16_bf16 v[16:31], v[108:111], a[92:95], v[16:31]\n"
    "s_mov_b32 s72, 0\n"
    "s_mov_b32 s73, s78\n"
    "s_mov_b64 exec, s[72:73]\n"
    "buffer_load_dwordx2 v[248:249], v245, s[64:67], 0 offen sc1\n"
    "s_mov_b64 exec, -1\n"
    "s_nop 3\n"
    "s_waitcnt lgkmcnt(0)\n"
    "v_mfma_f32_32x32x16_bf16 v[16:31], v[128:131], a[96:99], v[16:31]\n"
    "ds_read_b64_tr_b16 v[80:81], v10 offset:32768\n"
    "ds_read_b64_tr_b16 v[82:83], v10 offset:33280\n"
    "ds_read_b64_tr_b16 v[84:85], v10 offset:34816\n"
    "v_mfma_f32_32x32x16_bf16 v[16:31], v[132:135], a[100:103], v[16:31]\n"
    "ds_read_b64_tr_b16 v[86:87], v10 offset:35328\n"
    "ds_read_b64_tr_b16 v[88:89], v10 offset:36864\n"
    "ds_read_b64_tr_b16 v[90:91], v10 offset:37376\n"
    "v_mfma_f32_32x32x16_bf16 v[16:31], v[136:139], a[104:107], v[16:31]\n"
    "ds_read_b64_tr_b16 v[92:93], v10 offset:38912\n"
    "ds_read_b64_tr_b16 v[94:95], v10 offset:39424\n"
    "v_mfma_f32_32x32x16_bf16 v[16:31], v[140:143], a[108:111], v[16:31]\n"
    "ds_read_b64_tr_b16 v[96:97], v10 offset:40960\n"
    "ds_read_b64_tr_b16 v[98:99], v10 offset:41472\n"
    "ds_read_b64_tr_b16 v[100:101], v10 offset:43008\n"
    "v_mfma_f32_32x32x16_bf16 v[16:31], v[144:147], a[112:115], v[16:31]\n"
    "ds_read_b64_tr_b16 v[102:103], v10 offset:43520\n"
    "ds_read_b64_tr_b16 v[104:105], v10 offset:45056\n"
    "ds_read_b64_tr_b16 v[106:107], v10 offset:45568\n"
    "v_mfma_f32_32x32x16_bf16 v[16:31], v[148:151], a[116:119], v[16:31]\n"
    "ds_read_b64_tr_b16 v[108:109], v10 offset:47104\n"
    "ds_read_b64_tr_b16 v[110:111], v10 offset:47616\n"
    "v_mfma_f32_32x32x16_bf16 v[16:31], v[152:155], a[120:123], v[16:31]\n"
    "v_mfma_f32_32x32x16_bf16 v[16:31], v[156:159], a[124:127], v[16:31]\n"
    "s_waitcnt lgkmcnt(0)\n"
    "v_mfma_f32_32x32x16_bf16 v[16:31], v[80:83], a[128:131], v[16:31]\n"
    "ds_read_b64_tr_b16 v[128:129], v11 offset:0\n"
    "ds_read_b64_tr_b16 v[130:131], v11 offset:512\n"
    "ds_read_b64_tr_b16 v[132:133], v11 offset:2048\n"
    "v_mfma_f32_32x32x16_bf16 v[16:31], v[84:87], a[132:135], v[16:31]\n"
    "ds_read_b64_tr_b16 v[134:135], v11 offset:2560\n"
    "ds_read_b64_tr_b16 v[136:137], v11 offset:4096\n"
    "ds_read_b64_tr_b16 v[138:139], v11 offset:4608\n"
    "v_mfma_f32_32x32x16_bf16 v[16:31], v[88:91], a[136:139], v[16:31]\n"
    "ds_read_b64_tr_b16 v[140:141], v11 offset:6144\n"
    "ds_read_b64_tr_b16 v[142:143], v11 offset:6656\n"
    "v_mfma_f32_32x32x16_bf16 v[16:31], v[92:95], a[140:143], v[16:31]\n"
    "ds_read_b64_tr_b16 v[144:145], v11 offset:8192\n"
    "ds_read_b64_tr_b16 v[146:147], v11 offset:8704\n"
    "ds_read_b64_tr_b16 v[148:149], v11 offset:10240\n"
    "v_mfma_f32_32x32x16_bf16 v[16:31], v[96:99], a[144:147], v[16:31]\n"
    "ds_read_b64_tr_b16 v[150:151], v11 offset:10752\n"
    "ds_read_b64_tr_b16 v[152:153], v11 offset:12288\n"
    "ds_read_b64_tr_b16 v[154:155], v11 offset:12800\n"
    "v_mfma_f32_32x32x16_bf16 v[16:31], v[100:103], a[148:151], v[16:31]\n"
    "ds_read_b64_tr_b16 v[156:157], v11 offset:14336\n"
    "ds_read_b64_tr_b16 v[158:159], v11 offset:14848\n"
    "v_mfma_f32_32x32x16_bf16 v[16:31], v[104:107], a[152:155], v[16:31]\n"
    "s_waitcnt vmcnt(1)\n"
    "s_barrier\n"
    "s_or_b32 s70, s70, 0\n"
    "s_or_b32 s71, s71, s79\n"
    "s_cmp_lg_u32 s36, 0\n"
    "s_cbranch_scc1 .L3_npb\n"
    "s_mov_b32 exec_lo, 0\n"
    "s_mov_b32 exec_hi, s79\n"
    "buffer_store_dwordx2 v[246:247], v250, s[64:67], s74 offen sc1\n"
    "s_mov_b64 exec, -1\n"
    ".L3_npb:\n"
    "s_nop 3\n"
    "v_mfma_f32_32x32x16_bf16 v[16:31], v[108:111], a[156:159], v[16:31]\n"
    "s_waitcnt lgkmcnt(0)\n"
    "v_mfma_f32_32x32x16_bf16 v[16:31], v[128:131], a[160:163], v[16:31]\n"
    "ds_read_b64_tr_b16 v[80:81], v11 offset:16384\n"
    "ds_read_b64_tr_b16 v[82:83], v11 offset:16896\n"
    "ds_read_b64_tr_b16 v[84:85], v11 offset:18432\n"
    "v_mfma_f32_32x32x16_bf16 v[16:31], v[132:135], a[164:167], v[16:31]\n"
    "ds_read_b64_tr_b16 v[86:87], v11 offset:18944\n"
    "ds_read_b64_tr_b16 v[88:89], v11 offset:20480\n"
    "ds_read_b64_tr_b16 v[90:91], v11 offset:20992\n"
    "v_mfma_f32_32x32x16_bf16 v[16:31], v[136:139], a[168:171], v[16:31]\n"
    "ds_read_b64_tr_b16 v[92:93], v11 offset:22528\n"
    "ds_read_b64_tr_b16 v[94:95], v11 offset:23040\n"
    "v_mfma_f32_32x32x16_bf16 v[16:31], v[140:143], a[172:175], v[16:31]\n"
    "ds_read_b64_tr_b16 v[96:97], v11 offset:24576\n"
    "ds_read_b64_tr_b16 v[98:99], v11 offset:25088\n"
    "ds_read_b64_tr_b16 v[100:101], v11 offset:26624\n"
    "v_mfma_f32_32x32x16_bf16 v[16:31], v[144:147], a[176:179], v[16:31]\n"
    "ds_read_b64_tr_b16 v[102:103], v11 offset:27136\n"
    "ds_read_b64_tr_b16 v[104:105], v11 offset:28672\n"
    "ds_read_b64_tr_b16 v[106:107], v11 offset:29184\n"
    "v_mfma_f32_32x32x16_bf16 v[16:31], v[148:151], a[180:183], v[16:31]\n"
    "ds_read_b64_tr_b16 v[108:109], v11 offset:30720\n"
    "ds_read_b64_tr_b16 v[110:111], v11 offset:31232\n"
    "v_mfma_f32_32x32x16_bf16 v[16:31], v[152:155], a[184:187], v[16:31]\n"
    "s_lshl_b32 s37, s91, 2\n"
    "s_add_u32 s37, s37, s86\n"
    "s_lshl_b32 s37, s37, 2\n"
    "s_add_u32 s37, s37, s36\n"
    "s_lshl_b32 s37, s37, 14\n"
    "s_waitcnt vmcnt(0)\n"
    "s_mov_b64 exec, s[72:73]\n"
    "v_cmp_eq_u64 vcc, s[54:55], v[248:249]\n"
    "s_mov_b64 exec, -1\n"
    "s_cmp_eq_u64 vcc, s[72:73]\n"
    "s_cbranch_scc1 .L3_goA\n"
    "s_mov_b32 s50, 0\n"
    ".L3_pollA:\n"
    "s_mov_b64 exec, s[72:73]\n"
    "buffer_load_dwordx2 v[248:249], v245, s[64:67], 0 offen sc1\n"
    "s_add_u32 s50, s50, 1\n"
    "s_waitcnt vmcnt(0)\n"
    "v_cmp_eq_u64 vcc, s[54:55], v[248:249]\n"
    "s_mov_b64 exec, -1\n"
    "s_cmp_eq_u64 vcc, s[72:73]\n"
    "s_cbranch_scc1 .L3_goA\n"
    "s_cmp_ge_u32 s50, 0x80000\n"
    "s_cbranch_scc1 .L3_goA\n"
    "s_sleep 1\n"
    "s_and_b32 s76, s50, 63\n"
    "s_cbranch_scc1 .L3_pollA\n"
    "s_call_b64 s[52:53], .L3_refresh\n"
    "s_branch .L3_pollA\n"
    ".L3_goA:\n"
    "s_nop 3\n"
    "s_add_u32 s88, s37, 8192\n"
    "buffer_load_dwordx4 v[192:195], v7, s[60:63], s88 offen sc1\n"
    "buffer_load_dwordx4 v[196:199], v7, s[60:63], s88 offen offset:1024 sc1\n"
    "s_add_u32 s88, s37, 12288\n"
    "buffer_load_dwordx4 v[208:211], v7, s[60:63], s88 offen sc1\n"
    "buffer_load_dwordx4 v[212:215], v7, s[60:63], s88 offen offset:1024 sc1\n"
    "v_mfma_f32_32x32x16_bf16 v[16:31], v[156:159], a[188:191], v[16:31]\n"
    "s_waitcnt lgkmcnt(0)\n"
    "v_mfma_f32_32x32x16_bf16 v[16:31], v[80:83], a[192:195], v[16:31]\n"
    "ds_read_b64_tr_b16 v[128:129], v11 offset:32768\n"
    "ds_read_b64_tr_b16 v[130:131], v11 offset:33280\n"
    "ds_read_b64_tr_b16 v[132:133], v11 offset:34816\n"
    "v_mfma_f32_32x32x16_bf16 v[16:31], v[84:87], a[196:199], v[16:31]\n"
    "ds_read_b64_tr_b16 v[134:135], v11 offset:35328\n"
    "ds_read_b64_tr_b16 v[136:137], v11 offset:36864\n"
    "ds_read_b64_tr_b16 v[138:139], v11 offset:37376\n"
    "v_mfma_f32_32x32x16_bf16 v[16:31], v[88:91], a[200:203], v[16:31]\n"
    "ds_read_b64_tr_b16 v[140:141], v11 offset:38912\n"
    "ds_read_b64_tr_b16 v[142:143], v11 offset:39424\n"
    "v_mfma_f32_32x32x16_bf16 v[16:31], v[92:95], a[204:207], v[16:31]\n"
    "ds_read_b64_tr_b16 v[144:145], v11 offset:40960\n"
    "ds_read_b64_tr_b16 v[146:147], v11 offset:41472\n"
    "ds_read_b64_tr_b16 v[148:149], v11 offset:43008\n"
    "v_mfma_f32_32x32x16_bf16 v[16:31], v[96:99], a[208:211], v[16:31]\n"
    "ds_read_b64_tr_b16 v[150:151], v11 offset:43520\n"
    "ds_read_b64_tr_b16 v[152:153], v11 offset:45056\n"
    "ds_read_b64_tr_b16 v[154:155], v11 offset:45568\n"
    "v_mfma_f32_32x32x16_bf16 v[16:31], v[100:103], a[212:215], v[16:31]\n"
    "ds_read_b64_tr_b16 v[156:157], v11 offset:47104\n"
    "ds_read_b64_tr_b16 v[158:159], v11 offset:47616\n"
    "v_mfma_f32_32x32x16_bf16 v[16:31], v[104:107], a[216:219], v[16:31]\n"
    "v_mfma_f32_32x32x16_bf16 v[16:31], v[108:111], a[220:223], v[16:31]\n"
    "s_waitcnt lgkmcnt(0)\n"
    "v_mfma_f32_32x32x16_bf16 v[16:31], v[128:131], a[224:227], v[16:31]\n"
    "v_mfma_f32_32x32x16_bf16 v[16:31], v[132:135], a[228:231], v[16:31]\n"
    "v_mfma_f32_32x32x16_bf16 v[16:31], v[136:139], a[232:235], v[16:31]\n"
    "v_mfma_f32_32x32x16_bf16 v[16:31], v[140:143], a[236:239], v[16:31]\n"
    "v_mfma_f32_32x32x16_bf16 v[16:31], v[144:147], a[240:243], v[16:31]\n"
    "v_mfma_f32_32x32x16_bf16 v[16:31], v[148:151], a[244:247], v[16:31]\n"
    "v_mfma_f32_32x32x16_bf16 v[16:31], v[152:155], a[248:251], v[16:31]\n"
    "v_mfma_f32_32x32x16_bf16 v[16:31], v[156:159], a[252:255], v[16:31]\n"
    "s_cmp_eq_u32 s36, 0\n"
    "s_cselect_b32 s72, -1, 0\n"
    "s_mov_b32 s73, s79\n"
    "s_mov_b64 exec, s[72:73]\n"
    "buffer_load_dwordx2 v[248:249], v245, s[64:67], 0 offen sc1\n"
    "s_mov_b64 exec, -1\n"
    "s_nop 3\n"
    "s_waitcnt vmcnt(0)\n"
    "s_mov_b64 exec, s[72:73]\n"
    "v_cmp_eq_u64 vcc, s[54:55], v[248:249]\n"
    "s_mov_b64 exec, -1\n"
    "s_cmp_eq_u64 vcc, s[72:73]\n"
    "s_cbranch_scc1 .L3_goB\n"
    "s_mov_b32 s50, 0\n"
    ".L3_pollB:\n"
    "s_mov_b64 exec, s[72:73]\n"
    "buffer_load_dwordx2 v[248:249], v245, s[64:67], 0 offen sc1\n"
    "s_add_u32 s50, s50, 1\n"
    "s_waitcnt vmcnt(0)\n"
    "v_cmp_eq_u64 vcc, s[54:55], v[248:249]\n"
    "s_mov_b64 exec, -1\n"
    "s_cmp_eq_u64 vcc, s[72:73]\n"
    "s_cbranch_scc1 .L3_goB\n"
    "s_cmp_ge_u32 s50, 0x80000\n"
    "s_cbranch_scc1 .L3_goB\n"
    "s_sleep 1\n"
    "s_and_b32 s76, s50, 63\n"
    "s_cbranch_scc1 .L3_pollB\n"
    "s_call_b64 s[52:53], .L3_refresh\n"
    "s_branch .L3_pollB\n"
    ".L3_goB:\n"
    "s_add_u32 s88, s37, 4096\n"
    "buffer_load_dwordx4 v[176:179], v7, s[60:63], s88 offen sc1\n"
    "buffer_load_dwordx4 v[180:183], v7, s[60:63], s88 offen offset:1024 sc1\n"
    "buffer_load_dwordx2 v[252:253], v244, s[64:67], 0 offen sc1\n"
    "s_mov_b32 s28, %[out_lo]\n"
    "s_and_b32 s29, %[out_hi], 0xffff\n"
    "s_mov_b32 s30, 1966080\n"
    "s_mov_b32 s31, 0x20000\n"
    "s_lshl_b32 s37, s84, 2\n"
    "s_add_u32 s37, s37, s36\n"
    "s_mul_i32 s37, s37, 40960\n"
    "s_lshl_b32 s38, s85, 2\n"
    "s_add_u32 s38, s38, s86\n"
    "s_lshl_b32 s38, s38, 6\n"
    "s_add_u32 s37, s37, s38\n"
    "v_and_b32 v242, 31, v4\n"
    "v_mul_u32_u24 v242, 1280, v242\n"
    "v_lshrrev_b32 v241, 5, v4\n"
    "v_lshl_add_u32 v242, v241, 4, v242\n"
    "v_lshlrev_b32 v240, 16, v192\n"
    "v_and_b32 v243, 0xffff0000, v192\n"
    "v_add_f32 v16, v16, v240\n"
    "v_add_f32 v17, v17, v243\n"
    "v_lshlrev_b32 v240, 16, v193\n"
    "v_and_b32 v243, 0xffff0000, v193\n"
    "v_add_f32 v18, v18, v240\n"
    "v_add_f32 v19, v19, v243\n"
    "v_lshlrev_b32 v240, 16, v194\n"
    "v_and_b32 v243, 0xffff0000, v194\n"
    "v_add_f32 v20, v20, v240\n"
    "v_add_f32 v21, v21, v243\n"
    "v_lshlrev_b32 v240, 16, v195\n"
    "v_and_b32 v243, 0xffff0000, v195\n"
    "v_add_f32 v22, v22, v240\n"
    "v_add_f32 v23, v23, v243\n"
    "v_lshlrev_b32 v240, 16, v196\n"
    "v_and_b32 v243, 0xffff0000, v196\n"
    "v_add_f32 v24, v24, v240\n"
    "v_add_f32 v25, v25, v243\n"
    "v_lshlrev_b32 v240, 16, v197\n"
    "v_and_b32 v243, 0xffff0000, v197\n"
    "v_add_f32 v26, v26, v240\n"
    "v_add_f32 v27, v27, v243\n"
    "v_lshlrev_b32 v240, 16, v198\n"
    "v_and_b32 v243, 0xffff0000, v198\n"
    "v_add_f32 v28, v28, v240\n"
    "v_add_f32 v29, v29, v243\n"
    "v_lshlrev_b32 v240, 16, v199\n"
    "v_and_b32 v243, 0xffff0000, v199\n"
    "v_add_f32 v30, v30, v240\n"
    "v_add_f32 v31, v31, v243\n"
    "v_lshlrev_b32 v240, 16, v208\n"
    "v_and_b32 v243, 0xffff0000, v208\n"
    "v_add_f32 v16, v16, v240\n"
    "v_add_f32 v17, v17, v243\n"
    "v_lshlrev_b32 v240, 16, v209\n"
    "v_and_b32 v243, 0xffff0000, v209\n"
    "v_add_f32 v18, v18, v240\n"
    "v_add_f32 v19, v19, v243\n"
    "v_lshlrev_b32 v240, 16, v210\n"
    "v_and_b32 v243, 0xffff0000, v210\n"
    "v_add_f32 v20, v20, v240\n"
    "v_add_f32 v21, v21, v243\n"
    "v_lshlrev_b32 v240, 16, v211\n"
    "v_and_b32 v243, 0xffff0000, v211\n"
    "v_add_f32 v22, v22, v240\n"
    "v_add_f32 v23, v23, v243\n"
    "v_lshlrev_b32 v240, 16, v212\n"
    "v_and_b32 v243, 0xffff0000, v212\n"
    "v_add_f32 v24, v24, v240\n"
    "v_add_f32 v25, v25, v243\n"
    "v_lshlrev_b32 v240, 16, v213\n"
    "v_and_b32 v243, 0xffff0000, v213\n"
    "v_add_f32 v26, v26, v240\n"
    "v_add_f32 v27, v27, v243\n"
    "v_lshlrev_b32 v240, 16, v214\n"
    "v_and_b32 v243, 0xffff0000, v214\n"
    "v_add_f32 v28, v28, v240\n"
    "v_add_f32 v29, v29, v243\n"
    "v_lshlrev_b32 v240, 16, v215\n"
    "v_and_b32 v243, 0xffff0000, v215\n"
    "v_add_f32 v30, v30, v240\n"
    "v_add_f32 v31, v31, v243\n"
    "s_waitcnt vmcnt(1)\n"
    "v_lshlrev_b32 v240, 16, v176\n"
    "v_and_b32 v243, 0xffff0000, v176\n"
    "v_add_f32 v16, v16, v240\n"
    "v_add_f32 v17, v17, v243\n"
    "v_lshlrev_b32 v240, 16, v177\n"
    "v_and_b32 v243, 0xffff0000, v177\n"
    "v_add_f32 v18, v18, v240\n"
    "v_add_f32 v19, v19, v243\n"
    "v_lshlrev_b32 v240, 16, v178\n"
    "v_and_b32 v243, 0xffff0000, v178\n"
    "v_add_f32 v20, v20, v240\n"
    "v_add_f32 v21, v21, v243\n"
    "v_lshlrev_b32 v240, 16, v179\n"
    "v_and_b32 v243, 0xffff0000, v179\n"
    "v_add_f32 v22, v22, v240\n"
    "v_add_f32 v23, v23, v243\n"
    "v_lshlrev_b32 v240, 16, v180\n"
    "v_and_b32 v243, 0xffff0000, v180\n"
    "v_add_f32 v24, v24, v240\n"
    "v_add_f32 v25, v25, v243\n"
    "v_lshlrev_b32 v240, 16, v181\n"
    "v_and_b32 v243, 0xffff0000, v181\n"
    "v_add_f32 v26, v26, v240\n"
    "v_add_f32 v27, v27, v243\n"
    "v_lshlrev_b32 v240, 16, v182\n"
    "v_and_b32 v243, 0xffff0000, v182\n"
    "v_add_f32 v28, v28, v240\n"
    "v_add_f32 v29, v29, v243\n"
    "v_lshlrev_b32 v240, 16, v183\n"
    "v_and_b32 v243, 0xffff0000, v183\n"
    "v_add_f32 v30, v30, v240\n"
    "v_add_f32 v31, v31, v243\n"
    "v_cvt_pk_bf16_f32 v32, v16, v17\n"
    "v_cvt_pk_bf16_f32 v33, v18, v19\n"
    "v_cvt_pk_bf16_f32 v34, v20, v21\n"
    "v_cvt_pk_bf16_f32 v35, v22, v23\n"
    "v_cvt_pk_bf16_f32 v36, v24, v25\n"
    "v_cvt_pk_bf16_f32 v37, v26, v27\n"
    "v_cvt_pk_bf16_f32 v38, v28, v29\n"
    "v_cvt_pk_bf16_f32 v39, v30, v31\n"
    "buffer_store_dwordx4 v[32:35], v242, s[28:31], s37 offen\n"
    "buffer_store_dwordx4 v[36:39], v242, s[28:31], s37 offen offset:32\n"
    "s_waitcnt vmcnt(2)\n"
    "v_cmp_ne_u64 vcc, v[246:247], v[252:253]\n"
    "s_cmp_lg_u32 s36, 0\n"
    "s_cbranch_scc1 .L3_fin\n"
    "s_cbranch_vccz .L3_fin\n"
    "s_call_b64 s[52:53], .L3_refresh\n"
    ".L3_fin:\n"
    ".L3_end:\n"
    "s_branch .L_end\n"
    ".L_codeend:\n"
    ".L_stub0:\n"
    "s_call_b64 s[50:51], .L_slowFa0\n"
    "s_branch .L_ret0\n"
    ".L_stub1:\n"
    "s_call_b64 s[50:51], .L_slowFb1\n"
    "s_branch .L_ret1\n"
    ".L_stub2:\n"
    "s_call_b64 s[50:51], .L_slowFa2\n"
    "s_branch .L_ret2\n"
    ".L_stub3:\n"
    "s_call_b64 s[50:51], .L_slowNb0\n"
    "s_branch .L_ret3\n"
    ".L_stub4:\n"
    "s_call_b64 s[50:51], .L_slowNa1\n"
    "s_branch .L_ret4\n"
    ".L_stub5:\n"
    "s_call_b64 s[50:51], .L_slowNb2\n"
    "s_branch .L_ret5\n"
    ".L_stub6:\n"
    "s_call_b64 s[50:51], .L_slowNa0\n"
    "s_branch .L_ret6\n"
    ".L_stub7:\n"
    "s_call_b64 s[50:51], .L_slowNb1\n"
    "s_branch .L_ret7\n"
    ".L_stub8:\n"
    "s_call_b64 s[50:51], .L_slowNa2\n"
    "s_branch .L_ret8\n"
    ".L_stub9:\n"
    "s_call_b64 s[50:51], .L_slowNb0\n"
    "s_branch .L_ret9\n"
    ".L_stub10:\n"
    "s_call_b64 s[50:51], .L_slowNa1\n"
    "s_branch .L_ret10\n"
    ".L_stub11:\n"
    "s_call_b64 s[50:51], .L_slowNb2\n"
    "s_branch .L_ret11\n"
    ".L_stub12:\n"
    "s_call_b64 s[50:51], .L_slowNa0\n"
    "s_branch .L_ret12\n"
    ".L_stub13:\n"
    "s_call_b64 s[50:51], .L_slowNb1\n"
    "s_branch .L_ret13\n"
    ".L_stub14:\n"
    "s_call_b64 s[50:51], .L_slowNa2\n"
    "s_branch .L_ret14\n"
    ".L_stub15:\n"
    "s_call_b64 s[50:51], .L_slowNb0\n"
    "s_branch .L_ret15\n"
    ".L_stub16:\n"
    "s_call_b64 s[50:51], .L_slowNa1\n"
    "s_branch .L_ret16\n"
    ".L_stub17:\n"
    "s_call_b64 s[50:51], .L_slowNb2\n"
    "s_branch .L_ret17\n"
    ".L_stub18:\n"
    "s_call_b64 s[50:51], .L_slowNa0\n"
    "s_branch .L_ret18\n"
    ".L_stub19:\n"
    "s_call_b64 s[50:51], .L_slowNb1\n"
    "s_branch .L_ret19\n"
    ".L_stub20:\n"
    "s_call_b64 s[50:51], .L_slowNa2\n"
    "s_branch .L_ret20\n"
    ".L_stub21:\n"
    "s_call_b64 s[50:51], .L_slowNb0\n"
    "s_branch .L_ret21\n"
    ".L_stub22:\n"
    "s_call_b64 s[50:51], .L_slowNa1\n"
    "s_branch .L_ret22\n"
    ".L_stub23:\n"
    "s_call_b64 s[50:51], .L_slowNb2\n"
    "s_branch .L_ret23\n"
    ".L_slowFa0:\n"
    "s_nop 15\n"
    "s_nop 15\n"
    "v_max3_f32 v252, v176, v177, v178\n"
    "v_max3_f32 v252, v252, v179, v180\n"
    "v_max3_f32 v252, v252, v181, v182\n"
    "v_max3_f32 v252, v252, v183, v184\n"
    "v_max3_f32 v252, v252, v185, v186\n"
    "v_max3_f32 v252, v252, v187, v188\n"
    "v_max3_f32 v252, v252, v189, v190\n"
    "v_max_f32 v252, v252, v191\n"
    "v_mov_b32 v253, v252\n"
    "s_nop 1\n"
    "v_permlane32_swap_b32 v252, v253\n"
    "v_max_f32 v252, v252, v253\n"
    "v_sub_f32 v176, v176, v252\n"
    "v_sub_f32 v177, v177, v252\n"
    "v_sub_f32 v178, v178, v252\n"
    "v_sub_f32 v179, v179, v252\n"
    "v_sub_f32 v180, v180, v252\n"
    "v_sub_f32 v181, v181, v252\n"
    "v_sub_f32 v182, v182, v252\n"
    "v_sub_f32 v183, v183, v252\n"
    "v_sub_f32 v184, v184, v252\n"
    "v_sub_f32 v185, v185, v252\n"
    "v_sub_f32 v186, v186, v252\n"
    "v_sub_f32 v187, v187, v252\n"
    "v_sub_f32 v188, v188, v252\n"
    "v_sub_f32 v189, v189, v252\n"
    "v_sub_f32 v190, v190, v252\n"
    "v_sub_f32 v191, v191, v252\n"
    "v_sub_f32 v128, v128, v252\n"
    "v_mov_b32 v129, v128\n"
    "v_mov_b32 v130, v128\n"
    "v_mov_b32 v131, v128\n"
    "v_mov_b32 v132, v128\n"
    "v_mov_b32 v133, v128\n"
    "v_mov_b32 v134, v128\n"
    "v_mov_b32 v135, v128\n"
    "v_mov_b32 v136, v128\n"
    "v_mov_b32 v137, v128\n"
    "v_mov_b32 v138, v128\n"
    "v_mov_b32 v139, v128\n"
    "v_mov_b32 v140, v128\n"
    "v_mov_b32 v141, v128\n"
    "v_mov_b32 v142, v128\n"
    "v_mov_b32 v143, v128\n"
    "v_exp_f32 v254, v176\n"
    "v_exp_f32 v255, v177\n"
    "s_nop 0\n"
    "v_add_f32 v24, v254, v255\n"
    "v_cvt_pk_bf16_f32 v224, v254, v255\n"
    "v_exp_f32 v254, v178\n"
    "v_exp_f32 v255, v179\n"
    "s_nop 0\n"
    "v_add_f32 v24, v24, v254\n"
    "v_add_f32 v24, v24, v255\n"
    "v_cvt_pk_bf16_f32 v225, v254, v255\n"
    "v_exp_f32 v254, v180\n"
    "v_exp_f32 v255, v181\n"
    "s_nop 0\n"
    "v_add_f32 v24, v24, v254\n"
    "v_add_f32 v24, v24, v255\n"
    "v_cvt_pk_bf16_f32 v226, v254, v255\n"
    "v_exp_f32 v254, v182\n"
    "v_exp_f32 v255, v183\n"
    "s_nop 0\n"
    "v_add_f32 v24, v24, v254\n"
    "v_add_f32 v24, v24, v255\n"
    "v_cvt_pk_bf16_f32 v227, v254, v255\n"
    "v_exp_f32 v254, v184\n"
    "v_exp_f32 v255, v185\n"
    "s_nop 0\n"
    "v_add_f32 v24, v24, v254\n"
    "v_add_f32 v24, v24, v255\n"
    "v_cvt_pk_bf16_f32 v228, v254, v255\n"
    "v_exp_f32 v254, v186\n"
    "v_exp_f32 v255, v187\n"
    "s_nop 0\n"
    "v_add_f32 v24, v24, v254\n"
    "v_add_f32 v24, v24, v255\n"
    "v_cvt_pk_bf16_f32 v229, v254, v255\n"
    "v_exp_f32 v254, v188\n"
    "v_exp_f32 v255, v189\n"
    "s_nop 0\n"
    "v_add_f32 v24, v24, v254\n"
    "v_add_f32 v24, v24, v255\n"
    "v_cvt_pk_bf16_f32 v230, v254, v255\n"
    "v_exp_f32 v254, v190\n"
    "v_exp_f32 v255, v191\n"
    "s_nop 0\n"
    "v_add_f32 v24, v24, v254\n"
    "v_add_f32 v24, v24, v255\n"
    "v_cvt_pk_bf16_f32 v231, v254, v255\n"
    "s_nop 1\n"
    "s_setpc_b64 s[50:51]\n"
    ".L_slowFa2:\n"
    "s_nop 15\n"
    "s_nop 15\n"
    "v_max3_f32 v252, v208, v209, v210\n"
    "v_max3_f32 v252, v252, v211, v212\n"
    "v_max3_f32 v252, v252, v213, v214\n"
    "v_max3_f32 v252, v252, v215, v216\n"
    "v_max3_f32 v252, v252, v217, v218\n"
    "v_max3_f32 v252, v252, v219, v220\n"
    "v_max3_f32 v252, v252, v221, v222\n"
    "v_max_f32 v252, v252, v223\n"
    "v_mov_b32 v253, v252\n"
    "s_nop 1\n"
    "v_permlane32_swap_b32 v252, v253\n"
    "v_max_f32 v252, v252, v253\n"
    "v_sub_f32 v208, v208, v252\n"
    "v_sub_f32 v209, v209, v252\n"
    "v_sub_f32 v210, v210, v252\n"
    "v_sub_f32 v211, v211, v252\n"
    "v_sub_f32 v212, v212, v252\n"
    "v_sub_f32 v213, v213, v252\n"
    "v_sub_f32 v214, v214, v252\n"
    "v_sub_f32 v215, v215, v252\n"
    "v_sub_f32 v216, v216, v252\n"
    "v_sub_f32 v217, v217, v252\n"
    "v_sub_f32 v218, v218, v252\n"
    "v_sub_f32 v219, v219, v252\n"
    "v_sub_f32 v220, v220, v252\n"
    "v_sub_f32 v221, v221, v252\n"
    "v_sub_f32 v222, v222, v252\n"
    "v_sub_f32 v223, v223, v252\n"
    "v_sub_f32 v160, v160, v252\n"
    "v_mov_b32 v161, v160\n"
    "v_mov_b32 v162, v160\n"
    "v_mov_b32 v163, v160\n"
    "v_mov_b32 v164, v160\n"
    "v_mov_b32 v165, v160\n"
    "v_mov_b32 v166, v160\n"
    "v_mov_b32 v167, v160\n"
    "v_mov_b32 v168, v160\n"
    "v_mov_b32 v169, v160\n"
    "v_mov_b32 v170, v160\n"
    "v_mov_b32 v171, v160\n"
    "v_mov_b32 v172, v160\n"
    "v_mov_b32 v173, v160\n"
    "v_mov_b32 v174, v160\n"
    "v_mov_b32 v175, v160\n"
    "v_exp_f32 v254, v208\n"
    "v_exp_f32 v255, v209\n"
    "s_nop 0\n"
    "v_add_f32 v24, v254, v255\n"
    "v_cvt_pk_bf16_f32 v240, v254, v255\n"
    "v_exp_f32 v254, v210\n"
    "v_exp_f32 v255, v211\n"
    "s_nop 0\n"
    "v_add_f32 v24, v24, v254\n"
    "v_add_f32 v24, v24, v255\n"
    "v_cvt_pk_bf16_f32 v241, v254, v255\n"
    "v_exp_f32 v254, v212\n"
    "v_exp_f32 v255, v213\n"
    "s_nop 0\n"
    "v_add_f32 v24, v24, v254\n"
    "v_add_f32 v24, v24, v255\n"
    "v_cvt_pk_bf16_f32 v242, v254, v255\n"
    "v_exp_f32 v254, v214\n"
    "v_exp_f32 v255, v215\n"
    "s_nop 0\n"
    "v_add_f32 v24, v24, v254\n"
    "v_add_f32 v24, v24, v255\n"
    "v_cvt_pk_bf16_f32 v243, v254, v255\n"
    "v_exp_f32 v254, v216\n"
    "v_exp_f32 v255, v217\n"
    "s_nop 0\n"
    "v_add_f32 v24, v24, v254\n"
    "v_add_f32 v24, v24, v255\n"
    "v_cvt_pk_bf16_f32 v244, v254, v255\n"
    "v_exp_f32 v254, v218\n"
    "v_exp_f32 v255, v219\n"
    "s_nop 0\n"
    "v_add_f32 v24, v24, v254\n"
    "v_add_f32 v24, v24, v255\n"
    "v_cvt_pk_bf16_f32 v245, v254, v255\n"
    "v_exp_f32 v254, v220\n"
    "v_exp_f32 v255, v221\n"
    "s_nop 0\n"
    "v_add_f32 v24, v24, v254\n"
    "v_add_f32 v24, v24, v255\n"
    "v_cvt_pk_bf16_f32 v246, v254, v255\n"
    "v_exp_f32 v254, v222\n"
    "v_exp_f32 v255, v223\n"
    "s_nop 0\n"
    "v_add_f32 v24, v24, v254\n"
    "v_add_f32 v24, v24, v255\n"
    "v_cvt_pk_bf16_f32 v247, v254, v255\n"
    "s_nop 1\n"
    "s_setpc_b64 s[50:51]\n"
    ".L_slowFb1:\n"
    "s_nop 15\n"
    "s_nop 15\n"
    "v_max3_f32 v252, v192, v193, v194\n"
    "v_max3_f32 v252, v252, v195, v196\n"
    "v_max3_f32 v252, v252, v197, v198\n"
    "v_max3_f32 v252, v252, v199, v200\n"
    "v_max3_f32 v252, v252, v201, v202\n"
    "v_max3_f32 v252, v252, v203, v204\n"
    "v_max3_f32 v252, v252, v205, v206\n"
    "v_max_f32 v252, v252, v207\n"
    "v_mov_b32 v253, v252\n"
    "s_nop 1\n"
    "v_permlane32_swap_b32 v252, v253\n"
    "v_max_f32 v252, v252, v253\n"
    "v_sub_f32 v192, v192, v252\n"
    "v_sub_f32 v193, v193, v252\n"
    "v_sub_f32 v194, v194, v252\n"
    "v_sub_f32 v195, v195, v252\n"
    "v_sub_f32 v196, v196, v252\n"
    "v_sub_f32 v197, v197, v252\n"
    "v_sub_f32 v198, v198, v252\n"
    "v_sub_f32 v199, v199, v252\n"
    "v_sub_f32 v200, v200, v252\n"
    "v_sub_f32 v201, v201, v252\n"
    "v_sub_f32 v202, v202, v252\n"
    "v_sub_f32 v203, v203, v252\n"
    "v_sub_f32 v204, v204, v252\n"
    "v_sub_f32 v205, v205, v252\n"
    "v_sub_f32 v206, v206, v252\n"
    "v_sub_f32 v207, v207, v252\n"
    "v_sub_f32 v144, v144, v252\n"
    "v_mov_b32 v145, v144\n"
    "v_mov_b32 v146, v144\n"
    "v_mov_b32 v147, v144\n"
    "v_mov_b32 v148, v144\n"
    "v_mov_b32 v149, v144\n"
    "v_mov_b32 v150, v144\n"
    "v_mov_b32 v151, v144\n"
    "v_mov_b32 v152, v144\n"
    "v_mov_b32 v153, v144\n"
    "v_mov_b32 v154, v144\n"
    "v_mov_b32 v155, v144\n"
    "v_mov_b32 v156, v144\n"
    "v_mov_b32 v157, v144\n"
    "v_mov_b32 v158, v144\n"
    "v_mov_b32 v159, v144\n"
    "v_exp_f32 v254, v192\n"
    "v_exp_f32 v255, v193\n"
    "s_nop 0\n"
    "v_add_f32 v25, v254, v255\n"
    "v_cvt_pk_bf16_f32 v232, v254, v255\n"
    "v_exp_f32 v254, v194\n"
    "v_exp_f32 v255, v195\n"
    "s_nop 0\n"
    "v_add_f32 v25, v25, v254\n"
    "v_add_f32 v25, v25, v255\n"
    "v_cvt_pk_bf16_f32 v233, v254, v255\n"
    "v_exp_f32 v254, v196\n"
    "v_exp_f32 v255, v197\n"
    "s_nop 0\n"
    "v_add_f32 v25, v25, v254\n"
    "v_add_f32 v25, v25, v255\n"
    "v_cvt_pk_bf16_f32 v234, v254, v255\n"
    "v_exp_f32 v254, v198\n"
    "v_exp_f32 v255, v199\n"
    "s_nop 0\n"
    "v_add_f32 v25, v25, v254\n"
    "v_add_f32 v25, v25, v255\n"
    "v_cvt_pk_bf16_f32 v235, v254, v255\n"
    "v_exp_f32 v254, v200\n"
    "v_exp_f32 v255, v201\n"
    "s_nop 0\n"
    "v_add_f32 v25, v25, v254\n"
    "v_add_f32 v25, v25, v255\n"
    "v_cvt_pk_bf16_f32 v236, v254, v255\n"
    "v_exp_f32 v254, v202\n"
    "v_exp_f32 v255, v203\n"
    "s_nop 0\n"
    "v_add_f32 v25, v25, v254\n"
    "v_add_f32 v25, v25, v255\n"
    "v_cvt_pk_bf16_f32 v237, v254, v255\n"
    "v_exp_f32 v254, v204\n"
    "v_exp_f32 v255, v205\n"
    "s_nop 0\n"
    "v_add_f32 v25, v25, v254\n"
    "v_add_f32 v25, v25, v255\n"
    "v_cvt_pk_bf16_f32 v238, v254, v255\n"
    "v_exp_f32 v254, v206\n"
    "v_exp_f32 v255, v207\n"
    "s_nop 0\n"
    "v_add_f32 v25, v25, v254\n"
    "v_add_f32 v25, v25, v255\n"
    "v_cvt_pk_bf16_f32 v239, v254, v255\n"
    "s_nop 1\n"
    "s_setpc_b64 s[50:51]\n"
    ".L_slowNa0:\n"
    "s_nop 15\n"
    "s_nop 15\n"
    "v_max3_f32 v252, v176, v177, v178\n"
    "v_max3_f32 v252, v252, v179, v180\n"
    "v_max3_f32 v252, v252, v181, v182\n"
    "v_max3_f32 v252, v252, v183, v184\n"
    "v_max3_f32 v252, v252, v185, v186\n"
    "v_max3_f32 v252, v252, v187, v188\n"
    "v_max3_f32 v252, v252, v189, v190\n"
    "v_max_f32 v252, v252, v191\n"
    "v_mov_b32 v253, v252\n"
    "s_nop 1\n"
    "v_permlane32_swap_b32 v252, v253\n"
    "v_max_f32 v252, v252, v253\n"
    "v_max_f32 v252, 0, v252\n"
    "v_sub_f32 v176, v176, v252\n"
    "v_sub_f32 v177, v177, v252\n"
    "v_sub_f32 v178, v178, v252\n"
    "v_sub_f32 v179, v179, v252\n"
    "v_sub_f32 v180, v180, v252\n"
    "v_sub_f32 v181, v181, v252\n"
    "v_sub_f32 v182, v182, v252\n"
    "v_sub_f32 v183, v183, v252\n"
    "v_sub_f32 v184, v184, v252\n"
    "v_sub_f32 v185, v185, v252\n"
    "v_sub_f32 v186, v186, v252\n"
    "v_sub_f32 v187, v187, v252\n"
    "v_sub_f32 v188, v188, v252\n"
    "v_sub_f32 v189, v189, v252\n"
    "v_sub_f32 v190, v190, v252\n"
    "v_sub_f32 v191, v191, v252\n"
    "v_sub_f32 v128, v128, v252\n"
    "v_mov_b32 v129, v128\n"
    "v_mov_b32 v130, v128\n"
    "v_mov_b32 v131, v128\n"
    "v_mov_b32 v132, v128\n"
    "v_mov_b32 v133, v128\n"
    "v_mov_b32 v134, v128\n"
    "v_mov_b32 v135, v128\n"
    "v_mov_b32 v136, v128\n"
    "v_mov_b32 v137, v128\n"
    "v_mov_b32 v138, v128\n"
    "v_mov_b32 v139, v128\n"
    "v_mov_b32 v140, v128\n"
    "v_mov_b32 v141, v128\n"
    "v_mov_b32 v142, v128\n"
    "v_mov_b32 v143, v128\n"
    "v_sub_f32 v253, 0, v252\n"
    "v_exp_f32 v253, v253\n"
    "s_nop 0\n"
    "v_mul_f32 v21, v21, v253\n"
    "v_mul_f32 v32, v32, v253\n"
    "v_mul_f32 v33, v33, v253\n"
    "v_mul_f32 v34, v34, v253\n"
    "v_mul_f32 v35, v35, v253\n"
    "v_mul_f32 v36, v36, v253\n"
    "v_mul_f32 v37, v37, v253\n"
    "v_mul_f32 v38, v38, v253\n"
    "v_mul_f32 v39, v39, v253\n"
    "v_mul_f32 v40, v40, v253\n"
    "v_mul_f32 v41, v41, v253\n"
    "v_mul_f32 v42, v42, v253\n"
    "v_mul_f32 v43, v43, v253\n"
    "v_mul_f32 v44, v44, v253\n"
    "v_mul_f32 v45, v45, v253\n"
    "v_mul_f32 v46, v46, v253\n"
    "v_mul_f32 v47, v47, v253\n"
    "v_mul_f32 v80, v80, v253\n"
    "v_mul_f32 v81, v81, v253\n"
    "v_mul_f32 v82, v82, v253\n"
    "v_mul_f32 v83, v83, v253\n"
    "v_mul_f32 v84, v84, v253\n"
    "v_mul_f32 v85, v85, v253\n"
    "v_mul_f32 v86, v86, v253\n"
    "v_mul_f32 v87, v87, v253\n"
    "v_mul_f32 v88, v88, v253\n"
    "v_mul_f32 v89, v89, v253\n"
    "v_mul_f32 v90, v90, v253\n"
    "v_mul_f32 v91, v91, v253\n"
    "v_mul_f32 v92, v92, v253\n"
    "v_mul_f32 v93, v93, v253\n"
    "v_mul_f32 v94, v94, v253\n"
    "v_mul_f32 v95, v95, v253\n"
    "v_accvgpr_read_b32 v254, a0\n"
    "v_mul_f32 v254, v254, v253\n"
    "v_accvgpr_write_b32 a0, v254\n"
    "v_accvgpr_read_b32 v254, a1\n"
    "v_mul_f32 v254, v254, v253\n"
    "v_accvgpr_write_b32 a1, v254\n"
    "v_accvgpr_read_b32 v254, a2\n"
    "v_mul_f32 v254, v254, v253\n"
    "v_accvgpr_write_b32 a2, v254\n"
    "v_accvgpr_read_b32 v254, a3\n"
    "v_mul_f32 v254, v254, v253\n"
    "v_accvgpr_write_b32 a3, v254\n"
    "v_accvgpr_read_b32 v254, a4\n"
    "v_mul_f32 v254, v254, v253\n"
    "v_accvgpr_write_b32 a4, v254\n"
    "v_accvgpr_read_b32 v254, a5\n"
    "v_mul_f32 v254, v254, v253\n"
    "v_accvgpr_write_b32 a5, v254\n"
    "v_accvgpr_read_b32 v254, a6\n"
    "v_mul_f32 v254, v254, v253\n"
    "v_accvgpr_write_b32 a6, v254\n"
    "v_accvgpr_read_b32 v254, a7\n"
    "v_mul_f32 v254, v254, v253\n"
    "v_accvgpr_write_b32 a7, v254\n"
    "v_accvgpr_read_b32 v254, a8\n"
    "v_mul_f32 v254, v254, v253\n"
    "v_accvgpr_write_b32 a8, v254\n"
    "v_accvgpr_read_b32 v254, a9\n"
    "v_mul_f32 v254, v254, v253\n"
    "v_accvgpr_write_b32 a9, v254\n"
    "v_accvgpr_read_b32 v254, a10\n"
    "v_mul_f32 v254, v254, v253\n"
    "v_accvgpr_write_b32 a10, v254\n"
    "v_accvgpr_read_b32 v254, a11\n"
    "v_mul_f32 v254, v254, v253\n"
    "v_accvgpr_write_b32 a11, v254\n"
    "v_accvgpr_read_b32 v254, a12\n"
    "v_mul_f32 v254, v254, v253\n"
    "v_accvgpr_write_b32 a12, v254\n"
    "v_accvgpr_read_b32 v254, a13\n"
    "v_mul_f32 v254, v254, v253\n"
    "v_accvgpr_write_b32 a13, v254\n"
    "v_accvgpr_read_b32 v254, a14\n"
    "v_mul_f32 v254, v254, v253\n"
    "v_accvgpr_write_b32 a14, v254\n"
    "v_accvgpr_read_b32 v254, a15\n"
    "v_mul_f32 v254, v254, v253\n"
    "v_accvgpr_write_b32 a15, v254\n"
    "v_accvgpr_read_b32 v254, a48\n"
    "v_mul_f32 v254, v254, v253\n"
    "v_accvgpr_write_b32 a48, v254\n"
    "v_accvgpr_read_b32 v254, a49\n"
    "v_mul_f32 v254, v254, v253\n"
    "v_accvgpr_write_b32 a49, v254\n"
    "v_accvgpr_read_b32 v254, a50\n"
    "v_mul_f32 v254, v254, v253\n"
    "v_accvgpr_write_b32 a50, v254\n"
    "v_accvgpr_read_b32 v254, a51\n"
    "v_mul_f32 v254, v254, v253\n"
    "v_accvgpr_write_b32 a51, v254\n"
    "v_accvgpr_read_b32 v254, a52\n"
    "v_mul_f32 v254, v254, v253\n"
    "v_accvgpr_write_b32 a52, v254\n"
    "v_accvgpr_read_b32 v254, a53\n"
    "v_mul_f32 v254, v254, v253\n"
    "v_accvgpr_write_b32 a53, v254\n"
    "v_accvgpr_read_b32 v254, a54\n"
    "v_mul_f32 v254, v254, v253\n"
    "v_accvgpr_write_b32 a54, v254\n"
    "v_accvgpr_read_b32 v254, a55\n"
    "v_mul_f32 v254, v254, v253\n"
    "v_accvgpr_write_b32 a55, v254\n"
    "v_accvgpr_read_b32 v254, a56\n"
    "v_mul_f32 v254, v254, v253\n"
    "v_accvgpr_write_b32 a56, v254\n"
    "v_accvgpr_read_b32 v254, a57\n"
    "v_mul_f32 v254, v254, v253\n"
    "v_accvgpr_write_b32 a57, v254\n"
    "v_accvgpr_read_b32 v254, a58\n"
    "v_mul_f32 v254, v254, v253\n"
    "v_accvgpr_write_b32 a58, v254\n"
    "v_accvgpr_read_b32 v254, a59\n"
    "v_mul_f32 v254, v254, v253\n"
    "v_accvgpr_write_b32 a59, v254\n"
    "v_accvgpr_read_b32 v254, a60\n"
    "v_mul_f32 v254, v254, v253\n"
    "v_accvgpr_write_b32 a60, v254\n"
    "v_accvgpr_read_b32 v254, a61\n"
    "v_mul_f32 v254, v254, v253\n"
    "v_accvgpr_write_b32 a61, v254\n"
    "v_accvgpr_read_b32 v254, a62\n"
    "v_mul_f32 v254, v254, v253\n"
    "v_accvgpr_write_b32 a62, v254\n"
    "v_accvgpr_read_b32 v254, a63\n"
    "v_mul_f32 v254, v254, v253\n"
    "v_accvgpr_write_b32 a63, v254\n"
    "v_exp_f32 v254, v176\n"
    "v_exp_f32 v255, v177\n"
    "s_nop 0\n"
    "v_add_f32 v24, v254, v255\n"
    "v_cvt_pk_bf16_f32 v224, v254, v255\n"
    "v_exp_f32 v254, v178\n"
    "v_exp_f32 v255, v179\n"
    "s_nop 0\n"
    "v_add_f32 v24, v24, v254\n"
    "v_add_f32 v24, v24, v255\n"
    "v_cvt_pk_bf16_f32 v225, v254, v255\n"
    "v_exp_f32 v254, v180\n"
    "v_exp_f32 v255, v181\n"
    "s_nop 0\n"
    "v_add_f32 v24, v24, v254\n"
    "v_add_f32 v24, v24, v255\n"
    "v_cvt_pk_bf16_f32 v226, v254, v255\n"
    "v_exp_f32 v254, v182\n"
    "v_exp_f32 v255, v183\n"
    "s_nop 0\n"
    "v_add_f32 v24, v24, v254\n"
    "v_add_f32 v24, v24, v255\n"
    "v_cvt_pk_bf16_f32 v227, v254, v255\n"
    "v_exp_f32 v254, v184\n"
    "v_exp_f32 v255, v185\n"
    "s_nop 0\n"
    "v_add_f32 v24, v24, v254\n"
    "v_add_f32 v24, v24, v255\n"
    "v_cvt_pk_bf16_f32 v228, v254, v255\n"
    "v_exp_f32 v254, v186\n"
    "v_exp_f32 v255, v187\n"
    "s_nop 0\n"
    "v_add_f32 v24, v24, v254\n"
    "v_add_f32 v24, v24, v255\n"
    "v_cvt_pk_bf16_f32 v229, v254, v255\n"
    "v_exp_f32 v254, v188\n"
    "v_exp_f32 v255, v189\n"
    "s_nop 0\n"
    "v_add_f32 v24, v24, v254\n"
    "v_add_f32 v24, v24, v255\n"
    "v_cvt_pk_bf16_f32 v230, v254, v255\n"
    "v_exp_f32 v254, v190\n"
    "v_exp_f32 v255, v191\n"
    "s_nop 0\n"
    "v_add_f32 v24, v24, v254\n"
    "v_add_f32 v24, v24, v255\n"
    "v_cvt_pk_bf16_f32 v231, v254, v255\n"
    "s_nop 1\n"
    "s_setpc_b64 s[50:51]\n"
    ".L_slowNa1:\n"
    "s_nop 15\n"
    "s_nop 15\n"
    "v_max3_f32 v252, v192, v193, v194\n"
    "v_max3_f32 v252, v252, v195, v196\n"
    "v_max3_f32 v252, v252, v197, v198\n"
    "v_max3_f32 v252, v252, v199, v200\n"
    "v_max3_f32 v252, v252, v201, v202\n"
    "v_max3_f32 v252, v252, v203, v204\n"
    "v_max3_f32 v252, v252, v205, v206\n"
    "v_max_f32 v252, v252, v207\n"
    "v_mov_b32 v253, v252\n"
    "s_nop 1\n"
    "v_permlane32_swap_b32 v252, v253\n"
    "v_max_f32 v252, v252, v253\n"
    "v_max_f32 v252, 0, v252\n"
    "v_sub_f32 v192, v192, v252\n"
    "v_sub_f32 v193, v193, v252\n"
    "v_sub_f32 v194, v194, v252\n"
    "v_sub_f32 v195, v195, v252\n"
    "v_sub_f32 v196, v196, v252\n"
    "v_sub_f32 v197, v197, v252\n"
    "v_sub_f32 v198, v198, v252\n"
    "v_sub_f32 v199, v199, v252\n"
    "v_sub_f32 v200, v200, v252\n"
    "v_sub_f32 v201, v201, v252\n"
    "v_sub_f32 v202, v202, v252\n"
    "v_sub_f32 v203, v203, v252\n"
    "v_sub_f32 v204, v204, v252\n"
    "v_sub_f32 v205, v205, v252\n"
    "v_sub_f32 v206, v206, v252\n"
    "v_sub_f32 v207, v207, v252\n"
    "v_sub_f32 v144, v144, v252\n"
    "v_mov_b32 v145, v144\n"
    "v_mov_b32 v146, v144\n"
    "v_mov_b32 v147, v144\n"
    "v_mov_b32 v148, v144\n"
    "v_mov_b32 v149, v144\n"
    "v_mov_b32 v150, v144\n"
    "v_mov_b32 v151, v144\n"
    "v_mov_b32 v152, v144\n"
    "v_mov_b32 v153, v144\n"
    "v_mov_b32 v154, v144\n"
    "v_mov_b32 v155, v144\n"
    "v_mov_b32 v156, v144\n"
    "v_mov_b32 v157, v144\n"
    "v_mov_b32 v158, v144\n"
    "v_mov_b32 v159, v144\n"
    "v_sub_f32 v253, 0, v252\n"
    "v_exp_f32 v253, v253\n"
    "s_nop 0\n"
    "v_mul_f32 v22, v22, v253\n"
    "v_mul_f32 v48, v48, v253\n"
    "v_mul_f32 v49, v49, v253\n"
    "v_mul_f32 v50, v50, v253\n"
    "v_mul_f32 v51, v51, v253\n"
    "v_mul_f32 v52, v52, v253\n"
    "v_mul_f32 v53, v53, v253\n"
    "v_mul_f32 v54, v54, v253\n"
    "v_mul_f32 v55, v55, v253\n"
    "v_mul_f32 v56, v56, v253\n"
    "v_mul_f32 v57, v57, v253\n"
    "v_mul_f32 v58, v58, v253\n"
    "v_mul_f32 v59, v59, v253\n"
    "v_mul_f32 v60, v60, v253\n"
    "v_mul_f32 v61, v61, v253\n"
    "v_mul_f32 v62, v62, v253\n"
    "v_mul_f32 v63, v63, v253\n"
    "v_mul_f32 v96, v96, v253\n"
    "v_mul_f32 v97, v97, v253\n"
    "v_mul_f32 v98, v98, v253\n"
    "v_mul_f32 v99, v99, v253\n"
    "v_mul_f32 v100, v100, v253\n"
    "v_mul_f32 v101, v101, v253\n"
    "v_mul_f32 v102, v102, v253\n"
    "v_mul_f32 v103, v103, v253\n"
    "v_mul_f32 v104, v104, v253\n"
    "v_mul_f32 v105, v105, v253\n"
    "v_mul_f32 v106, v106, v253\n"
    "v_mul_f32 v107, v107, v253\n"
    "v_mul_f32 v108, v108, v253\n"
    "v_mul_f32 v109, v109, v253\n"
    "v_mul_f32 v110, v110, v253\n"
    "v_mul_f32 v111, v111, v253\n"
    "v_accvgpr_read_b32 v254, a16\n"
    "v_mul_f32 v254, v254, v253\n"
    "v_accvgpr_write_b32 a16, v254\n"
    "v_accvgpr_read_b32 v254, a17\n"
    "v_mul_f32 v254, v254, v253\n"
    "v_accvgpr_write_b32 a17, v254\n"
    "v_accvgpr_read_b32 v254, a18\n"
    "v_mul_f32 v254, v254, v253\n"
    "v_accvgpr_write_b32 a18, v254\n"
    "v_accvgpr_read_b32 v254, a19\n"
    "v_mul_f32 v254, v254, v253\n"
    "v_accvgpr_write_b32 a19, v254\n"
    "v_accvgpr_read_b32 v254, a20\n"
    "v_mul_f32 v254, v254, v253\n"
    "v_accvgpr_write_b32 a20, v254\n"
    "v_accvgpr_read_b32 v254, a21\n"
    "v_mul_f32 v254, v254, v253\n"
    "v_accvgpr_write_b32 a21, v254\n"
    "v_accvgpr_read_b32 v254, a22\n"
    "v_mul_f32 v254, v254, v253\n"
    "v_accvgpr_write_b32 a22, v254\n"
    "v_accvgpr_read_b32 v254, a23\n"
    "v_mul_f32 v254, v254, v253\n"
    "v_accvgpr_write_b32 a23, v254\n"
    "v_accvgpr_read_b32 v254, a24\n"
    "v_mul_f32 v254, v254, v253\n"
    "v_accvgpr_write_b32 a24, v254\n"
    "v_accvgpr_read_b32 v254, a25\n"
    "v_mul_f32 v254, v254, v253\n"
    "v_accvgpr_write_b32 a25, v254\n"
    "v_accvgpr_read_b32 v254, a26\n"
    "v_mul_f32 v254, v254, v253\n"
    "v_accvgpr_write_b32 a26, v254\n"
    "v_accvgpr_read_b32 v254, a27\n"
    "v_mul_f32 v254, v254, v253\n"
    "v_accvgpr_write_b32 a27, v254\n"
    "v_accvgpr_read_b32 v254, a28\n"
    "v_mul_f32 v254, v254, v253\n"
    "v_accvgpr_write_b32 a28, v254\n"
    "v_accvgpr_read_b32 v254, a29\n"
    "v_mul_f32 v254, v254, v253\n"
    "v_accvgpr_write_b32 a29, v254\n"
    "v_accvgpr_read_b32 v254, a30\n"
    "v_mul_f32 v254, v254, v253\n"
    "v_accvgpr_write_b32 a30, v254\n"
    "v_accvgpr_read_b32 v254, a31\n"
    "v_mul_f32 v254, v254, v253\n"
    "v_accvgpr_write_b32 a31, v254\n"
    "v_accvgpr_read_b32 v254, a64\n"
    "v_mul_f32 v254, v254, v253\n"
    "v_accvgpr_write_b32 a64, v254\n"
    "v_accvgpr_read_b32 v254, a65\n"
    "v_mul_f32 v254, v254, v253\n"
    "v_accvgpr_write_b32 a65, v254\n"
    "v_accvgpr_read_b32 v254, a66\n"
    "v_mul_f32 v254, v254, v253\n"
    "v_accvgpr_write_b32 a66, v254\n"
    "v_accvgpr_read_b32 v254, a67\n"
    "v_mul_f32 v254, v254, v253\n"
    "v_accvgpr_write_b32 a67, v254\n"
    "v_accvgpr_read_b32 v254, a68\n"
    "v_mul_f32 v254, v254, v253\n"
    "v_accvgpr_write_b32 a68, v254\n"
    "v_accvgpr_read_b32 v254, a69\n"
    "v_mul_f32 v254, v254, v253\n"
    "v_accvgpr_write_b32 a69, v254\n"
    "v_accvgpr_read_b32 v254, a70\n"
    "v_mul_f32 v254, v254, v253\n"
    "v_accvgpr_write_b32 a70, v254\n"
    "v_accvgpr_read_b32 v254, a71\n"
    "v_mul_f32 v254, v254, v253\n"
    "v_accvgpr_write_b32 a71, v254\n"
    "v_accvgpr_read_b32 v254, a72\n"
    "v_mul_f32 v254, v254, v253\n"
    "v_accvgpr_write_b32 a72, v254\n"
    "v_accvgpr_read_b32 v254, a73\n"
    "v_mul_f32 v254, v254, v253\n"
    "v_accvgpr_write_b32 a73, v254\n"
    "v_accvgpr_read_b32 v254, a74\n"
    "v_mul_f32 v254, v254, v253\n"
    "v_accvgpr_write_b32 a74, v254\n"
    "v_accvgpr_read_b32 v254, a75\n"
    "v_mul_f32 v254, v254, v253\n"
    "v_accvgpr_write_b32 a75, v254\n"
    "v_accvgpr_read_b32 v254, a76\n"
    "v_mul_f32 v254, v254, v253\n"
    "v_accvgpr_write_b32 a76, v254\n"
    "v_accvgpr_read_b32 v254, a77\n"
    "v_mul_f32 v254, v254, v253\n"
    "v_accvgpr_write_b32 a77, v254\n"
    "v_accvgpr_read_b32 v254, a78\n"
    "v_mul_f32 v254, v254, v253\n"
    "v_accvgpr_write_b32 a78, v254\n"
    "v_accvgpr_read_b32 v254, a79\n"
    "v_mul_f32 v254, v254, v253\n"
    "v_accvgpr_write_b32 a79, v254\n"
    "v_exp_f32 v254, v192\n"
    "v_exp_f32 v255, v193\n"
    "s_nop 0\n"
    "v_add_f32 v24, v254, v255\n"
    "v_cvt_pk_bf16_f32 v232, v254, v255\n"
    "v_exp_f32 v254, v194\n"
    "v_exp_f32 v255, v195\n"
    "s_nop 0\n"
    "v_add_f32 v24, v24, v254\n"
    "v_add_f32 v24, v24, v255\n"
    "v_cvt_pk_bf16_f32 v233, v254, v255\n"
    "v_exp_f32 v254, v196\n"
    "v_exp_f32 v255, v197\n"
    "s_nop 0\n"
    "v_add_f32 v24, v24, v254\n"
    "v_add_f32 v24, v24, v255\n"
    "v_cvt_pk_bf16_f32 v234, v254, v255\n"
    "v_exp_f32 v254, v198\n"
    "v_exp_f32 v255, v199\n"
    "s_nop 0\n"
    "v_add_f32 v24, v24, v254\n"
    "v_add_f32 v24, v24, v255\n"
    "v_cvt_pk_bf16_f32 v235, v254, v255\n"
    "v_exp_f32 v254, v200\n"
    "v_exp_f32 v255, v201\n"
    "s_nop 0\n"
    "v_add_f32 v24, v24, v254\n"
    "v_add_f32 v24, v24, v255\n"
    "v_cvt_pk_bf16_f32 v236, v254, v255\n"
    "v_exp_f32 v254, v202\n"
    "v_exp_f32 v255, v203\n"
    "s_nop 0\n"
    "v_add_f32 v24, v24, v254\n"
    "v_add_f32 v24, v24, v255\n"
    "v_cvt_pk_bf16_f32 v237, v254, v255\n"
    "v_exp_f32 v254, v204\n"
    "v_exp_f32 v255, v205\n"
    "s_nop 0\n"
    "v_add_f32 v24, v24, v254\n"
    "v_add_f32 v24, v24, v255\n"
    "v_cvt_pk_bf16_f32 v238, v254, v255\n"
    "v_exp_f32 v254, v206\n"
    "v_exp_f32 v255, v207\n"
    "s_nop 0\n"
    "v_add_f32 v24, v24, v254\n"
    "v_add_f32 v24, v24, v255\n"
    "v_cvt_pk_bf16_f32 v239, v254, v255\n"
    "s_nop 1\n"
    "s_setpc_b64 s[50:51]\n"
    ".L_slowNa2:\n"
    "s_nop 15\n"
    "s_nop 15\n"
    "v_max3_f32 v252, v208, v209, v210\n"
    "v_max3_f32 v252, v252, v211, v212\n"
    "v_max3_f32 v252, v252, v213, v214\n"
    "v_max3_f32 v252, v252, v215, v216\n"
    "v_max3_f32 v252, v252, v217, v218\n"
    "v_max3_f32 v252, v252, v219, v220\n"
    "v_max3_f32 v252, v252, v221, v222\n"
    "v_max_f32 v252, v252, v223\n"
    "v_mov_b32 v253, v252\n"
    "s_nop 1\n"
    "v_permlane32_swap_b32 v252, v253\n"
    "v_max_f32 v252, v252, v253\n"
    "v_max_f32 v252, 0, v252\n"
    "v_sub_f32 v208, v208, v252\n"
    "v_sub_f32 v209, v209, v252\n"
    "v_sub_f32 v210, v210, v252\n"
    "v_sub_f32 v211, v211, v252\n"
    "v_sub_f32 v212, v212, v252\n"
    "v_sub_f32 v213, v213, v252\n"
    "v_sub_f32 v214, v214, v252\n"
    "v_sub_f32 v215, v215, v252\n"
    "v_sub_f32 v216, v216, v252\n"
    "v_sub_f32 v217, v217, v252\n"
    "v_sub_f32 v218, v218, v252\n"
    "v_sub_f32 v219, v219, v252\n"
    "v_sub_f32 v220, v220, v252\n"
    "v_sub_f32 v221, v221, v252\n"
    "v_sub_f32 v222, v222, v252\n"
    "v_sub_f32 v223, v223, v252\n"
    "v_sub_f32 v160, v160, v252\n"
    "v_mov_b32 v161, v160\n"
    "v_mov_b32 v162, v160\n"
    "v_mov_b32 v163, v160\n"
    "v_mov_b32 v164, v160\n"
    "v_mov_b32 v165, v160\n"
    "v_mov_b32 v166, v160\n"
    "v_mov_b32 v167, v160\n"
    "v_mov_b32 v168, v160\n"
    "v_mov_b32 v169, v160\n"
    "v_mov_b32 v170, v160\n"
    "v_mov_b32 v171, v160\n"
    "v_mov_b32 v172, v160\n"
    "v_mov_b32 v173, v160\n"
    "v_mov_b32 v174, v160\n"
    "v_mov_b32 v175, v160\n"
    "v_sub_f32 v253, 0, v252\n"
    "v_exp_f32 v253, v253\n"
    "s_nop 0\n"
    "v_mul_f32 v23, v23, v253\n"
    "v_mul_f32 v64, v64, v253\n"
    "v_mul_f32 v65, v65, v253\n"
    "v_mul_f32 v66, v66, v253\n"
    "v_mul_f32 v67, v67, v253\n"
    "v_mul_f32 v68, v68, v253\n"
    "v_mul_f32 v69, v69, v253\n"
    "v_mul_f32 v70, v70, v253\n"
    "v_mul_f32 v71, v71, v253\n"
    "v_mul_f32 v72, v72, v253\n"
    "v_mul_f32 v73, v73, v253\n"
    "v_mul_f32 v74, v74, v253\n"
    "v_mul_f32 v75, v75, v253\n"
    "v_mul_f32 v76, v76, v253\n"
    "v_mul_f32 v77, v77, v253\n"
    "v_mul_f32 v78, v78, v253\n"
    "v_mul_f32 v79, v79, v253\n"
    "v_mul_f32 v112, v112, v253\n"
    "v_mul_f32 v113, v113, v253\n"
    "v_mul_f32 v114, v114, v253\n"
    "v_mul_f32 v115, v115, v253\n"
    "v_mul_f32 v116, v116, v253\n"
    "v_mul_f32 v117, v117, v253\n"
    "v_mul_f32 v118, v118, v253\n"
    "v_mul_f32 v119, v119, v253\n"
    "v_mul_f32 v120, v120, v253\n"
    "v_mul_f32 v121, v121, v253\n"
    "v_mul_f32 v122, v122, v253\n"
    "v_mul_f32 v123, v123, v253\n"
    "v_mul_f32 v124, v124, v253\n"
    "v_mul_f32 v125, v125, v253\n"
    "v_mul_f32 v126, v126, v253\n"
    "v_mul_f32 v127, v127, v253\n"
    "v_accvgpr_read_b32 v254, a32\n"
    "v_mul_f32 v254, v254, v253\n"
    "v_accvgpr_write_b32 a32, v254\n"
    "v_accvgpr_read_b32 v254, a33\n"
    "v_mul_f32 v254, v254, v253\n"
    "v_accvgpr_write_b32 a33, v254\n"
    "v_accvgpr_read_b32 v254, a34\n"
    "v_mul_f32 v254, v254, v253\n"
    "v_accvgpr_write_b32 a34, v254\n"
    "v_accvgpr_read_b32 v254, a35\n"
    "v_mul_f32 v254, v254, v253\n"
    "v_accvgpr_write_b32 a35, v254\n"
    "v_accvgpr_read_b32 v254, a36\n"
    "v_mul_f32 v254, v254, v253\n"
    "v_accvgpr_write_b32 a36, v254\n"
    "v_accvgpr_read_b32 v254, a37\n"
    "v_mul_f32 v254, v254, v253\n"
    "v_accvgpr_write_b32 a37, v254\n"
    "v_accvgpr_read_b32 v254, a38\n"
    "v_mul_f32 v254, v254, v253\n"
    "v_accvgpr_write_b32 a38, v254\n"
    "v_accvgpr_read_b32 v254, a39\n"
    "v_mul_f32 v254, v254, v253\n"
    "v_accvgpr_write_b32 a39, v254\n"
    "v_accvgpr_read_b32 v254, a40\n"
    "v_mul_f32 v254, v254, v253\n"
    "v_accvgpr_write_b32 a40, v254\n"
    "v_accvgpr_read_b32 v254, a41\n"
    "v_mul_f32 v254, v254, v253\n"
    "v_accvgpr_write_b32 a41, v254\n"
    "v_accvgpr_read_b32 v254, a42\n"
    "v_mul_f32 v254, v254, v253\n"
    "v_accvgpr_write_b32 a42, v254\n"
    "v_accvgpr_read_b32 v254, a43\n"
    "v_mul_f32 v254, v254, v253\n"
    "v_accvgpr_write_b32 a43, v254\n"
    "v_accvgpr_read_b32 v254, a44\n"
    "v_mul_f32 v254, v254, v253\n"
    "v_accvgpr_write_b32 a44, v254\n"
    "v_accvgpr_read_b32 v254, a45\n"
    "v_mul_f32 v254, v254, v253\n"
    "v_accvgpr_write_b32 a45, v254\n"
    "v_accvgpr_read_b32 v254, a46\n"
    "v_mul_f32 v254, v254, v253\n"
    "v_accvgpr_write_b32 a46, v254\n"
    "v_accvgpr_read_b32 v254, a47\n"
    "v_mul_f32 v254, v254, v253\n"
    "v_accvgpr_write_b32 a47, v254\n"
    "v_accvgpr_read_b32 v254, a80\n"
    "v_mul_f32 v254, v254, v253\n"
    "v_accvgpr_write_b32 a80, v254\n"
    "v_accvgpr_read_b32 v254, a81\n"
    "v_mul_f32 v254, v254, v253\n"
    "v_accvgpr_write_b32 a81, v254\n"
    "v_accvgpr_read_b32 v254, a82\n"
    "v_mul_f32 v254, v254, v253\n"
    "v_accvgpr_write_b32 a82, v254\n"
    "v_accvgpr_read_b32 v254, a83\n"
    "v_mul_f32 v254, v254, v253\n"
    "v_accvgpr_write_b32 a83, v254\n"
    "v_accvgpr_read_b32 v254, a84\n"
    "v_mul_f32 v254, v254, v253\n"
    "v_accvgpr_write_b32 a84, v254\n"
    "v_accvgpr_read_b32 v254, a85\n"
    "v_mul_f32 v254, v254, v253\n"
    "v_accvgpr_write_b32 a85, v254\n"
    "v_accvgpr_read_b32 v254, a86\n"
    "v_mul_f32 v254, v254, v253\n"
    "v_accvgpr_write_b32 a86, v254\n"
    "v_accvgpr_read_b32 v254, a87\n"
    "v_mul_f32 v254, v254, v253\n"
    "v_accvgpr_write_b32 a87, v254\n"
    "v_accvgpr_read_b32 v254, a88\n"
    "v_mul_f32 v254, v254, v253\n"
    "v_accvgpr_write_b32 a88, v254\n"
    "v_accvgpr_read_b32 v254, a89\n"
    "v_mul_f32 v254, v254, v253\n"
    "v_accvgpr_write_b32 a89, v254\n"
    "v_accvgpr_read_b32 v254, a90\n"
    "v_mul_f32 v254, v254, v253\n"
    "v_accvgpr_write_b32 a90, v254\n"
    "v_accvgpr_read_b32 v254, a91\n"
    "v_mul_f32 v254, v254, v253\n"
    "v_accvgpr_write_b32 a91, v254\n"
    "v_accvgpr_read_b32 v254, a92\n"
    "v_mul_f32 v254, v254, v253\n"
    "v_accvgpr_write_b32 a92, v254\n"
    "v_accvgpr_read_b32 v254, a93\n"
    "v_mul_f32 v254, v254, v253\n"
    "v_accvgpr_write_b32 a93, v254\n"
    "v_accvgpr_read_b32 v254, a94\n"
    "v_mul_f32 v254, v254, v253\n"
    "v_accvgpr_write_b32 a94, v254\n"
    "v_accvgpr_read_b32 v254, a95\n"
    "v_mul_f32 v254, v254, v253\n"
    "v_accvgpr_write_b32 a95, v254\n"
    "v_exp_f32 v254, v208\n"
    "v_exp_f32 v255, v209\n"
    "s_nop 0\n"
    "v_add_f32 v24, v254, v255\n"
    "v_cvt_pk_bf16_f32 v240, v254, v255\n"
    "v_exp_f32 v254, v210\n"
    "v_exp_f32 v255, v211\n"
    "s_nop 0\n"
    "v_add_f32 v24, v24, v254\n"
    "v_add_f32 v24, v24, v255\n"
    "v_cvt_pk_bf16_f32 v241, v254, v255\n"
    "v_exp_f32 v254, v212\n"
    "v_exp_f32 v255, v213\n"
    "s_nop 0\n"
    "v_add_f32 v24, v24, v254\n"
    "v_add_f32 v24, v24, v255\n"
    "v_cvt_pk_bf16_f32 v242, v254, v255\n"
    "v_exp_f32 v254, v214\n"
    "v_exp_f32 v255, v215\n"
    "s_nop 0\n"
    "v_add_f32 v24, v24, v254\n"
    "v_add_f32 v24, v24, v255\n"
    "v_cvt_pk_bf16_f32 v243, v254, v255\n"
    "v_exp_f32 v254, v216\n"
    "v_exp_f32 v255, v217\n"
    "s_nop 0\n"
    "v_add_f32 v24, v24, v254\n"
    "v_add_f32 v24, v24, v255\n"
    "v_cvt_pk_bf16_f32 v244, v254, v255\n"
    "v_exp_f32 v254, v218\n"
    "v_exp_f32 v255, v219\n"
    "s_nop 0\n"
    "v_add_f32 v24, v24, v254\n"
    "v_add_f32 v24, v24, v255\n"
    "v_cvt_pk_bf16_f32 v245, v254, v255\n"
    "v_exp_f32 v254, v220\n"
    "v_exp_f32 v255, v221\n"
    "s_nop 0\n"
    "v_add_f32 v24, v24, v254\n"
    "v_add_f32 v24, v24, v255\n"
    "v_cvt_pk_bf16_f32 v246, v254, v255\n"
    "v_exp_f32 v254, v222\n"
    "v_exp_f32 v255, v223\n"
    "s_nop 0\n"
    "v_add_f32 v24, v24, v254\n"
    "v_add_f32 v24, v24, v255\n"
    "v_cvt_pk_bf16_f32 v247, v254, v255\n"
    "s_nop 1\n"
    "s_setpc_b64 s[50:51]\n"
    ".L_slowNb0:\n"
    "s_nop 15\n"
    "s_nop 15\n"
    "v_max3_f32 v252, v176, v177, v178\n"
    "v_max3_f32 v252, v252, v179, v180\n"
    "v_max3_f32 v252, v252, v181, v182\n"
    "v_max3_f32 v252, v252, v183, v184\n"
    "v_max3_f32 v252, v252, v185, v186\n"
    "v_max3_f32 v252, v252, v187, v188\n"
    "v_max3_f32 v252, v252, v189, v190\n"
    "v_max_f32 v252, v252, v191\n"
    "v_mov_b32 v253, v252\n"
    "s_nop 1\n"
    "v_permlane32_swap_b32 v252, v253\n"
    "v_max_f32 v252, v252, v253\n"
    "v_max_f32 v252, 0, v252\n"
    "v_sub_f32 v176, v176, v252\n"
    "v_sub_f32 v177, v177, v252\n"
    "v_sub_f32 v178, v178, v252\n"
    "v_sub_f32 v179, v179, v252\n"
    "v_sub_f32 v180, v180, v252\n"
    "v_sub_f32 v181, v181, v252\n"
    "v_sub_f32 v182, v182, v252\n"
    "v_sub_f32 v183, v183, v252\n"
    "v_sub_f32 v184, v184, v252\n"
    "v_sub_f32 v185, v185, v252\n"
    "v_sub_f32 v186, v186, v252\n"
    "v_sub_f32 v187, v187, v252\n"
    "v_sub_f32 v188, v188, v252\n"
    "v_sub_f32 v189, v189, v252\n"
    "v_sub_f32 v190, v190, v252\n"
    "v_sub_f32 v191, v191, v252\n"
    "v_sub_f32 v128, v128, v252\n"
    "v_mov_b32 v129, v128\n"
    "v_mov_b32 v130, v128\n"
    "v_mov_b32 v131, v128\n"
    "v_mov_b32 v132, v128\n"
    "v_mov_b32 v133, v128\n"
    "v_mov_b32 v134, v128\n"
    "v_mov_b32 v135, v128\n"
    "v_mov_b32 v136, v128\n"
    "v_mov_b32 v137, v128\n"
    "v_mov_b32 v138, v128\n"
    "v_mov_b32 v139, v128\n"
    "v_mov_b32 v140, v128\n"
    "v_mov_b32 v141, v128\n"
    "v_mov_b32 v142, v128\n"
    "v_mov_b32 v143, v128\n"
    "v_sub_f32 v253, 0, v252\n"
    "v_exp_f32 v253, v253\n"
    "s_nop 0\n"
    "v_mul_f32 v21, v21, v253\n"
    "v_mul_f32 v32, v32, v253\n"
    "v_mul_f32 v33, v33, v253\n"
    "v_mul_f32 v34, v34, v253\n"
    "v_mul_f32 v35, v35, v253\n"
    "v_mul_f32 v36, v36, v253\n"
    "v_mul_f32 v37, v37, v253\n"
    "v_mul_f32 v38, v38, v253\n"
    "v_mul_f32 v39, v39, v253\n"
    "v_mul_f32 v40, v40, v253\n"
    "v_mul_f32 v41, v41, v253\n"
    "v_mul_f32 v42, v42, v253\n"
    "v_mul_f32 v43, v43, v253\n"
    "v_mul_f32 v44, v44, v253\n"
    "v_mul_f32 v45, v45, v253\n"
    "v_mul_f32 v46, v46, v253\n"
    "v_mul_f32 v47, v47, v253\n"
    "v_mul_f32 v80, v80, v253\n"
    "v_mul_f32 v81, v81, v253\n"
    "v_mul_f32 v82, v82, v253\n"
    "v_mul_f32 v83, v83, v253\n"
    "v_mul_f32 v84, v84, v253\n"
    "v_mul_f32 v85, v85, v253\n"
    "v_mul_f32 v86, v86, v253\n"
    "v_mul_f32 v87, v87, v253\n"
    "v_mul_f32 v88, v88, v253\n"
    "v_mul_f32 v89, v89, v253\n"
    "v_mul_f32 v90, v90, v253\n"
    "v_mul_f32 v91, v91, v253\n"
    "v_mul_f32 v92, v92, v253\n"
    "v_mul_f32 v93, v93, v253\n"
    "v_mul_f32 v94, v94, v253\n"
    "v_mul_f32 v95, v95, v253\n"
    "v_accvgpr_read_b32 v254, a0\n"
    "v_mul_f32 v254, v254, v253\n"
    "v_accvgpr_write_b32 a0, v254\n"
    "v_accvgpr_read_b32 v254, a1\n"
    "v_mul_f32 v254, v254, v253\n"
    "v_accvgpr_write_b32 a1, v254\n"
    "v_accvgpr_read_b32 v254, a2\n"
    "v_mul_f32 v254, v254, v253\n"
    "v_accvgpr_write_b32 a2, v254\n"
    "v_accvgpr_read_b32 v254, a3\n"
    "v_mul_f32 v254, v254, v253\n"
    "v_accvgpr_write_b32 a3, v254\n"
    "v_accvgpr_read_b32 v254, a4\n"
    "v_mul_f32 v254, v254, v253\n"
    "v_accvgpr_write_b32 a4, v254\n"
    "v_accvgpr_read_b32 v254, a5\n"
    "v_mul_f32 v254, v254, v253\n"
    "v_accvgpr_write_b32 a5, v254\n"
    "v_accvgpr_read_b32 v254, a6\n"
    "v_mul_f32 v254, v254, v253\n"
    "v_accvgpr_write_b32 a6, v254\n"
    "v_accvgpr_read_b32 v254, a7\n"
    "v_mul_f32 v254, v254, v253\n"
    "v_accvgpr_write_b32 a7, v254\n"
    "v_accvgpr_read_b32 v254, a8\n"
    "v_mul_f32 v254, v254, v253\n"
    "v_accvgpr_write_b32 a8, v254\n"
    "v_accvgpr_read_b32 v254, a9\n"
    "v_mul_f32 v254, v254, v253\n"
    "v_accvgpr_write_b32 a9, v254\n"
    "v_accvgpr_read_b32 v254, a10\n"
    "v_mul_f32 v254, v254, v253\n"
    "v_accvgpr_write_b32 a10, v254\n"
    "v_accvgpr_read_b32 v254, a11\n"
    "v_mul_f32 v254, v254, v253\n"
    "v_accvgpr_write_b32 a11, v254\n"
    "v_accvgpr_read_b32 v254, a12\n"
    "v_mul_f32 v254, v254, v253\n"
    "v_accvgpr_write_b32 a12, v254\n"
    "v_accvgpr_read_b32 v254, a13\n"
    "v_mul_f32 v254, v254, v253\n"
    "v_accvgpr_write_b32 a13, v254\n"
    "v_accvgpr_read_b32 v254, a14\n"
    "v_mul_f32 v254, v254, v253\n"
    "v_accvgpr_write_b32 a14, v254\n"
    "v_accvgpr_read_b32 v254, a15\n"
    "v_mul_f32 v254, v254, v253\n"
    "v_accvgpr_write_b32 a15, v254\n"
    "v_accvgpr_read_b32 v254, a48\n"
    "v_mul_f32 v254, v254, v253\n"
    "v_accvgpr_write_b32 a48, v254\n"
    "v_accvgpr_read_b32 v254, a49\n"
    "v_mul_f32 v254, v254, v253\n"
    "v_accvgpr_write_b32 a49, v254\n"
    "v_accvgpr_read_b32 v254, a50\n"
    "v_mul_f32 v254, v254, v253\n"
    "v_accvgpr_write_b32 a50, v254\n"
    "v_accvgpr_read_b32 v254, a51\n"
    "v_mul_f32 v254, v254, v253\n"
    "v_accvgpr_write_b32 a51, v254\n"
    "v_accvgpr_read_b32 v254, a52\n"
    "v_mul_f32 v254, v254, v253\n"
    "v_accvgpr_write_b32 a52, v254\n"
    "v_accvgpr_read_b32 v254, a53\n"
    "v_mul_f32 v254, v254, v253\n"
    "v_accvgpr_write_b32 a53, v254\n"
    "v_accvgpr_read_b32 v254, a54\n"
    "v_mul_f32 v254, v254, v253\n"
    "v_accvgpr_write_b32 a54, v254\n"
    "v_accvgpr_read_b32 v254, a55\n"
    "v_mul_f32 v254, v254, v253\n"
    "v_accvgpr_write_b32 a55, v254\n"
    "v_accvgpr_read_b32 v254, a56\n"
    "v_mul_f32 v254, v254, v253\n"
    "v_accvgpr_write_b32 a56, v254\n"
    "v_accvgpr_read_b32 v254, a57\n"
    "v_mul_f32 v254, v254, v253\n"
    "v_accvgpr_write_b32 a57, v254\n"
    "v_accvgpr_read_b32 v254, a58\n"
    "v_mul_f32 v254, v254, v253\n"
    "v_accvgpr_write_b32 a58, v254\n"
    "v_accvgpr_read_b32 v254, a59\n"
    "v_mul_f32 v254, v254, v253\n"
    "v_accvgpr_write_b32 a59, v254\n"
    "v_accvgpr_read_b32 v254, a60\n"
    "v_mul_f32 v254, v254, v253\n"
    "v_accvgpr_write_b32 a60, v254\n"
    "v_accvgpr_read_b32 v254, a61\n"
    "v_mul_f32 v254, v254, v253\n"
    "v_accvgpr_write_b32 a61, v254\n"
    "v_accvgpr_read_b32 v254, a62\n"
    "v_mul_f32 v254, v254, v253\n"
    "v_accvgpr_write_b32 a62, v254\n"
    "v_accvgpr_read_b32 v254, a63\n"
    "v_mul_f32 v254, v254, v253\n"
    "v_accvgpr_write_b32 a63, v254\n"
    "v_exp_f32 v254, v176\n"
    "v_exp_f32 v255, v177\n"
    "s_nop 0\n"
    "v_add_f32 v25, v254, v255\n"
    "v_cvt_pk_bf16_f32 v224, v254, v255\n"
    "v_exp_f32 v254, v178\n"
    "v_exp_f32 v255, v179\n"
    "s_nop 0\n"
    "v_add_f32 v25, v25, v254\n"
    "v_add_f32 v25, v25, v255\n"
    "v_cvt_pk_bf16_f32 v225, v254, v255\n"
    "v_exp_f32 v254, v180\n"
    "v_exp_f32 v255, v181\n"
    "s_nop 0\n"
    "v_add_f32 v25, v25, v254\n"
    "v_add_f32 v25, v25, v255\n"
    "v_cvt_pk_bf16_f32 v226, v254, v255\n"
    "v_exp_f32 v254, v182\n"
    "v_exp_f32 v255, v183\n"
    "s_nop 0\n"
    "v_add_f32 v25, v25, v254\n"
    "v_add_f32 v25, v25, v255\n"
    "v_cvt_pk_bf16_f32 v227, v254, v255\n"
    "v_exp_f32 v254, v184\n"
    "v_exp_f32 v255, v185\n"
    "s_nop 0\n"
    "v_add_f32 v25, v25, v254\n"
    "v_add_f32 v25, v25, v255\n"
    "v_cvt_pk_bf16_f32 v228, v254, v255\n"
    "v_exp_f32 v254, v186\n"
    "v_exp_f32 v255, v187\n"
    "s_nop 0\n"
    "v_add_f32 v25, v25, v254\n"
    "v_add_f32 v25, v25, v255\n"
    "v_cvt_pk_bf16_f32 v229, v254, v255\n"
    "v_exp_f32 v254, v188\n"
    "v_exp_f32 v255, v189\n"
    "s_nop 0\n"
    "v_add_f32 v25, v25, v254\n"
    "v_add_f32 v25, v25, v255\n"
    "v_cvt_pk_bf16_f32 v230, v254, v255\n"
    "v_exp_f32 v254, v190\n"
    "v_exp_f32 v255, v191\n"
    "s_nop 0\n"
    "v_add_f32 v25, v25, v254\n"
    "v_add_f32 v25, v25, v255\n"
    "v_cvt_pk_bf16_f32 v231, v254, v255\n"
    "s_nop 1\n"
    "s_setpc_b64 s[50:51]\n"
    ".L_slowNb1:\n"
    "s_nop 15\n"
    "s_nop 15\n"
    "v_max3_f32 v252, v192, v193, v194\n"
    "v_max3_f32 v252, v252, v195, v196\n"
    "v_max3_f32 v252, v252, v197, v198\n"
    "v_max3_f32 v252, v252, v199, v200\n"
    "v_max3_f32 v252, v252, v201, v202\n"
    "v_max3_f32 v252, v252, v203, v204\n"
    "v_max3_f32 v252, v252, v205, v206\n"
    "v_max_f32 v252, v252, v207\n"
    "v_mov_b32 v253, v252\n"
    "s_nop 1\n"
    "v_permlane32_swap_b32 v252, v253\n"
    "v_max_f32 v252, v252, v253\n"
    "v_max_f32 v252, 0, v252\n"
    "v_sub_f32 v192, v192, v252\n"
    "v_sub_f32 v193, v193, v252\n"
    "v_sub_f32 v194, v194, v252\n"
    "v_sub_f32 v195, v195, v252\n"
    "v_sub_f32 v196, v196, v252\n"
    "v_sub_f32 v197, v197, v252\n"
    "v_sub_f32 v198, v198, v252\n"
    "v_sub_f32 v199, v199, v252\n"
    "v_sub_f32 v200, v200, v252\n"
    "v_sub_f32 v201, v201, v252\n"
    "v_sub_f32 v202, v202, v252\n"
    "v_sub_f32 v203, v203, v252\n"
    "v_sub_f32 v204, v204, v252\n"
    "v_sub_f32 v205, v205, v252\n"
    "v_sub_f32 v206, v206, v252\n"
    "v_sub_f32 v207, v207, v252\n"
    "v_sub_f32 v144, v144, v252\n"
    "v_mov_b32 v145, v144\n"
    "v_mov_b32 v146, v144\n"
    "v_mov_b32 v147, v144\n"
    "v_mov_b32 v148, v144\n"
    "v_mov_b32 v149, v144\n"
    "v_mov_b32 v150, v144\n"
    "v_mov_b32 v151, v144\n"
    "v_mov_b32 v152, v144\n"
    "v_mov_b32 v153, v144\n"
    "v_mov_b32 v154, v144\n"
    "v_mov_b32 v155, v144\n"
    "v_mov_b32 v156, v144\n"
    "v_mov_b32 v157, v144\n"
    "v_mov_b32 v158, v144\n"
    "v_mov_b32 v159, v144\n"
    "v_sub_f32 v253, 0, v252\n"
    "v_exp_f32 v253, v253\n"
    "s_nop 0\n"
    "v_mul_f32 v22, v22, v253\n"
    "v_mul_f32 v48, v48, v253\n"
    "v_mul_f32 v49, v49, v253\n"
    "v_mul_f32 v50, v50, v253\n"
    "v_mul_f32 v51, v51, v253\n"
    "v_mul_f32 v52, v52, v253\n"
    "v_mul_f32 v53, v53, v253\n"
    "v_mul_f32 v54, v54, v253\n"
    "v_mul_f32 v55, v55, v253\n"
    "v_mul_f32 v56, v56, v253\n"
    "v_mul_f32 v57, v57, v253\n"
    "v_mul_f32 v58, v58, v253\n"
    "v_mul_f32 v59, v59, v253\n"
    "v_mul_f32 v60, v60, v253\n"
    "v_mul_f32 v61, v61, v253\n"
    "v_mul_f32 v62, v62, v253\n"
    "v_mul_f32 v63, v63, v253\n"
    "v_mul_f32 v96, v96, v253\n"
    "v_mul_f32 v97, v97, v253\n"
    "v_mul_f32 v98, v98, v253\n"
    "v_mul_f32 v99, v99, v253\n"
    "v_mul_f32 v100, v100, v253\n"
    "v_mul_f32 v101, v101, v253\n"
    "v_mul_f32 v102, v102, v253\n"
    "v_mul_f32 v103, v103, v253\n"
    "v_mul_f32 v104, v104, v253\n"
    "v_mul_f32 v105, v105, v253\n"
    "v_mul_f32 v106, v106, v253\n"
    "v_mul_f32 v107, v107, v253\n"
    "v_mul_f32 v108, v108, v253\n"
    "v_mul_f32 v109, v109, v253\n"
    "v_mul_f32 v110, v110, v253\n"
    "v_mul_f32 v111, v111, v253\n"
    "v_accvgpr_read_b32 v254, a16\n"
    "v_mul_f32 v254, v254, v253\n"
    "v_accvgpr_write_b32 a16, v254\n"
    "v_accvgpr_read_b32 v254, a17\n"
    "v_mul_f32 v254, v254, v253\n"
    "v_accvgpr_write_b32 a17, v254\n"
    "v_accvgpr_read_b32 v254, a18\n"
    "v_mul_f32 v254, v254, v253\n"
    "v_accvgpr_write_b32 a18, v254\n"
    "v_accvgpr_read_b32 v254, a19\n"
    "v_mul_f32 v254, v254, v253\n"
    "v_accvgpr_write_b32 a19, v254\n"
    "v_accvgpr_read_b32 v254, a20\n"
    "v_mul_f32 v254, v254, v253\n"
    "v_accvgpr_write_b32 a20, v254\n"
    "v_accvgpr_read_b32 v254, a21\n"
    "v_mul_f32 v254, v254, v253\n"
    "v_accvgpr_write_b32 a21, v254\n"
    "v_accvgpr_read_b32 v254, a22\n"
    "v_mul_f32 v254, v254, v253\n"
    "v_accvgpr_write_b32 a22, v254\n"
    "v_accvgpr_read_b32 v254, a23\n"
    "v_mul_f32 v254, v254, v253\n"
    "v_accvgpr_write_b32 a23, v254\n"
    "v_accvgpr_read_b32 v254, a24\n"
    "v_mul_f32 v254, v254, v253\n"
    "v_accvgpr_write_b32 a24, v254\n"
    "v_accvgpr_read_b32 v254, a25\n"
    "v_mul_f32 v254, v254, v253\n"
    "v_accvgpr_write_b32 a25, v254\n"
    "v_accvgpr_read_b32 v254, a26\n"
    "v_mul_f32 v254, v254, v253\n"
    "v_accvgpr_write_b32 a26, v254\n"
    "v_accvgpr_read_b32 v254, a27\n"
    "v_mul_f32 v254, v254, v253\n"
    "v_accvgpr_write_b32 a27, v254\n"
    "v_accvgpr_read_b32 v254, a28\n"
    "v_mul_f32 v254, v254, v253\n"
    "v_accvgpr_write_b32 a28, v254\n"
    "v_accvgpr_read_b32 v254, a29\n"
    "v_mul_f32 v254, v254, v253\n"
    "v_accvgpr_write_b32 a29, v254\n"
    "v_accvgpr_read_b32 v254, a30\n"
    "v_mul_f32 v254, v254, v253\n"
    "v_accvgpr_write_b32 a30, v254\n"
    "v_accvgpr_read_b32 v254, a31\n"
    "v_mul_f32 v254, v254, v253\n"
    "v_accvgpr_write_b32 a31, v254\n"
    "v_accvgpr_read_b32 v254, a64\n"
    "v_mul_f32 v254, v254, v253\n"
    "v_accvgpr_write_b32 a64, v254\n"
    "v_accvgpr_read_b32 v254, a65\n"
    "v_mul_f32 v254, v254, v253\n"
    "v_accvgpr_write_b32 a65, v254\n"
    "v_accvgpr_read_b32 v254, a66\n"
    "v_mul_f32 v254, v254, v253\n"
    "v_accvgpr_write_b32 a66, v254\n"
    "v_accvgpr_read_b32 v254, a67\n"
    "v_mul_f32 v254, v254, v253\n"
    "v_accvgpr_write_b32 a67, v254\n"
    "v_accvgpr_read_b32 v254, a68\n"
    "v_mul_f32 v254, v254, v253\n"
    "v_accvgpr_write_b32 a68, v254\n"
    "v_accvgpr_read_b32 v254, a69\n"
    "v_mul_f32 v254, v254, v253\n"
    "v_accvgpr_write_b32 a69, v254\n"
    "v_accvgpr_read_b32 v254, a70\n"
    "v_mul_f32 v254, v254, v253\n"
    "v_accvgpr_write_b32 a70, v254\n"
    "v_accvgpr_read_b32 v254, a71\n"
    "v_mul_f32 v254, v254, v253\n"
    "v_accvgpr_write_b32 a71, v254\n"
    "v_accvgpr_read_b32 v254, a72\n"
    "v_mul_f32 v254, v254, v253\n"
    "v_accvgpr_write_b32 a72, v254\n"
    "v_accvgpr_read_b32 v254, a73\n"
    "v_mul_f32 v254, v254, v253\n"
    "v_accvgpr_write_b32 a73, v254\n"
    "v_accvgpr_read_b32 v254, a74\n"
    "v_mul_f32 v254, v254, v253\n"
    "v_accvgpr_write_b32 a74, v254\n"
    "v_accvgpr_read_b32 v254, a75\n"
    "v_mul_f32 v254, v254, v253\n"
    "v_accvgpr_write_b32 a75, v254\n"
    "v_accvgpr_read_b32 v254, a76\n"
    "v_mul_f32 v254, v254, v253\n"
    "v_accvgpr_write_b32 a76, v254\n"
    "v_accvgpr_read_b32 v254, a77\n"
    "v_mul_f32 v254, v254, v253\n"
    "v_accvgpr_write_b32 a77, v254\n"
    "v_accvgpr_read_b32 v254, a78\n"
    "v_mul_f32 v254, v254, v253\n"
    "v_accvgpr_write_b32 a78, v254\n"
    "v_accvgpr_read_b32 v254, a79\n"
    "v_mul_f32 v254, v254, v253\n"
    "v_accvgpr_write_b32 a79, v254\n"
    "v_exp_f32 v254, v192\n"
    "v_exp_f32 v255, v193\n"
    "s_nop 0\n"
    "v_add_f32 v25, v254, v255\n"
    "v_cvt_pk_bf16_f32 v232, v254, v255\n"
    "v_exp_f32 v254, v194\n"
    "v_exp_f32 v255, v195\n"
    "s_nop 0\n"
    "v_add_f32 v25, v25, v254\n"
    "v_add_f32 v25, v25, v255\n"
    "v_cvt_pk_bf16_f32 v233, v254, v255\n"
    "v_exp_f32 v254, v196\n"
    "v_exp_f32 v255, v197\n"
    "s_nop 0\n"
    "v_add_f32 v25, v25, v254\n"
    "v_add_f32 v25, v25, v255\n"
    "v_cvt_pk_bf16_f32 v234, v254, v255\n"
    "v_exp_f32 v254, v198\n"
    "v_exp_f32 v255, v199\n"
    "s_nop 0\n"
    "v_add_f32 v25, v25, v254\n"
    "v_add_f32 v25, v25, v255\n"
    "v_cvt_pk_bf16_f32 v235, v254, v255\n"
    "v_exp_f32 v254, v200\n"
    "v_exp_f32 v255, v201\n"
    "s_nop 0\n"
    "v_add_f32 v25, v25, v254\n"
    "v_add_f32 v25, v25, v255\n"
    "v_cvt_pk_bf16_f32 v236, v254, v255\n"
    "v_exp_f32 v254, v202\n"
    "v_exp_f32 v255, v203\n"
    "s_nop 0\n"
    "v_add_f32 v25, v25, v254\n"
    "v_add_f32 v25, v25, v255\n"
    "v_cvt_pk_bf16_f32 v237, v254, v255\n"
    "v_exp_f32 v254, v204\n"
    "v_exp_f32 v255, v205\n"
    "s_nop 0\n"
    "v_add_f32 v25, v25, v254\n"
    "v_add_f32 v25, v25, v255\n"
    "v_cvt_pk_bf16_f32 v238, v254, v255\n"
    "v_exp_f32 v254, v206\n"
    "v_exp_f32 v255, v207\n"
    "s_nop 0\n"
    "v_add_f32 v25, v25, v254\n"
    "v_add_f32 v25, v25, v255\n"
    "v_cvt_pk_bf16_f32 v239, v254, v255\n"
    "s_nop 1\n"
    "s_setpc_b64 s[50:51]\n"
    ".L_slowNb2:\n"
    "s_nop 15\n"
    "s_nop 15\n"
    "v_max3_f32 v252, v208, v209, v210\n"
    "v_max3_f32 v252, v252, v211, v212\n"
    "v_max3_f32 v252, v252, v213, v214\n"
    "v_max3_f32 v252, v252, v215, v216\n"
    "v_max3_f32 v252, v252, v217, v218\n"
    "v_max3_f32 v252, v252, v219, v220\n"
    "v_max3_f32 v252, v252, v221, v222\n"
    "v_max_f32 v252, v252, v223\n"
    "v_mov_b32 v253, v252\n"
    "s_nop 1\n"
    "v_permlane32_swap_b32 v252, v253\n"
    "v_max_f32 v252, v252, v253\n"
    "v_max_f32 v252, 0, v252\n"
    "v_sub_f32 v208, v208, v252\n"
    "v_sub_f32 v209, v209, v252\n"
    "v_sub_f32 v210, v210, v252\n"
    "v_sub_f32 v211, v211, v252\n"
    "v_sub_f32 v212, v212, v252\n"
    "v_sub_f32 v213, v213, v252\n"
    "v_sub_f32 v214, v214, v252\n"
    "v_sub_f32 v215, v215, v252\n"
    "v_sub_f32 v216, v216, v252\n"
    "v_sub_f32 v217, v217, v252\n"
    "v_sub_f32 v218, v218, v252\n"
    "v_sub_f32 v219, v219, v252\n"
    "v_sub_f32 v220, v220, v252\n"
    "v_sub_f32 v221, v221, v252\n"
    "v_sub_f32 v222, v222, v252\n"
    "v_sub_f32 v223, v223, v252\n"
    "v_sub_f32 v160, v160, v252\n"
    "v_mov_b32 v161, v160\n"
    "v_mov_b32 v162, v160\n"
    "v_mov_b32 v163, v160\n"
    "v_mov_b32 v164, v160\n"
    "v_mov_b32 v165, v160\n"
    "v_mov_b32 v166, v160\n"
    "v_mov_b32 v167, v160\n"
    "v_mov_b32 v168, v160\n"
    "v_mov_b32 v169, v160\n"
    "v_mov_b32 v170, v160\n"
    "v_mov_b32 v171, v160\n"
    "v_mov_b32 v172, v160\n"
    "v_mov_b32 v173, v160\n"
    "v_mov_b32 v174, v160\n"
    "v_mov_b32 v175, v160\n"
    "v_sub_f32 v253, 0, v252\n"
    "v_exp_f32 v253, v253\n"
    "s_nop 0\n"
    "v_mul_f32 v23, v23, v253\n"
    "v_mul_f32 v64, v64, v253\n"
    "v_mul_f32 v65, v65, v253\n"
    "v_mul_f32 v66, v66, v253\n"
    "v_mul_f32 v67, v67, v253\n"
    "v_mul_f32 v68, v68, v253\n"
    "v_mul_f32 v69, v69, v253\n"
    "v_mul_f32 v70, v70, v253\n"
    "v_mul_f32 v71, v71, v253\n"
    "v_mul_f32 v72, v72, v253\n"
    "v_mul_f32 v73, v73, v253\n"
    "v_mul_f32 v74, v74, v253\n"
    "v_mul_f32 v75, v75, v253\n"
    "v_mul_f32 v76, v76, v253\n"
    "v_mul_f32 v77, v77, v253\n"
    "v_mul_f32 v78, v78, v253\n"
    "v_mul_f32 v79, v79, v253\n"
    "v_mul_f32 v112, v112, v253\n"
    "v_mul_f32 v113, v113, v253\n"
    "v_mul_f32 v114, v114, v253\n"
    "v_mul_f32 v115, v115, v253\n"
    "v_mul_f32 v116, v116, v253\n"
    "v_mul_f32 v117, v117, v253\n"
    "v_mul_f32 v118, v118, v253\n"
    "v_mul_f32 v119, v119, v253\n"
    "v_mul_f32 v120, v120, v253\n"
    "v_mul_f32 v121, v121, v253\n"
    "v_mul_f32 v122, v122, v253\n"
    "v_mul_f32 v123, v123, v253\n"
    "v_mul_f32 v124, v124, v253\n"
    "v_mul_f32 v125, v125, v253\n"
    "v_mul_f32 v126, v126, v253\n"
    "v_mul_f32 v127, v127, v253\n"
    "v_accvgpr_read_b32 v254, a32\n"
    "v_mul_f32 v254, v254, v253\n"
    "v_accvgpr_write_b32 a32, v254\n"
    "v_accvgpr_read_b32 v254, a33\n"
    "v_mul_f32 v254, v254, v253\n"
    "v_accvgpr_write_b32 a33, v254\n"
    "v_accvgpr_read_b32 v254, a34\n"
    "v_mul_f32 v254, v254, v253\n"
    "v_accvgpr_write_b32 a34, v254\n"
    "v_accvgpr_read_b32 v254, a35\n"
    "v_mul_f32 v254, v254, v253\n"
    "v_accvgpr_write_b32 a35, v254\n"
    "v_accvgpr_read_b32 v254, a36\n"
    "v_mul_f32 v254, v254, v253\n"
    "v_accvgpr_write_b32 a36, v254\n"
    "v_accvgpr_read_b32 v254, a37\n"
    "v_mul_f32 v254, v254, v253\n"
    "v_accvgpr_write_b32 a37, v254\n"
    "v_accvgpr_read_b32 v254, a38\n"
    "v_mul_f32 v254, v254, v253\n"
    "v_accvgpr_write_b32 a38, v254\n"
    "v_accvgpr_read_b32 v254, a39\n"
    "v_mul_f32 v254, v254, v253\n"
    "v_accvgpr_write_b32 a39, v254\n"
    "v_accvgpr_read_b32 v254, a40\n"
    "v_mul_f32 v254, v254, v253\n"
    "v_accvgpr_write_b32 a40, v254\n"
    "v_accvgpr_read_b32 v254, a41\n"
    "v_mul_f32 v254, v254, v253\n"
    "v_accvgpr_write_b32 a41, v254\n"
    "v_accvgpr_read_b32 v254, a42\n"
    "v_mul_f32 v254, v254, v253\n"
    "v_accvgpr_write_b32 a42, v254\n"
    "v_accvgpr_read_b32 v254, a43\n"
    "v_mul_f32 v254, v254, v253\n"
    "v_accvgpr_write_b32 a43, v254\n"
    "v_accvgpr_read_b32 v254, a44\n"
    "v_mul_f32 v254, v254, v253\n"
    "v_accvgpr_write_b32 a44, v254\n"
    "v_accvgpr_read_b32 v254, a45\n"
    "v_mul_f32 v254, v254, v253\n"
    "v_accvgpr_write_b32 a45, v254\n"
    "v_accvgpr_read_b32 v254, a46\n"
    "v_mul_f32 v254, v254, v253\n"
    "v_accvgpr_write_b32 a46, v254\n"
    "v_accvgpr_read_b32 v254, a47\n"
    "v_mul_f32 v254, v254, v253\n"
    "v_accvgpr_write_b32 a47, v254\n"
    "v_accvgpr_read_b32 v254, a80\n"
    "v_mul_f32 v254, v254, v253\n"
    "v_accvgpr_write_b32 a80, v254\n"
    "v_accvgpr_read_b32 v254, a81\n"
    "v_mul_f32 v254, v254, v253\n"
    "v_accvgpr_write_b32 a81, v254\n"
    "v_accvgpr_read_b32 v254, a82\n"
    "v_mul_f32 v254, v254, v253\n"
    "v_accvgpr_write_b32 a82, v254\n"
    "v_accvgpr_read_b32 v254, a83\n"
    "v_mul_f32 v254, v254, v253\n"
    "v_accvgpr_write_b32 a83, v254\n"
    "v_accvgpr_read_b32 v254, a84\n"
    "v_mul_f32 v254, v254, v253\n"
    "v_accvgpr_write_b32 a84, v254\n"
    "v_accvgpr_read_b32 v254, a85\n"
    "v_mul_f32 v254, v254, v253\n"
    "v_accvgpr_write_b32 a85, v254\n"
    "v_accvgpr_read_b32 v254, a86\n"
    "v_mul_f32 v254, v254, v253\n"
    "v_accvgpr_write_b32 a86, v254\n"
    "v_accvgpr_read_b32 v254, a87\n"
    "v_mul_f32 v254, v254, v253\n"
    "v_accvgpr_write_b32 a87, v254\n"
    "v_accvgpr_read_b32 v254, a88\n"
    "v_mul_f32 v254, v254, v253\n"
    "v_accvgpr_write_b32 a88, v254\n"
    "v_accvgpr_read_b32 v254, a89\n"
    "v_mul_f32 v254, v254, v253\n"
    "v_accvgpr_write_b32 a89, v254\n"
    "v_accvgpr_read_b32 v254, a90\n"
    "v_mul_f32 v254, v254, v253\n"
    "v_accvgpr_write_b32 a90, v254\n"
    "v_accvgpr_read_b32 v254, a91\n"
    "v_mul_f32 v254, v254, v253\n"
    "v_accvgpr_write_b32 a91, v254\n"
    "v_accvgpr_read_b32 v254, a92\n"
    "v_mul_f32 v254, v254, v253\n"
    "v_accvgpr_write_b32 a92, v254\n"
    "v_accvgpr_read_b32 v254, a93\n"
    "v_mul_f32 v254, v254, v253\n"
    "v_accvgpr_write_b32 a93, v254\n"
    "v_accvgpr_read_b32 v254, a94\n"
    "v_mul_f32 v254, v254, v253\n"
    "v_accvgpr_write_b32 a94, v254\n"
    "v_accvgpr_read_b32 v254, a95\n"
    "v_mul_f32 v254, v254, v253\n"
    "v_accvgpr_write_b32 a95, v254\n"
    "v_exp_f32 v254, v208\n"
    "v_exp_f32 v255, v209\n"
    "s_nop 0\n"
    "v_add_f32 v25, v254, v255\n"
    "v_cvt_pk_bf16_f32 v240, v254, v255\n"
    "v_exp_f32 v254, v210\n"
    "v_exp_f32 v255, v211\n"
    "s_nop 0\n"
    "v_add_f32 v25, v25, v254\n"
    "v_add_f32 v25, v25, v255\n"
    "v_cvt_pk_bf16_f32 v241, v254, v255\n"
    "v_exp_f32 v254, v212\n"
    "v_exp_f32 v255, v213\n"
    "s_nop 0\n"
    "v_add_f32 v25, v25, v254\n"
    "v_add_f32 v25, v25, v255\n"
    "v_cvt_pk_bf16_f32 v242, v254, v255\n"
    "v_exp_f32 v254, v214\n"
    "v_exp_f32 v255, v215\n"
    "s_nop 0\n"
    "v_add_f32 v25, v25, v254\n"
    "v_add_f32 v25, v25, v255\n"
    "v_cvt_pk_bf16_f32 v243, v254, v255\n"
    "v_exp_f32 v254, v216\n"
    "v_exp_f32 v255, v217\n"
    "s_nop 0\n"
    "v_add_f32 v25, v25, v254\n"
    "v_add_f32 v25, v25, v255\n"
    "v_cvt_pk_bf16_f32 v244, v254, v255\n"
    "v_exp_f32 v254, v218\n"
    "v_exp_f32 v255, v219\n"
    "s_nop 0\n"
    "v_add_f32 v25, v25, v254\n"
    "v_add_f32 v25, v25, v255\n"
    "v_cvt_pk_bf16_f32 v245, v254, v255\n"
    "v_exp_f32 v254, v220\n"
    "v_exp_f32 v255, v221\n"
    "s_nop 0\n"
    "v_add_f32 v25, v25, v254\n"
    "v_add_f32 v25, v25, v255\n"
    "v_cvt_pk_bf16_f32 v246, v254, v255\n"
    "v_exp_f32 v254, v222\n"
    "v_exp_f32 v255, v223\n"
    "s_nop 0\n"
    "v_add_f32 v25, v25, v254\n"
    "v_add_f32 v25, v25, v255\n"
    "v_cvt_pk_bf16_f32 v247, v254, v255\n"
    "s_nop 1\n"
    "s_setpc_b64 s[50:51]\n"
    ".L3_refresh:\n"
    "buffer_load_dwordx2 v[246:247], v244, s[64:67], 0 offen sc1\n"
    "s_waitcnt vmcnt(0)\n"
    "s_mov_b64 exec, s[70:71]\n"
    "buffer_store_dwordx2 v[246:247], v250, s[64:67], s74 offen sc1\n"
    "s_mov_b64 exec, -1\n"
    "s_waitcnt vmcnt(0)\n"
    "s_setpc_b64 s[52:53]\n"
    ".L_end:\n"
    :
    : [tid] "v"(threadIdx.x), [bid] "s"(blockIdx.x), [ldsb] "s"(ldsb),
      [xp_lo] "s"((unsigned)(uintptr_t)xp), [xp_hi] "s"((unsigned)((uintptr_t)xp >> 32)),
      [kp_lo] "s"((unsigned)(uintptr_t)kp), [kp_hi] "s"((unsigned)((uintptr_t)kp >> 32)),
      [vp_lo] "s"((unsigned)(uintptr_t)vp), [vp_hi] "s"((unsigned)((uintptr_t)vp >> 32)),
      [wp_lo] "s"((unsigned)(uintptr_t)wp), [wp_hi] "s"((unsigned)((uintptr_t)wp >> 32)),
      [wo_lo] "s"((unsigned)(uintptr_t)wo), [wo_hi] "s"((unsigned)((uintptr_t)wo >> 32)),
      [out_lo] "s"((unsigned)(uintptr_t)out), [out_hi] "s"((unsigned)((uintptr_t)out >> 32)),
      [op_lo] "s"((unsigned)(uintptr_t)op), [op_hi] "s"((unsigned)((uintptr_t)op >> 32))
    : "v3", "v4", "v5", "v6", "v7", "v8", "v9", "v10", "v11", "v12", "v13", "v14", "v15", "v16", "v17", "v18", "v19", "v20", "v21", "v22", "v23", "v24", "v25", "v26", "v27", "v28", "v29", "v30", "v31", "v32", "v33", "v34", "v35", "v36", "v37", "v38", "v39", "v40", "v41", "v42", "v43", "v44", "v45", "v46", "v47", "v48", "v49", "v50", "v51", "v52", "v53", "v54", "v55", "v56", "v57", "v58", "v59", "v60", "v61", "v62", "v63", "v64", "v65", "v66", "v67", "v68", "v69", "v70", "v71", "v72", "v73", "v74", "v75", "v76", "v77", "v78", "v79", "v80", "v81", "v82", "v83", "v84", "v85", "v86", "v87", "v88", "v89", "v90", "v91", "v92", "v93", "v94", "v95", "v96", "v97", "v98", "v99", "v100", "v101", "v102", "v103", "v104", "v105", "v106", "v107", "v108", "v109", "v110", "v111", "v112", "v113", "v114", "v115", "v116", "v117", "v118", "v119", "v120", "v121", "v122", "v123", "v124", "v125", "v126", "v127", "v128", "v129", "v130", "v131", "v132", "v133", "v134", "v135", "v136", "v137", "v138", "v139", "v140", "v141", "v142", "v143", "v144", "v145", "v146", "v147", "v148", "v149", "v150", "v151", "v152", "v153", "v154", "v155", "v156", "v157", "v158", "v159", "v160", "v161", "v162", "v163", "v164", "v165", "v166", "v167", "v168", "v169", "v170", "v171", "v172", "v173", "v174", "v175", "v176", "v177", "v178", "v179", "v180", "v181", "v182", "v183", "v184", "v185", "v186", "v187", "v188", "v189", "v190", "v191", "v192", "v193", "v194", "v195", "v196", "v197", "v198", "v199", "v200", "v201", "v202", "v203", "v204", "v205", "v206", "v207", "v208", "v209", "v210", "v211", "v212", "v213", "v214", "v215", "v216", "v217", "v218", "v219", "v220", "v221", "v222", "v223", "v224", "v225", "v226", "v227", "v228", "v229", "v230", "v231", "v232", "v233", "v234", "v235", "v236", "v237", "v238", "v239", "v240", "v241", "v242", "v243", "v244", "v245", "v246", "v247", "v248", "v249", "v250", "v251", "v252", "v253", "v254", "v255", "a0", "a1", "a2", "a3", "a4", "a5", "a6", "a7", "a8", "a9", "a10", "a11", "a12", "a13", "a14", "a15", "a16", "a17", "a18", "a19", "a20", "a21", "a22", "a23", "a24", "a25", "a26", "a27", "a28", "a29", "a30", "a31", "a32", "a33", "a34", "a35", "a36", "a37", "a38", "a39", "a40", "a41", "a42", "a43", "a44", "a45", "a46", "a47", "a48", "a49", "a50", "a51", "a52", "a53", "a54", "a55", "a56", "a57", "a58", "a59", "a60", "a61", "a62", "a63", "a64", "a65", "a66", "a67", "a68", "a69", "a70", "a71", "a72", "a73", "a74", "a75", "a76", "a77", "a78", "a79", "a80", "a81", "a82", "a83", "a84", "a85", "a86", "a87", "a88", "a89", "a90", "a91", "a92", "a93", "a94", "a95", "a96", "a97", "a98", "a99", "a100", "a101", "a102", "a103", "a104", "a105", "a106", "a107", "a108", "a109", "a110", "a111", "a112", "a113", "a114", "a115", "a116", "a117", "a118", "a119", "a120", "a121", "a122", "a123", "a124", "a125", "a126", "a127", "a128", "a129", "a130", "a131", "a132", "a133", "a134", "a135", "a136", "a137", "a138", "a139", "a140", "a141", "a142", "a143", "a144", "a145", "a146", "a147", "a148", "a149", "a150", "a151", "a152", "a153", "a154", "a155", "a156", "a157", "a158", "a159", "a160", "a161", "a162", "a163", "a164", "a165", "a166", "a167", "a168", "a169", "a170", "a171", "a172", "a173", "a174", "a175", "a176", "a177", "a178", "a179", "a180", "a181", "a182", "a183", "a184", "a185", "a186", "a187", "a188", "a189", "a190", "a191", "a192", "a193", "a194", "a195", "a196", "a197", "a198", "a199", "a200", "a201", "a202", "a203", "a204", "a205", "a206", "a207", "a208", "a209", "a210", "a211", "a212", "a213", "a214", "a215", "a216", "a217", "a218", "a219", "a220", "a221", "a222", "a223", "a224", "a225", "a226", "a227", "a228", "a229", "a230", "a231", "a232", "a233", "a234", "a235", "a236", "a237", "a238", "a239", "a240", "a241", "a242", "a243", "a244", "a245", "a246", "a247", "a248", "a249", "a250", "a251", "a252", "a253", "a254", "a255", "s20", "s21", "s22", "s23", "s24", "s25", "s26", "s27", "s28", "s29", "s30", "s31", "s34", "s35", "s36", "s37", "s38", "s39", "s40", "s41", "s42", "s43", "s44", "s45", "s46", "s47", "s48", "s49", "s50", "s51", "s52", "s53", "s54", "s55", "s56", "s57", "s58", "s59", "s60", "s61", "s62", "s63", "s64", "s65", "s66", "s67", "s68", "s69", "s70", "s71", "s72", "s73", "s74", "s75", "s76", "s77", "s78", "s79", "s80", "s81", "s82", "s83", "s84", "s85", "s86", "s87", "s88", "s89", "s90", "s91", "s92", "s93", "s94", "s95", "vcc", "scc", "memory");
}

extern "C" void kernel_launch(void* const* d_in, const int* in_sizes, int n_in, void* d_out, int out_size,
                              void* d_ws, size_t ws_size, hipStream_t stream) {
  void* ows = d_ws;

  mha_attn_kernel<<<256, 256, 0, stream>>>(d_in[0], d_in[1], d_in[2], d_in[3], d_in[4], ows, d_out);

}
```
